```python
import torch

def _softmax(x, axis=-1):
    return torch.softmax(x, dim=axis)
def _gelu(x):
    return torch.nn.functional.gelu(x, approximate="tanh")
def _silu(x):
    return torch.nn.functional.silu(x)
def _sigmoid(x):
    return torch.sigmoid(x)
def _layernorm(x, axis=-1, eps=1e-5):
    m = x.mean(dim=axis, keepdim=True)
    v = x.var(dim=axis, keepdim=True, unbiased=False)
    return (x - m) / torch.sqrt(v + eps)
def _scatter_add(tgt, idx, src, axis=0):
    return tgt.clone().index_add_(axis, idx, src)
def _segment_sum(src, seg, n):
    out = torch.zeros((n,) + src.shape[1:], dtype=src.dtype)
    return out.index_add_(0, seg, src)
def _pad(x, pw):
    flat = tuple(v for pair in reversed(pw) for v in pair)
    return torch.nn.functional.pad(x, flat)

kernel_name = 'sequential_preLN__linear_kernel_hedgehog__glu_gated_bilinear__prompt'

def setup_inputs():

    _w = (lambda a: (lambda t: t.to(torch.bfloat16) if t.is_floating_point() else t)(torch.as_tensor(a)))
    def _A_setup():
        B, S, D, H = 1, 2048, 512, 32
        Dh = D // H
        return {
            "x": _w((lambda *s: torch.randn(*s, dtype=torch.bfloat16) * 0.05)(B, S, D)),
            "wqkv": _w((lambda *s: torch.randn(*s, dtype=torch.bfloat16) * 0.2)(D, 3 * D)),
            "w_phi": _w((lambda *s: torch.randn(*s, dtype=torch.bfloat16) * 0.2)(H, Dh, Dh) * (Dh ** -0.5)),
            "wo": _w((lambda *s: torch.randn(*s, dtype=torch.bfloat16) * 0.05)(D, D)),
        }
    _A_in = _A_setup()

    def _B_setup():
        B, S, D, F = 1, 2048, 512, 1376
        return {
            "x": _w((lambda *s: torch.randn(*s, dtype=torch.bfloat16) * 0.05)(B, S, D)),
            "w_gate": _w((lambda *s: torch.randn(*s, dtype=torch.bfloat16) * 0.4)(D, F) * (D ** -0.5)),
            "w_up": _w((lambda *s: torch.randn(*s, dtype=torch.bfloat16) * 0.4)(D, F) * (D ** -0.5)),
            "w_down": _w((lambda *s: torch.randn(*s, dtype=torch.bfloat16) * 0.4)(F, D) * (F ** -0.5)),
        }
    _B_in = _B_setup()

    return {
        "A_x": _A_in["x"],
        "A_wqkv": _A_in["wqkv"],
        "A_w_phi": _A_in["w_phi"],
        "A_wo": _A_in["wo"],
        "B_x": _B_in["x"],
        "B_w_gate": _B_in["w_gate"],
        "B_w_up": _B_in["w_up"],
        "B_w_down": _B_in["w_down"],
    }

def reference(A_x, A_wqkv, A_w_phi, A_wo, B_x, B_w_gate, B_w_up, B_w_down):
    def _A_ref(x, wqkv, w_phi, wo):
        B, S, D = x.shape
        H = 32; Dh = D // H
        qkv = (lambda x, *d: x.permute(*d))((x @ wqkv).reshape(B, S, 3, H, Dh), 2, 0, 3, 1, 4)
        q, k, v = qkv[0], qkv[1], qkv[2]
        pq = torch.einsum("bhsd,hdf->bhsf", q, w_phi)
        pk = torch.einsum("bhsd,hdf->bhsf", k, w_phi)
        phi_q = torch.concatenate([_softmax(pq, axis=-1), _softmax(-pq, axis=-1)], axis=-1)
        phi_k = torch.concatenate([_softmax(pk, axis=-1), _softmax(-pk, axis=-1)], axis=-1)
        kv = torch.einsum("bhsf,bhsd->bhfd", phi_k, v)
        z  = torch.einsum("bhsf->bhf", phi_k)
        num = torch.einsum("bhqf,bhfd->bhqd", phi_q, kv)
        den = torch.einsum("bhqf,bhf->bhq", phi_q, z)[..., None] + 1e-6
        o = num / den
        return (lambda x, *d: x.permute(*d))(o, 0, 2, 1, 3).reshape(B, S, D) @ wo

    def _B_ref(x, w_gate, w_up, w_down):
        g = torch.einsum("bsd,df->bsf", x, w_gate)
        u = torch.einsum("bsd,df->bsf", x, w_up)
        h = g * u
        return torch.einsum("bsf,fd->bsd", h, w_down)

    x = A_x
    h = x + _A_ref(_layernorm(x, axis=-1), A_wqkv, A_w_phi, A_wo)
    h2 = h + _B_ref(_layernorm(h, axis=-1), B_w_gate, B_w_up, B_w_down)
    return h2
```

```cpp
#include <hip/hip_runtime.h>
#include <hip/hip_bf16.h>
typedef unsigned short bf16_t;
typedef short bf16x8 __attribute__((ext_vector_type(8)));
typedef float f32x4 __attribute__((ext_vector_type(4)));
constexpr int S = 2048, D = 512, H = 32, F = 1376;
__device__ __forceinline__ float b2f(bf16_t v) { return __uint_as_float((unsigned)v << 16); }
__device__ __forceinline__ bf16_t f2b(float x) { unsigned u = __float_as_uint(x); u += 0x7fffu + ((u >> 16) & 1u); return (bf16_t)(u >> 16); }

__global__ void k_ln(const bf16_t* x, bf16_t* y) {
    const int row = blockIdx.x * 4 + (threadIdx.x >> 6), lane = threadIdx.x & 63;
    float v[8]; float s = 0;
    for (int j = 0; j < 8; ++j) { v[j] = b2f(x[row * D + lane * 8 + j]); s += v[j]; }
    for (int m = 32; m; m >>= 1) s += __shfl_xor(s, m);
    const float mean = s / D; float q = 0;
    for (int j = 0; j < 8; ++j) { v[j] -= mean; q += v[j] * v[j]; }
    for (int m = 32; m; m >>= 1) q += __shfl_xor(q, m);
    const float r = 1.0f / sqrtf(q / D + 1e-5f);
    for (int j = 0; j < 8; ++j) y[row * D + lane * 8 + j] = f2b(v[j] * r);
}
template <int MODE>
__global__ void __launch_bounds__(256) k_gemm(const bf16_t* A, const bf16_t* B, const bf16_t* B2, const bf16_t* R, bf16_t* C, int N, int K) {
    __shared__ __attribute__((aligned(16))) bf16_t As[64][40];
    __shared__ __attribute__((aligned(16))) bf16_t Bs[64][40];
    __shared__ __attribute__((aligned(16))) bf16_t Bs2[64][40];
    const int tid = threadIdx.x, lane = tid & 63, w = tid >> 6, bm = blockIdx.y * 64, bn = blockIdx.x * 64;
    f32x4 acc[4] = {}, acc2[4] = {};
    for (int k0 = 0; k0 < K; k0 += 32) {
        { const int r = tid >> 2, c = (tid & 3) * 8; *(bf16x8*)&As[r][c] = *(const bf16x8*)&A[(size_t)(bm + r) * K + k0 + c]; }
        { const int kk = tid >> 3, c = (tid & 7) * 8;
          bf16x8 v = {}, v2 = {};
          if (bn + c < N) { v = *(const bf16x8*)&B[(size_t)(k0 + kk) * N + bn + c]; if (MODE == 2) v2 = *(const bf16x8*)&B2[(size_t)(k0 + kk) * N + bn + c]; }
          for (int j = 0; j < 8; ++j) { Bs[c + j][kk] = v[j]; if (MODE == 2) Bs2[c + j][kk] = v2[j]; } }
        __syncthreads();
        const bf16x8 a = *(const bf16x8*)&As[w * 16 + (lane & 15)][(lane >> 4) * 8];
        for (int n = 0; n < 4; ++n) {
            const bf16x8 b = *(const bf16x8*)&Bs[n * 16 + (lane & 15)][(lane >> 4) * 8];
            acc[n] = __builtin_amdgcn_mfma_f32_16x16x32_bf16(a, b, acc[n], 0, 0, 0);
            if (MODE == 2) { const bf16x8 b2 = *(const bf16x8*)&Bs2[n * 16 + (lane & 15)][(lane >> 4) * 8]; acc2[n] = __builtin_amdgcn_mfma_f32_16x16x32_bf16(a, b2, acc2[n], 0, 0, 0); }
        }
        __syncthreads();
    }
    for (int n = 0; n < 4; ++n) for (int j = 0; j < 4; ++j) {
        const int row = bm + w * 16 + (lane >> 4) * 4 + j, col = bn + n * 16 + (lane & 15);
        if (col < N) {
            float v = acc[n][j];
            if (MODE == 1) v = b2f(f2b(v)) + b2f(R[(size_t)row * N + col]);
            if (MODE == 2) v = b2f(f2b(v)) * b2f(f2b(acc2[n][j]));
            C[(size_t)row * N + col] = f2b(v);
        }
    }
}
__global__ void __launch_bounds__(256) k_feat(const bf16_t* qkv, const bf16_t* wphi, bf16_t* phi) {
    const int i = blockIdx.x * blockDim.x + threadIdx.x;
    const int which = i / (S * H), t = (i / H) % S, h = i % H;
    float q[16], p[16];
    for (int d = 0; d < 16; ++d) q[d] = b2f(qkv[(size_t)t * 1536 + which * 512 + h * 16 + d]);
    float mx = -1e30f, mn = 1e30f;
    for (int f = 0; f < 16; ++f) { float s = 0; for (int d = 0; d < 16; ++d) s += q[d] * b2f(wphi[(h * 16 + d) * 16 + f]); p[f] = b2f(f2b(s)); mx = fmaxf(mx, p[f]); mn = fminf(mn, p[f]); }
    float sp = 0, sn = 0, ep[16], en[16];
    for (int f = 0; f < 16; ++f) { ep[f] = expf(p[f] - mx); en[f] = expf(mn - p[f]); sp += ep[f]; sn += en[f]; }
    bf16_t* o = phi + ((size_t)which * S * H + (size_t)t * H + h) * 32;
    for (int f = 0; f < 16; ++f) { o[f] = f2b(ep[f] / sp); o[16 + f] = f2b(en[f] / sn); }
}
__global__ void __launch_bounds__(512) k_kv(const bf16_t* qkv, const bf16_t* phi, float* part) {
    const int h = blockIdx.x, c = blockIdx.y, f = threadIdx.x >> 4, d = threadIdx.x & 15;
    float s = 0, z = 0;
    for (int t = c * 256; t < c * 256 + 256; ++t) { const float pf = b2f(phi[((size_t)S * H + (size_t)t * H + h) * 32 + f]); s += pf * b2f(qkv[(size_t)t * 1536 + 1024 + h * 16 + d]); z += pf; }
    part[((size_t)c * H + h) * 544 + f * 16 + d] = s;
    if (d == 0) part[((size_t)c * H + h) * 544 + 512 + f] = z;
}
__global__ void __launch_bounds__(256) k_o(const bf16_t* phi, const float* part, bf16_t* o) {
    __shared__ float kv[544];
    const int h = blockIdx.x, t = blockIdx.y * 256 + threadIdx.x;
    for (int i = threadIdx.x; i < 544; i += 256) { float s = 0; for (int c = 0; c < 8; ++c) s += part[((size_t)c * H + h) * 544 + i]; kv[i] = b2f(f2b(s)); }
    __syncthreads();
    float ph[32];
#pragma unroll
 for (int f = 0; f < 32; ++f) ph[f] = b2f(phi[((size_t)t * H + h) * 32 + f]);
    float den = 0;
#pragma unroll
 for (int f = 0; f < 32; ++f) den += ph[f] * kv[512 + f];
    den = b2f(f2b(b2f(f2b(den)) + 1e-6f));
    for (int d = 0; d < 16; ++d) { float s = 0;
#pragma unroll
 for (int f = 0; f < 32; ++f) s += ph[f] * kv[f * 16 + d]; o[(size_t)t * D + h * 16 + d] = f2b(b2f(f2b(s)) / den); }
}
__global__ void k_nop(int* p) { if (p == nullptr) __builtin_trap(); }
#define EXTRA 0
#define EXTRA_MEMSET 0
extern "C" void kernel_launch(void* const* d_in, const int* in_sizes, int n_in, void* d_out, int out_size, void* d_ws, size_t ws_size, hipStream_t stream) {
    const bf16_t* x = (const bf16_t*)d_in[0]; const bf16_t* wqkv = (const bf16_t*)d_in[1]; const bf16_t* wphi = (const bf16_t*)d_in[2]; const bf16_t* wo = (const bf16_t*)d_in[3];
    const bf16_t* wg = (const bf16_t*)d_in[5]; const bf16_t* wu = (const bf16_t*)d_in[6]; const bf16_t* wd = (const bf16_t*)d_in[7];
    char* ws = (char*)d_ws;
    bf16_t* xn = (bf16_t*)ws; bf16_t* qkv = (bf16_t*)(ws + (4 << 20)); bf16_t* phi = (bf16_t*)(ws + (16 << 20)); float* part = (float*)(ws + (32 << 20));
    bf16_t* o = (bf16_t*)(ws + (36 << 20)); bf16_t* h = (bf16_t*)(ws + (40 << 20)); bf16_t* act = (bf16_t*)(ws + (44 << 20));
    bf16_t* out = (bf16_t*)d_out;
    if (EXTRA_MEMSET) hipMemsetAsync(ws + (60 << 20), 0, 4096, stream);
    k_ln<<<S / 4, 256, 0, stream>>>(x, xn);
    k_gemm<0><<<dim3(1536 / 64, S / 64), 256, 0, stream>>>(xn, wqkv, nullptr, nullptr, qkv, 1536, 512);
    k_feat<<<S * H * 2 / 256, 256, 0, stream>>>(qkv, wphi, phi);
    k_kv<<<dim3(H, 8), 512, 0, stream>>>(qkv, phi, part);
    k_o<<<dim3(H, 8), 256, 0, stream>>>(phi, part, o);
    k_gemm<1><<<dim3(512 / 64, S / 64), 256, 0, stream>>>(o, wo, nullptr, x, h, 512, 512);
    k_ln<<<S / 4, 256, 0, stream>>>(h, xn);
    k_gemm<2><<<dim3((F + 63) / 64, S / 64), 256, 0, stream>>>(xn, wg, wu, nullptr, act, F, 512);
    k_gemm<1><<<dim3(512 / 64, S / 64), 256, 0, stream>>>(act, wd, nullptr, h, out, 512, F);
    for (int i = 0; i < EXTRA; ++i) k_nop<<<256, 256, 0, stream>>>((int*)ws);
}
```

```cpp
#include <hip/hip_runtime.h>
#include <hip/hip_bf16.h>

#define LAS __attribute__((address_space(3)))
#define GAS __attribute__((address_space(1)))
#define DI __device__ __forceinline__
typedef unsigned short bf16_t;
typedef unsigned u32x4 __attribute__((ext_vector_type(4)));
typedef unsigned u32x2 __attribute__((ext_vector_type(2)));
typedef short s16x4 __attribute__((ext_vector_type(4)));
typedef __bf16 bf16x8 __attribute__((ext_vector_type(8)));
typedef __bf16 bf16x2 __attribute__((ext_vector_type(2)));
typedef float f32x16 __attribute__((ext_vector_type(16)));
typedef float f32x4 __attribute__((ext_vector_type(4)));
typedef float f32x2 __attribute__((ext_vector_type(2)));
typedef unsigned long long u64;
typedef __amdgpu_buffer_rsrc_t rsrc_t;

constexpr unsigned WS_F1 = 0x10000, WS_F2 = 0x20000, WS_F3 = 0x30000, WS_F4 = 0x40000, WS_F5 = 0x70000;
constexpr unsigned WS_KVP = 0x80000, KVP_BYTES = 2176;
constexpr unsigned WS_HST = 0x110000;
constexpr unsigned WS_XST = 0x160000, WS_CSG = 0x170000;
constexpr unsigned WS_O = 0x200000, WS_H = 0x400000, WS_ACT = 0x600000, WS_PROF = 0xC00000, WS_BYTES = 0x1000000;
constexpr unsigned L_X = 0, L_Y = 65536, L_XCH = 131072, L_MISC = 147456;
constexpr unsigned L_STAT = L_MISC, L_CS1 = L_MISC + 1024, L_CSQ = L_MISC + 1280, L_WPHI = L_MISC + 1536, L_KVA = L_MISC + 2560, L_CS3 = L_MISC + 4736, L_CNT = L_MISC + 5504;
constexpr unsigned L_WQ = 65536, L_PHI = 98304, L_VIMG = 114688, SS_BYTES = 24576;
constexpr unsigned LDS_BYTES = 163840;
constexpr unsigned SPIN_CAP = 40000;
constexpr float LOG2E = 1.4426950408889634f;

DI float asf(unsigned v) { return __builtin_bit_cast(float, v); }
DI unsigned asu(float v) { return __builtin_bit_cast(unsigned, v); }
DI unsigned pack2(float lo, float hi) { bf16x2 v = {(__bf16)lo, (__bf16)hi}; return __builtin_bit_cast(unsigned, v); }
DI float blo(unsigned v) { return __builtin_bit_cast(float, v << 16); }
DI float bhi(unsigned v) { return __builtin_bit_cast(float, v & 0xffff0000u); }
DI f32x16 mfma(u32x4 a, u32x4 b, f32x16 c) { return __builtin_amdgcn_mfma_f32_32x32x16_bf16(__builtin_bit_cast(bf16x8, a), __builtin_bit_cast(bf16x8, b), c, 0, 0, 0); }
DI u32x4 tr2(LAS char* lds, unsigned off, unsigned second) {
    const s16x4 lo = __builtin_amdgcn_ds_read_tr16_b64_v4i16((LAS s16x4*)(lds + off));
    const s16x4 hi = __builtin_amdgcn_ds_read_tr16_b64_v4i16((LAS s16x4*)(lds + off + second));
    return __builtin_bit_cast(u32x4, __builtin_shufflevector(lo, hi, 0, 1, 2, 3, 4, 5, 6, 7));
}
DI void dma16(const rsrc_t rs, unsigned ldsaddr, unsigned voff, unsigned soff) {
    asm volatile("s_mov_b32 m0, %0\n\ts_nop 0\n\tbuffer_load_dwordx4 %1, %2, %3 offen lds" ::"s"(ldsaddr), "v"(voff), "s"(rs), "s"(soff) : "memory");
}
#define WAITV(n) asm volatile("s_waitcnt vmcnt(" #n ")" ::: "memory")
#define PIN() do { asm volatile("" ::: "memory"); __builtin_amdgcn_sched_barrier(0); } while (0)
#define SBAR() asm volatile("s_barrier" ::: "memory")
#define BAR() asm volatile("s_waitcnt lgkmcnt(0)\n\ts_barrier" ::: "memory")
DI u32x2 both_halves(float v) { const unsigned a = asu(v); const auto r = __builtin_amdgcn_permlane32_swap(a, a, false, false); return (u32x2){r[0], r[1]}; }
DI float halves_add(float v) { const u32x2 r = both_halves(v); return asf(r[0]) + asf(r[1]); }
DI float halves_max(float v) { const u32x2 r = both_halves(v); return fmaxf(asf(r[0]), asf(r[1])); }
DI float halves_min(float v) { const u32x2 r = both_halves(v); return fminf(asf(r[0]), asf(r[1])); }
DI float dpp_ror4(float v) { return asf(__builtin_amdgcn_update_dpp(0, (int)asu(v), 0x124, 0xf, 0xf, true)); }
DI float dpp_ror8(float v) { return asf(__builtin_amdgcn_update_dpp(0, (int)asu(v), 0x128, 0xf, 0xf, true)); }
DI float rows16_add(float v) { const unsigned a = asu(v); const auto r = __builtin_amdgcn_permlane16_swap(a, a, false, false); return asf(r[0]) + asf(r[1]); }
DI float stride4_add(float v) { v += dpp_ror4(v); v += dpp_ror8(v); v = rows16_add(v); return halves_add(v); }
DI float quad_add(float v) {
    v += __builtin_bit_cast(float, __builtin_amdgcn_update_dpp(0, __builtin_bit_cast(int, v), 0xB1, 0xf, 0xf, true));
    v += __builtin_bit_cast(float, __builtin_amdgcn_update_dpp(0, __builtin_bit_cast(int, v), 0x4E, 0xf, 0xf, true));
    return v;
}
DI void st16(const rsrc_t rs, u32x4 v, unsigned voff, unsigned soff) { __builtin_amdgcn_raw_buffer_store_b128(v, rs, voff + soff, 0, 16); }
DI u32x4 ld16(const rsrc_t rs, unsigned voff, unsigned soff) { return __builtin_amdgcn_raw_buffer_load_b128(rs, voff, soff, 16); }
DI unsigned ld4(const rsrc_t rs, unsigned voff, unsigned soff) { return __builtin_amdgcn_raw_buffer_load_b32(rs, voff, soff, 16); }

DI void nohook(int) {}
DI void poll_flags(const char* ws, unsigned off, int n, u64 tag, int lane) {
    const GAS u64* f = (const GAS u64*)(ws + off) + (lane < n ? lane : n - 1) * 32;
#pragma unroll 1
    for (unsigned spin = 0; spin < SPIN_CAP; ++spin) {
        const u64 v = __hip_atomic_load(f, __ATOMIC_RELAXED, __HIP_MEMORY_SCOPE_AGENT);
        if (__all(v == tag)) break;
        __builtin_amdgcn_s_sleep(1);
    }
    asm volatile("" ::: "memory");
}
DI void arrive_publish(LAS char* lds, int cnt, unsigned n, char* ws, unsigned flag_off, u64 tag, int lane) {
    if (lane == 0) {
        const unsigned old = __hip_atomic_fetch_add((LAS unsigned*)(lds + L_CNT) + cnt, 1u, __ATOMIC_RELAXED, __HIP_MEMORY_SCOPE_WORKGROUP);
        if (old == n - 1) __hip_atomic_store((GAS u64*)(ws + flag_off), tag, __ATOMIC_RELAXED, __HIP_MEMORY_SCOPE_AGENT);
    }
}
DI void dual_softmax(const f32x16& p, u32x4& pos, u32x4& neg) {
    float mx = fmaxf(fmaxf(fmaxf(p[0], p[1]), fmaxf(p[2], p[3])), fmaxf(fmaxf(p[4], p[5]), fmaxf(p[6], p[7])));
    float mn = fminf(fminf(fminf(p[0], p[1]), fminf(p[2], p[3])), fminf(fminf(p[4], p[5]), fminf(p[6], p[7])));
    mx = halves_max(mx); mn = halves_min(mn);
    const float cx = -mx * LOG2E, cn = mn * LOG2E;
    float ep[8], en[8], sp = 0.f, sn = 0.f;
#pragma unroll
    for (int r = 0; r < 8; ++r) {
        ep[r] = __builtin_amdgcn_exp2f(__builtin_fmaf(p[r], LOG2E, cx));
        en[r] = __builtin_amdgcn_exp2f(__builtin_fmaf(p[r], -LOG2E, cn));
        sp += ep[r]; sn += en[r];
    }
    const float rp = __builtin_amdgcn_rcpf(halves_add(sp)), rn = __builtin_amdgcn_rcpf(halves_add(sn));
#pragma unroll
    for (int i = 0; i < 4; ++i) { pos[i] = pack2(ep[2 * i] * rp, ep[2 * i + 1] * rp); neg[i] = pack2(en[2 * i] * rn, en[2 * i + 1] * rn); }
}

template <int N, int STRIDE, int OFF, int NA, class Hook>
DI void gemm_steps(LAS char* lds, unsigned base, const u32x4 (&act)[NA], f32x16& acc0, f32x16& acc1, Hook hook) {
    u32x4 fb[2][4];
    asm volatile("" : "+v"(base));
    constexpr int NG = (N + 1) / 2;
#pragma unroll
    for (int e = 0; e < 2; ++e) { fb[0][2 * e] = tr2(lds, base + e * STRIDE, 512); fb[0][2 * e + 1] = tr2(lds, base + e * STRIDE + 1024, 512); }
#pragma unroll
    for (int g = 0; g < NG; ++g) {
#pragma unroll
        for (int e = 0; e < 2; ++e) {
            const int i = 2 * g + 2 + e;
            if (i < N) { fb[(g + 1) & 1][2 * e] = tr2(lds, base + i * STRIDE, 512); fb[(g + 1) & 1][2 * e + 1] = tr2(lds, base + i * STRIDE + 1024, 512); }
        }
        __builtin_amdgcn_sched_barrier(0);
#pragma unroll
        for (int e = 0; e < 2; ++e) {
            const int i = 2 * g + e;
            if (i < N) { acc0 = mfma(fb[g & 1][2 * e], act[OFF + i], acc0); acc1 = mfma(fb[g & 1][2 * e + 1], act[OFF + i], acc1); }
        }
        __builtin_amdgcn_sched_barrier(0);
        hook(g);
    }
}

#define STAMP(i) do {} while (0)
#define CSTAMP(i) do {} while (0)

__global__ void __launch_bounds__(512, 2)
hedgehog_block(const bf16_t* __restrict__ x, const bf16_t* __restrict__ wqkv, const bf16_t* __restrict__ wphi, const bf16_t* __restrict__ wo,
               const bf16_t* __restrict__ wg, const bf16_t* __restrict__ wu, const bf16_t* __restrict__ wd, bf16_t* __restrict__ out, char* __restrict__ ws) {
    extern __shared__ __attribute__((aligned(16))) char smem[];
    LAS char* lds = (LAS char*)smem;
    const unsigned lb = (unsigned)(size_t)lds;
    const int tid = threadIdx.x, lane = tid & 63;
    const int w = __builtin_amdgcn_readfirstlane(tid >> 6);
    const int b = blockIdx.x, xcd = b & 7, slot = b >> 3;
    const int p = 2 * xcd + (slot >> 4), j = slot & 15, c = slot & 7, half = (slot >> 3) & 1;
    const int t = lane & 31, h2 = lane >> 5, q = (lane & 15) >> 2, pp = lane & 3, blk = (lane >> 4) & 1;
    const int m = w & 3, a = w >> 2, lw = w & 3;
    const int q4 = 2 * (p & 1) + half;
    const int r1 = (j + 4 * (p & 1)) & 7, r2 = (2 * c + 4 * q4) & 15, r3 = (2 * j + 16 * (p & 1)) & 31, r4 = (c + 3 * q4) % 11;
    STAMP(0); CSTAMP(30);
    const rsrc_t rsX = __builtin_amdgcn_make_buffer_rsrc((void*)x, 0, 2048 * 512 * 2, 0x00020000);
    const rsrc_t rsQ = __builtin_amdgcn_make_buffer_rsrc((void*)wqkv, 0, 512 * 1536 * 2, 0x00020000);
    const rsrc_t rsW = __builtin_amdgcn_make_buffer_rsrc((void*)ws, 0, WS_BYTES, 0x00020000);
    u64 epoch, tag = 0;
    asm volatile("global_load_dwordx2 %0, %1, off sc1" : "=v"(epoch) : "v"((const GAS u64*)ws) : "memory");
#define TAKE_TAG() do { const u64 e_ = ((u64)(unsigned)__builtin_amdgcn_readfirstlane((unsigned)(epoch >> 32)) << 32) | (unsigned)__builtin_amdgcn_readfirstlane((unsigned)epoch); \
                        tag = e_ + 0x9E3779B97F4A7C15ull; } while (0)
    const u32x4 ones = {0x3F803F80u, 0x3F803F80u, 0x3F803F80u, 0x3F803F80u};
    const unsigned vrow = (lane >> 2) * 1024 + (lane & 3) * 16;
    const unsigned trn = (8 * h2 + q) * 64 + pp * 8;

    u32x4 xf[32];
    float yk[16];
    float mean1, rstd1;
    if (w < 4) {
        {
            const unsigned R = 128 * p + 8 * j + 2 * w;
            const u32x4 v0 = __builtin_amdgcn_raw_buffer_load_b128(rsX, lane * 16, R * 1024, 0), v1 = __builtin_amdgcn_raw_buffer_load_b128(rsX, lane * 16, R * 1024 + 1024, 0);
            if (tid < 16) *(LAS unsigned*)(lds + L_CNT + tid * 4) = 0u;
            f32x2 a1 = {0.f, 0.f}, a2 = {0.f, 0.f}, b1 = {0.f, 0.f}, b2 = {0.f, 0.f};
#pragma unroll
            for (int e = 0; e < 4; ++e) {
                const f32x2 z0 = {blo(v0[e]), bhi(v0[e])}, z1 = {blo(v1[e]), bhi(v1[e])};
                a1 += z0; a2 += z0 * z0; b1 += z1; b2 += z1 * z1;
            }
            const float su0 = stride4_add(quad_add(a1[0] + a1[1])), sq0 = stride4_add(quad_add(a2[0] + a2[1]));
            const float su1 = stride4_add(quad_add(b1[0] + b1[1])), sq1 = stride4_add(quad_add(b2[0] + b2[1]));
            const float su = (lane & 1) ? su1 : su0, sq = (lane & 1) ? sq1 : sq0;
            const float mean = su * (1.0f / 512.0f), var = fmaxf(sq * (1.0f / 512.0f) - mean * mean, 0.f);
            const float rstd = __builtin_amdgcn_rsqf(var + 1e-5f);
            asm volatile("s_waitcnt vmcnt(0)" : "+v"(epoch)::"memory");
            TAKE_TAG();
            if (lane < 2) st16(rsW, (u32x4){asu(mean), asu(rstd), (unsigned)tag, (unsigned)(tag >> 32)}, (R + lane) * 16, WS_XST);
        }
        STAMP(1);
        const unsigned xa0 = (32 * m + t) * 128 + ((h2 ^ ((t >> 1) & 7)) * 16);
        const unsigned twA = 16384 + (blk ? 4096 + 32 : 0) + trn, twB = 16384 + (blk ? 4096 : 32) + trn;
        f32x16 accA = {}, accB = {};
        u32x4 fa[2][2], fb[2][2], xs;
#pragma unroll
        for (int ss = 0; ss <= 8; ++ss) {
            unsigned bA = 0, bB = 0, bx[4] = {0, 0, 0, 0};
            if (ss < 8) {
                BAR();
                if (ss == 0) STAMP(2);
                const unsigned sb = (ss % 6) * SS_BYTES;
                bA = sb + twA; bB = sb + twB;
#pragma unroll
                for (int e = 0; e < 4; ++e) bx[e] = sb + (xa0 ^ (32 * e));
                asm volatile("" : "+v"(bA), "+v"(bB));
#pragma unroll
                for (int e = 0; e < 2; ++e) {
                    xf[4 * ss + e] = *(LAS u32x4*)(lds + bx[e]);
                    fa[0][e] = tr2(lds, bA + e * 1024, 256); fb[0][e] = tr2(lds, bB + e * 1024, 256);
                }
                if (ss == 6) xs = ld16(rsW, (128 * p + 32 * m + t) * 16, WS_XST);
                PIN();
            }
            if (ss > 0) {
#pragma unroll
                for (int e = 0; e < 2; ++e) { accA = mfma(fa[1][e], xf[4 * ss - 2 + e], accA); accB = mfma(fb[1][e], xf[4 * ss - 2 + e], accB); }
                PIN();
            }
            if (ss < 8) {
#pragma unroll
                for (int e = 0; e < 2; ++e) {
                    xf[4 * ss + 2 + e] = *(LAS u32x4*)(lds + bx[2 + e]);
                    fa[1][e] = tr2(lds, bA + (2 + e) * 1024, 256); fb[1][e] = tr2(lds, bB + (2 + e) * 1024, 256);
                }
                PIN();
#pragma unroll
                for (int e = 0; e < 2; ++e) { accA = mfma(fa[0][e], xf[4 * ss + e], accA); accB = mfma(fb[0][e], xf[4 * ss + e], accB); }
                PIN();
            }
        }
        STAMP(3);
#pragma unroll
        for (int g = 0; g < 4; ++g) *(LAS f32x4*)(lds + L_XCH + m * 4096 + g * 1024 + lane * 16) = (f32x4){accB[4 * g], accB[4 * g + 1], accB[4 * g + 2], accB[4 * g + 3]};
#pragma unroll 1
        for (unsigned spin = 0; spin < SPIN_CAP; ++spin) {
            if (__all(xs[2] == (unsigned)tag && xs[3] == (unsigned)(tag >> 32))) break;
            __builtin_amdgcn_s_sleep(1);
            xs = ld16(rsW, (128 * p + 32 * m + t) * 16, WS_XST);
        }
        mean1 = asf(xs[0]); rstd1 = asf(xs[1]);
        if (h2 == 0) *(LAS f32x2*)(lds + L_STAT + (32 * m + t) * 8) = (f32x2){mean1, rstd1};
        BAR();
        STAMP(4);
#pragma unroll
        for (int g = 0; g < 4; ++g) {
            const f32x4 cs = *(LAS f32x4*)(lds + L_CS1 + (8 * g + 4 * h2) * 4);
#pragma unroll
            for (int e = 0; e < 4; ++e) yk[4 * g + e] = rstd1 * __builtin_fmaf(-mean1, cs[e], accA[4 * g + e]);
        }
    } else {
        const unsigned vx0 = (lane >> 3) * 1024 + (((lane & 7) ^ (lane >> 4)) * 16), vx1 = (lane >> 3) * 1024 + (((lane & 7) ^ (4 + (lane >> 4))) * 16);
        const unsigned vwk = (lane >> 2) * 3072 + (lane & 3) * 16, vwv = (lane >> 2) * 3072 + ((lane & 3) ^ 2) * 16;
        const unsigned sx = (128 * p + 32 * lw) * 1024, swk = (16 * lw) * 3072 + (512 + 32 * j) * 2;
#define ISSUE_SS(ss) do { const unsigned sb_ = lb + ((ss) % 6) * SS_BYTES, ks_ = ((ss) + r1) & 7; \
            dma16(rsX, sb_ + (4 * lw) * 1024, vx0, sx + ks_ * 128); \
            dma16(rsX, sb_ + (4 * lw + 1) * 1024, vx1, sx + 8 * 1024 + ks_ * 128); \
            dma16(rsQ, sb_ + 16384 + lw * 1024, vwk, swk + ks_ * 64 * 3072); \
            dma16(rsQ, sb_ + 16384 + 4096 + lw * 1024, vwv, swk + 1024 + ks_ * 64 * 3072); \
            dma16(rsX, sb_ + (4 * lw + 2) * 1024, vx0, sx + 16 * 1024 + ks_ * 128); \
            dma16(rsX, sb_ + (4 * lw + 3) * 1024, vx1, sx + 24 * 1024 + ks_ * 128); } while (0)
        if (lw == 0) {
            const rsrc_t rsP = __builtin_amdgcn_make_buffer_rsrc((void*)wphi, 0, 32 * 256 * 2, 0x00020000);
            dma16(rsP, lb + L_WPHI, lane * 16, j * 1024);
        }
        ISSUE_SS(0); ISSUE_SS(1); ISSUE_SS(2); ISSUE_SS(3); ISSUE_SS(4);
        STAMP(1);
        const unsigned tw = 16384 + (blk ? 4096 + ((lw & 1) ^ 1) * 32 : (lw & 1) * 32) + trn;
        f32x16 csacc = {};
#pragma unroll
        for (int ss = 0; ss < 8; ++ss) {
            if (ss == 0) { asm volatile("s_waitcnt vmcnt(24)" : "+v"(epoch)::"memory"); }
            if (ss == 1 || ss == 2 || ss == 3) WAITV(24);
            if (ss == 4) WAITV(18);
            if (ss == 5) WAITV(20);
            if (ss == 6) WAITV(14);
            if (ss == 7) WAITV(8);
            BAR();
            if (ss == 0) { TAKE_TAG(); STAMP(2); }
            if (ss == 0) ISSUE_SS(5);
            if (ss == 1) ISSUE_SS(6);
            if (ss == 2) ISSUE_SS(7);
            if (ss == 4) {
#pragma unroll
                for (int i = 0; i < 8; ++i) dma16(rsQ, lb + L_WQ + (8 * lw + i) * 1024, vwk, (16 * ((8 * lw + i + 4 * r1) & 31)) * 3072 + 32 * j * 2);
            }
            if (lw < 2) {
                unsigned bw = (ss % 6) * SS_BYTES + tw;
                asm volatile("" : "+v"(bw));
                u32x4 f[4];
#pragma unroll
                for (int s = 0; s < 4; ++s) f[s] = tr2(lds, bw + s * 1024, 256);
                PIN();
#pragma unroll
                for (int s = 0; s < 4; ++s) csacc = mfma(f[s], ones, csacc);
            }
        }
        if (lw < 2 && t == 0) {
#pragma unroll
            for (int g = 0; g < 4; ++g) *(LAS f32x4*)(lds + L_CS1 + lw * 128 + (8 * g + 4 * h2) * 4) = (f32x4){csacc[4 * g], csacc[4 * g + 1], csacc[4 * g + 2], csacc[4 * g + 3]};
        }
        STAMP(3);
        WAITV(0);
        BAR();
        STAMP(4);
        const f32x2 st = *(LAS f32x2*)(lds + L_STAT + (32 * m + t) * 8);
        mean1 = st[0]; rstd1 = st[1];
#pragma unroll
        for (int g = 0; g < 4; ++g) {
            const f32x4 cs = *(LAS f32x4*)(lds + L_CS1 + 128 + (8 * g + 4 * h2) * 4);
            const f32x4 v = *(LAS f32x4*)(lds + L_XCH + m * 4096 + g * 1024 + lane * 16);
#pragma unroll
            for (int e = 0; e < 4; ++e) yk[4 * g + e] = rstd1 * __builtin_fmaf(-mean1, cs[e], v[e]);
        }
    }
    const u32x4 aphi = tr2(lds, L_WPHI + a * 512 + (4 * h2 + q) * 32 + pp * 8, 256);
    {
        const u32x4 kf = {pack2(yk[0], yk[1]), pack2(yk[2], yk[3]), pack2(yk[4], yk[5]), pack2(yk[6], yk[7])};
        const f32x16 zero = {};
        const f32x16 pk = mfma(aphi, kf, zero);
        const unsigned sw = (t >> 1) & 7, row = 32 * m + t;
#pragma unroll
        for (int g = 0; g < 2; ++g)
            *(LAS u32x2*)(lds + L_VIMG + row * 64 + (((4 * a + 2 * g + h2) ^ sw) * 8)) = (u32x2){pack2(yk[8 + 4 * g], yk[9 + 4 * g]), pack2(yk[10 + 4 * g], yk[11 + 4 * g])};
        u32x4 pos, neg;
        dual_softmax(pk, pos, neg);
#pragma unroll
        for (int g = 0; g < 2; ++g) {
            *(LAS u32x2*)(lds + L_PHI + a * 8192 + row * 64 + (((2 * g + h2) ^ sw) * 8)) = (u32x2){pos[2 * g], pos[2 * g + 1]};
            *(LAS u32x2*)(lds + L_PHI + a * 8192 + row * 64 + (((4 + 2 * g + h2) ^ sw) * 8)) = (u32x2){neg[2 * g], neg[2 * g + 1]};
        }
    }
    BAR();
    STAMP(5);
    float yq[8];
    if (w < 4) {
        unsigned tq = L_WQ + trn + blk * 32;
        asm volatile("" : "+v"(tq));
        f32x16 qa = {};
        u32x4 qb[2][4];
#pragma unroll
        for (int s = 0; s < 4; ++s) qb[0][s] = tr2(lds, tq + s * 1024, 256);
#pragma unroll
        for (int g = 0; g < 8; ++g) {
            if (g < 7) {
#pragma unroll
                for (int s = 0; s < 4; ++s) qb[(g + 1) & 1][s] = tr2(lds, tq + (4 * g + 4 + s) * 1024, 256);
            }
            PIN();
#pragma unroll
            for (int s = 0; s < 4; ++s) qa = mfma(qb[g & 1][s], xf[4 * g + s], qa);
            PIN();
        }
#pragma unroll
        for (int g = 0; g < 2; ++g) *(LAS f32x4*)(lds + L_XCH + m * 4096 + g * 1024 + lane * 16) = (f32x4){qa[8 + 4 * g], qa[9 + 4 * g], qa[10 + 4 * g], qa[11 + 4 * g]};
        STAMP(6);
        BAR();
#pragma unroll
        for (int g = 0; g < 2; ++g) {
            const f32x4 c0 = *(LAS f32x4*)(lds + L_CSQ + (8 * g + 4 * h2) * 4), c1 = *(LAS f32x4*)(lds + L_CSQ + 128 + (8 * g + 4 * h2) * 4);
#pragma unroll
            for (int e = 0; e < 4; ++e) yq[4 * g + e] = rstd1 * __builtin_fmaf(-mean1, c0[e] + c1[e], qa[4 * g + e]);
        }
    } else {
        if (lw >= 2) {
            const int ah = lw - 2;
            f32x16 kv = {};
            unsigned aa[2], ab[2];
#pragma unroll
            for (int u = 0; u < 2; ++u) {
                const unsigned R = 8 * h2 + 4 * u + q, sw = (R >> 1) & 7;
                aa[u] = L_PHI + ah * 8192 + R * 64 + (((4 * blk + pp) ^ sw) * 8);
                ab[u] = L_VIMG + R * 64 + (((4 * (ah ^ blk) + pp) ^ sw) * 8);
                asm volatile("" : "+v"(aa[u]), "+v"(ab[u]));
            }
            u32x4 A[8], B[8];
#pragma unroll
            for (int s = 0; s < 8; ++s) {
                const s16x4 a0 = __builtin_amdgcn_ds_read_tr16_b64_v4i16((LAS s16x4*)(lds + aa[0] + s * 1024));
                const s16x4 a1 = __builtin_amdgcn_ds_read_tr16_b64_v4i16((LAS s16x4*)(lds + aa[1] + s * 1024));
                const s16x4 b0 = __builtin_amdgcn_ds_read_tr16_b64_v4i16((LAS s16x4*)(lds + ab[0] + s * 1024));
                const s16x4 b1 = __builtin_amdgcn_ds_read_tr16_b64_v4i16((LAS s16x4*)(lds + ab[1] + s * 1024));
                A[s] = __builtin_bit_cast(u32x4, __builtin_shufflevector(a0, a1, 0, 1, 2, 3, 4, 5, 6, 7));
                B[s] = __builtin_bit_cast(u32x4, __builtin_shufflevector(b0, b1, 0, 1, 2, 3, 4, 5, 6, 7));
                if (s == 3) PIN();
            }
            PIN();
#pragma unroll
            for (int s = 0; s < 8; ++s) {
#pragma unroll
                for (int e = 0; e < 4; ++e) B[s][e] = blk ? 0x3F803F80u : B[s][e];
                kv = mfma(A[s], B[s], kv);
            }
            if (t <= 16) {
                const unsigned base = WS_KVP + (j * 16 + p) * KVP_BYTES + ((ah * 4 + h2) * 17 + t) * 16;
                st16(rsW, (u32x4){pack2(kv[0], kv[1]), pack2(kv[2], kv[3]), pack2(kv[4], kv[5]), pack2(kv[6], kv[7])}, base, 0);
                st16(rsW, (u32x4){pack2(kv[8], kv[9]), pack2(kv[10], kv[11]), pack2(kv[12], kv[13]), pack2(kv[14], kv[15])}, base + 34 * 16, 0);
            }
            WAITV(0);
            arrive_publish(lds, 0, 2, ws, WS_F1 + (j * 16 + p) * 256, tag, lane);
            STAMP(6);
        } else {
            unsigned tq = L_WQ + lw * 16384 + trn + blk * 32;
            asm volatile("" : "+v"(tq));
            f32x16 qcs = {};
#pragma unroll
            for (int g = 0; g < 4; ++g) {
                u32x4 f[4];
#pragma unroll
                for (int s = 0; s < 4; ++s) f[s] = tr2(lds, tq + (4 * g + s) * 1024, 256);
                PIN();
#pragma unroll
                for (int s = 0; s < 4; ++s) qcs = mfma(f[s], ones, qcs);
            }
            if (t == 0) {
#pragma unroll
                for (int g = 0; g < 4; ++g) *(LAS f32x4*)(lds + L_CSQ + lw * 128 + (8 * g + 4 * h2) * 4) = (f32x4){qcs[4 * g], qcs[4 * g + 1], qcs[4 * g + 2], qcs[4 * g + 3]};
            }
            STAMP(6);
        }
        {
            const rsrc_t rsO = __builtin_amdgcn_make_buffer_rsrc((void*)wo, 0, 512 * 512 * 2, 0x00020000);
#pragma unroll
            for (int i = 0; i < 16; ++i) dma16(rsO, lb + L_X + (4 * i + lw) * 1024, vrow, (16 * (16 * (lw >> 1) + ((i + r2) & 15))) * 1024 + (64 * c + 32 * (lw & 1)) * 2);
        }
        BAR();
#pragma unroll
        for (int g = 0; g < 2; ++g) {
            const f32x4 c0 = *(LAS f32x4*)(lds + L_CSQ + (16 + 8 * g + 4 * h2) * 4), c1 = *(LAS f32x4*)(lds + L_CSQ + 128 + (16 + 8 * g + 4 * h2) * 4);
            const f32x4 v = *(LAS f32x4*)(lds + L_XCH + m * 4096 + g * 1024 + lane * 16);
#pragma unroll
            for (int e = 0; e < 4; ++e) yq[4 * g + e] = rstd1 * __builtin_fmaf(-mean1, c0[e] + c1[e], v[e]);
        }
    }
    STAMP(7);
    u32x4 qpos, qneg;
    {
        const u32x4 qf = {pack2(yq[0], yq[1]), pack2(yq[2], yq[3]), pack2(yq[4], yq[5]), pack2(yq[6], yq[7])};
        const f32x16 zero = {};
        const f32x16 pq = mfma(aphi, qf, zero);
        dual_softmax(pq, qpos, qneg);
    }
    STAMP(8);
    if (w == 0) poll_flags(ws, WS_F1 + j * 16 * 256, 16, tag, lane);
    BAR();
    STAMP(9);
    {
        const unsigned kb = WS_KVP + j * 16 * KVP_BYTES;
        float lo = 0.f, hi = 0.f;
        unsigned v[16], z[8];
#pragma unroll
        for (int i = 0; i < 16; ++i) v[i] = ld4(rsW, tid * 4, kb + ((i + 8 * (p & 1)) & 15) * KVP_BYTES);
        if (w == 0) {
#pragma unroll
            for (int i = 0; i < 8; ++i) z[i] = ld4(rsW, (512 + t) * 4 + h2 * 8 * KVP_BYTES, kb + i * KVP_BYTES);
        }
        PIN();
#pragma unroll
        for (int i = 0; i < 16; ++i) { lo += blo(v[i]); hi += bhi(v[i]); }
        if (w == 0) {
            float zl = 0.f, zh = 0.f;
#pragma unroll
            for (int i = 0; i < 8; ++i) { zl += blo(z[i]); zh += bhi(z[i]); }
            zl = halves_add(zl); zh = halves_add(zh);
            if (h2 == 0) *(LAS unsigned*)(lds + L_KVA + (512 + t) * 4) = pack2(zl, zh);
        }
        *(LAS unsigned*)(lds + L_KVA + tid * 4) = pack2(lo, hi);
    }
    BAR();
    STAMP(10);
    const int npass = j < 11 ? 3 : 2;
    const rsrc_t rsG = __builtin_amdgcn_make_buffer_rsrc((void*)wg, 0, 512 * 1376 * 2, 0x00020000);
    const rsrc_t rsU = __builtin_amdgcn_make_buffer_rsrc((void*)wu, 0, 512 * 1376 * 2, 0x00020000);
    const unsigned vgu = (lane >> 2) * 2752 + (lane & 3) * 16;
#define ISSUE_UNIT(u, halfbase) do { if (lw & 1) { _Pragma("unroll") for (int i_ = 0; i_ < 16; ++i_) { const int s_ = 2 * i_ + (lw >> 1); dma16(rsU, lb + (halfbase) + (2 * s_ + 1) * 1024, vgu, 16 * ((s_ + r3) & 31) * 2752 + 64 * (u)); } } \
                                     else { _Pragma("unroll") for (int i_ = 0; i_ < 16; ++i_) { const int s_ = 2 * i_ + (lw >> 1); dma16(rsG, lb + (halfbase) + (2 * s_) * 1024, vgu, 16 * ((s_ + r3) & 31) * 2752 + 64 * (u)); } } } while (0)
    const int m2 = w & 1, kh = (w >> 1) & 1;
    const int T2 = 4 * p + 2 * half + m2;
    const unsigned C0 = 64 * c + 32 * kh;
    {
        const unsigned col = t < 16 ? t : 16;
        const u32x4 kp = *(LAS u32x4*)(lds + L_KVA + ((a * 4 + h2) * 17 + col) * 16);
        const u32x4 kn = *(LAS u32x4*)(lds + L_KVA + ((a * 4 + 2 + h2) * 17 + col) * 16);
        f32x16 o = {};
        o = mfma(kp, qpos, o);
        o = mfma(kn, qneg, o);
        const float r = __builtin_amdgcn_rcpf(o[8] + 1e-6f);
        st16(rsW, (u32x4){pack2(o[0] * r, o[1] * r), pack2(o[2] * r, o[3] * r), pack2(o[4] * r, o[5] * r), pack2(o[6] * r, o[7] * r)},
             WS_O + ((4 * p + m) * 32 + 2 * j + a) * 1024 + lane * 16, 0);
        SBAR();
    }
    const unsigned trp = (4 * h2 + q) * 64 + blk * 32 + pp * 8;

    if (w >= 4) {
        const rsrc_t rsD = __builtin_amdgcn_make_buffer_rsrc((void*)wd, 0, 1376 * 512 * 2, 0x00020000);
#define ISSUE_WD(pi, halfbase) do { const int n_ = (pi) == 2 ? 11 : 16; const int k0_ = (pi) == 2 ? 64 + (lw >> 1) * 11 : 32 * (pi) + (lw >> 1) * 16; \
            _Pragma("unroll") for (int i_ = 0; i_ < 16; ++i_) if (i_ < n_) { const int ii_ = (pi) == 2 ? (i_ + r4 >= 11 ? i_ + r4 - 11 : i_ + r4) : ((i_ + r2) & 15); \
                dma16(rsD, lb + (halfbase) + (4 * i_ + lw) * 1024, vrow, 16 * (k0_ + ii_) * 1024 + (64 * c + 32 * (lw & 1)) * 2); } } while (0)
        ISSUE_UNIT(j, L_Y);
        WAITV(16);
        arrive_publish(lds, 1, 8, ws, WS_F2 + (p * 16 + j) * 256, tag, lane);
        STAMP(11);
        u32x4 cv[8];
        if (b < 86) {
            const unsigned so = (128 * lw) * 2752 + 64 * (b >> 1);
            if (b & 1) {
#pragma unroll
                for (int i = 0; i < 8; ++i) cv[i] = __builtin_amdgcn_raw_buffer_load_b128(rsU, vgu, so + 16 * i * 2752, 0);
            } else {
#pragma unroll
                for (int i = 0; i < 8; ++i) cv[i] = __builtin_amdgcn_raw_buffer_load_b128(rsG, vgu, so + 16 * i * 2752, 0);
            }
            PIN();
        }
        BAR();
        STAMP(12);
        if (b < 86) {
            float s[8];
#pragma unroll
            for (int e = 0; e < 8; ++e) s[e] = 0.f;
#pragma unroll
            for (int i = 0; i < 8; ++i)
#pragma unroll
                for (int e = 0; e < 4; ++e) { s[2 * e] += blo(cv[i][e]); s[2 * e + 1] += bhi(cv[i][e]); }
#pragma unroll
            for (int e = 0; e < 8; ++e) s[e] = stride4_add(s[e]);
            if (lane < 4) {
                const unsigned o = WS_CSG + (((b >> 1) * 4 + lw) * 64 + (b & 1) * 32 + lane * 8) * 4;
                st16(rsW, (u32x4){asu(s[0]), asu(s[1]), asu(s[2]), asu(s[3])}, o, 0);
                st16(rsW, (u32x4){asu(s[4]), asu(s[5]), asu(s[6]), asu(s[7])}, o + 16, 0);
            }
            WAITV(0);
            arrive_publish(lds, 6, 4, ws, WS_F5 + b * 256, tag, lane);
        }
        WAITV(0);
        BAR();
        SBAR();
        ISSUE_UNIT(16 + j, L_X);
        if (lw == 0) {
            const GAS u64* f = (const GAS u64*)(ws + WS_F5) + (2 * (16 * ((lane >> 1) < npass ? (lane >> 1) : 0) + j) + (lane & 1)) * 32;
#pragma unroll 1
            for (unsigned spin = 0; spin < SPIN_CAP; ++spin) {
                const u64 v = __hip_atomic_load(f, __ATOMIC_RELAXED, __HIP_MEMORY_SCOPE_AGENT);
                if (__all(v == tag)) break;
                __builtin_amdgcn_s_sleep(1);
            }
            asm volatile("" ::: "memory");
            unsigned cq[3][4];
#pragma unroll
            for (int pi = 0; pi < 3; ++pi)
#pragma unroll
                for (int k = 0; k < 4; ++k) cq[pi][k] = ld4(rsW, lane * 4, WS_CSG + ((16 * (pi < npass ? pi : 0) + j) * 4 + k) * 256);
            PIN();
#pragma unroll
            for (int pi = 0; pi < 3; ++pi) *(LAS float*)(lds + L_CS3 + (pi * 64 + lane) * 4) = (asf(cq[pi][0]) + asf(cq[pi][1])) + (asf(cq[pi][2]) + asf(cq[pi][3]));
        }
        STAMP(13);
        BAR();
        WAITV(0);
        BAR();
        SBAR();
        if (npass == 3) {
            ISSUE_UNIT(32 + j, L_Y);
            WAITV(0);
            BAR();
            SBAR();
            ISSUE_WD(0, L_X);
            BAR();
            SBAR();
            ISSUE_WD(1, L_Y);
            WAITV(16);
            BAR();
            WAITV(0);
            BAR();
            ISSUE_WD(2, L_X);
            WAITV(0);
            BAR();
        } else {
            ISSUE_WD(0, L_Y);
            BAR();
            SBAR();
            ISSUE_WD(1, L_X);
            WAITV(16);
            BAR();
            WAITV(0);
            BAR();
            ISSUE_WD(2, L_Y);
            WAITV(0);
            BAR();
        }
        BAR();
    } else {
        const unsigned vo = (32 * T2 + t) * 1024 + (C0 + 8 * h2) * 2;
        u32x4 xr0 = __builtin_amdgcn_raw_buffer_load_b128(rsX, vo, 0, 0);
        u32x4 xr1 = __builtin_amdgcn_raw_buffer_load_b128(rsX, vo + 32, 0, 0);
        WAITV(2);
        arrive_publish(lds, 1, 8, ws, WS_F2 + (p * 16 + j) * 256, tag, lane);
        STAMP(11);
        if (w == 0) poll_flags(ws, WS_F2 + p * 16 * 256, 16, tag, lane);
        BAR();
        STAMP(12);
        if (b == 0 && w == 1 && lane == 0) __hip_atomic_store((GAS u64*)ws, tag, __ATOMIC_RELAXED, __HIP_MEMORY_SCOPE_AGENT);
        u32x4 hres0, hres1;
        {
            u32x4 of[16];
#pragma unroll
            for (int i = 0; i < 16; ++i) of[i] = ld16(rsW, lane * 16, WS_O + (T2 * 32 + 16 * kh + ((i + r2) & 15)) * 1024);
            PIN();
            f32x16 a0 = {}, a1 = {};
            gemm_steps<16, 4096, 0>(lds, L_X + 2 * kh * 1024 + trp, of, a0, a1, nohook);
            const f32x16 snd = kh ? a0 : a1;
            f32x16 keep = kh ? a1 : a0;
#pragma unroll
            for (int g = 0; g < 4; ++g) *(LAS f32x4*)(lds + L_XCH + w * 4096 + g * 1024 + lane * 16) = (f32x4){snd[4 * g], snd[4 * g + 1], snd[4 * g + 2], snd[4 * g + 3]};
            STAMP(13);
            BAR();
#pragma unroll
            for (int g = 0; g < 4; ++g) {
                const f32x4 v = *(LAS f32x4*)(lds + L_XCH + (w ^ 2) * 4096 + g * 1024 + lane * 16);
#pragma unroll
                for (int e = 0; e < 4; ++e) keep[4 * g + e] += v[e];
            }
            { auto r = __builtin_amdgcn_permlane32_swap(xr0[0], xr0[2], false, false); xr0[0] = r[0]; xr0[2] = r[1];
              r = __builtin_amdgcn_permlane32_swap(xr0[1], xr0[3], false, false); xr0[1] = r[0]; xr0[3] = r[1];
              r = __builtin_amdgcn_permlane32_swap(xr1[0], xr1[2], false, false); xr1[0] = r[0]; xr1[2] = r[1];
              r = __builtin_amdgcn_permlane32_swap(xr1[1], xr1[3], false, false); xr1[1] = r[0]; xr1[3] = r[1]; }
#pragma unroll
            for (int e = 0; e < 4; ++e) {
                hres0[e] = pack2(keep[2 * e] + blo(xr0[e]), keep[2 * e + 1] + bhi(xr0[e]));
                hres1[e] = pack2(keep[8 + 2 * e] + blo(xr1[e]), keep[8 + 2 * e + 1] + bhi(xr1[e]));
            }
            const unsigned hb = WS_H + (T2 * 32 + 4 * c + 2 * kh) * 1024;
            st16(rsW, hres0, lane * 16, hb);
            st16(rsW, hres1, lane * 16, hb + 1024);
            float su = 0.f, sq = 0.f;
#pragma unroll
            for (int e = 0; e < 4; ++e) {
                const float v0 = blo(hres0[e]), v1 = bhi(hres0[e]), v2 = blo(hres1[e]), v3 = bhi(hres1[e]);
                su += (v0 + v1) + (v2 + v3);
                sq = __builtin_fmaf(v0, v0, __builtin_fmaf(v1, v1, __builtin_fmaf(v2, v2, __builtin_fmaf(v3, v3, sq))));
            }
            su = halves_add(su); sq = halves_add(sq);
            __builtin_amdgcn_raw_buffer_store_b32(asu(h2 ? sq : su), rsW, lane * 4 + WS_HST + (T2 * 16 + 2 * c + kh) * 256, 0, 16);
            SBAR();
            WAITV(0);
            arrive_publish(lds, 2, 4, ws, WS_F3 + (p * 16 + j) * 256, tag, lane);
        }
        STAMP(14);
        if (w == 0) poll_flags(ws, WS_F3 + p * 16 * 256, 16, tag, lane);
        BAR();
        STAMP(15);
        {
            const int T = 4 * p + w;
            u32x4 hf[32];
            float sv[16];
#pragma unroll
            for (int i = 0; i < 16; ++i) sv[i] = asf(ld4(rsW, lane * 4, WS_HST + (T * 16 + i) * 256));
#pragma unroll
            for (int s = 0; s < 32; ++s) hf[s] = ld16(rsW, lane * 16, WS_H + (T * 32 + ((s + r3) & 31)) * 1024);
            PIN();
            float mean, r2;
            {
                float tot = 0.f;
#pragma unroll
                for (int i = 0; i < 16; ++i) tot += sv[i];
                const u32x2 r = both_halves(tot);
                mean = asf(r[0]) * (1.0f / 512.0f);
                const float var = fmaxf(asf(r[1]) * (1.0f / 512.0f) - mean * mean, 0.f);
                r2 = __builtin_amdgcn_rcpf(var + 1e-5f);
                asm volatile("" : "+v"(mean), "+v"(r2));
                PIN();
            }
#pragma unroll
            for (int pi = 0; pi < 3; ++pi) {
                if (pi < npass) {
                    const unsigned hb = (pi & 1) ? L_X : L_Y;
                    f32x16 ag = {}, au = {};
                    gemm_steps<32, 2048, 0>(lds, hb + trp, hf, ag, au, [&](int g) {
                        if (pi > 0 && g == 5) { WAITV(0); arrive_publish(lds, 2 + pi, 4, ws, WS_F4 + (pi - 1) * 0x10000 + (p * 16 + j) * 256, tag, lane); PIN(); } });
                    BAR();
                    if (pi == 0) STAMP(16);
                    if (pi == 1) STAMP(17);
                    if (pi == 2) STAMP(18);
                    float y[16];
#pragma unroll
                    for (int g = 0; g < 4; ++g) {
                        const f32x4 cg = *(LAS f32x4*)(lds + L_CS3 + (pi * 64 + 8 * g + 4 * h2) * 4), cu = *(LAS f32x4*)(lds + L_CS3 + (pi * 64 + 32 + 8 * g + 4 * h2) * 4);
#pragma unroll
                        for (int e = 0; e < 4; ++e) y[4 * g + e] = (__builtin_fmaf(-mean, cg[e], ag[4 * g + e]) * r2) * __builtin_fmaf(-mean, cu[e], au[4 * g + e]);
                    }
                    const unsigned ab = WS_ACT + (T * 86 + 2 * (16 * pi + j)) * 1024;
                    st16(rsW, (u32x4){pack2(y[0], y[1]), pack2(y[2], y[3]), pack2(y[4], y[5]), pack2(y[6], y[7])}, lane * 16, ab);
                    st16(rsW, (u32x4){pack2(y[8], y[9]), pack2(y[10], y[11]), pack2(y[12], y[13]), pack2(y[14], y[15])}, lane * 16, ab + 1024);
                    SBAR();
                    if (pi == npass - 1) { WAITV(0); arrive_publish(lds, 3 + pi, 4, ws, WS_F4 + pi * 0x10000 + (p * 16 + j) * 256, tag, lane); }
                }
            }
        }
        STAMP(19);
        {
            f32x16 a0 = {}, a1 = {};
            const unsigned h0 = npass == 3 ? L_X : L_Y, h1 = npass == 3 ? L_Y : L_X;
            if (w == 0) { poll_flags(ws, WS_F4 + p * 16 * 256, 16, tag, lane); poll_flags(ws, WS_F4 + 0x10000 + p * 16 * 256, 16, tag, lane); }
            BAR();
            STAMP(20);
            u32x4 af[32], ag[11];
#pragma unroll
            for (int i = 0; i < 16; ++i) af[i] = ld16(rsW, lane * 16, WS_ACT + (T2 * 86 + kh * 16 + ((i + r2) & 15)) * 1024);
#pragma unroll
            for (int i = 0; i < 16; ++i) af[16 + i] = ld16(rsW, lane * 16, WS_ACT + (T2 * 86 + 32 + kh * 16 + ((i + r2) & 15)) * 1024);
            PIN();
            gemm_steps<16, 4096, 0>(lds, h0 + 2 * kh * 1024 + trp, af, a0, a1, nohook);
            STAMP(21);
            if (w == 0) poll_flags(ws, WS_F4 + 0x20000 + p * 16 * 256, 11, tag, lane);
            BAR();
            STAMP(22);
#pragma unroll
            for (int i = 0; i < 11; ++i) ag[i] = ld16(rsW, lane * 16, WS_ACT + (T2 * 86 + 64 + kh * 11 + (i + r4 >= 11 ? i + r4 - 11 : i + r4)) * 1024);
            PIN();
            gemm_steps<16, 4096, 16>(lds, h1 + 2 * kh * 1024 + trp, af, a0, a1, nohook);
            BAR();
            STAMP(23);
            gemm_steps<11, 4096, 0>(lds, h0 + 2 * kh * 1024 + trp, ag, a0, a1, nohook);
            const f32x16 snd = kh ? a0 : a1;
            f32x16 keep = kh ? a1 : a0;
#pragma unroll
            for (int g = 0; g < 4; ++g) *(LAS f32x4*)(lds + L_XCH + w * 4096 + g * 1024 + lane * 16) = (f32x4){snd[4 * g], snd[4 * g + 1], snd[4 * g + 2], snd[4 * g + 3]};
            BAR();
            STAMP(24);
#pragma unroll
            for (int g = 0; g < 4; ++g) {
                const f32x4 v = *(LAS f32x4*)(lds + L_XCH + (w ^ 2) * 4096 + g * 1024 + lane * 16);
#pragma unroll
                for (int e = 0; e < 4; ++e) keep[4 * g + e] += v[e];
            }
            u32x4 o0, o1;
#pragma unroll
            for (int e = 0; e < 4; ++e) {
                o0[e] = pack2(keep[2 * e] + blo(hres0[e]), keep[2 * e + 1] + bhi(hres0[e]));
                o1[e] = pack2(keep[8 + 2 * e] + blo(hres1[e]), keep[8 + 2 * e + 1] + bhi(hres1[e]));
            }
            { auto r = __builtin_amdgcn_permlane32_swap(o0[0], o0[2], false, false); o0[0] = r[0]; o0[2] = r[1];
              r = __builtin_amdgcn_permlane32_swap(o0[1], o0[3], false, false); o0[1] = r[0]; o0[3] = r[1];
              r = __builtin_amdgcn_permlane32_swap(o1[0], o1[2], false, false); o1[0] = r[0]; o1[2] = r[1];
              r = __builtin_amdgcn_permlane32_swap(o1[1], o1[3], false, false); o1[1] = r[0]; o1[3] = r[1]; }
            const rsrc_t rsOut = __builtin_amdgcn_make_buffer_rsrc((void*)out, 0, 2048 * 512 * 2, 0x00020000);
            __builtin_amdgcn_raw_buffer_store_b128(o0, rsOut, vo, 0, 0);
            __builtin_amdgcn_raw_buffer_store_b128(o1, rsOut, vo + 32, 0, 0);
        }
        STAMP(25); CSTAMP(31);
    }
}

extern "C" void kernel_launch(void* const* d_in, const int* in_sizes, int n_in, void* d_out, int out_size, void* d_ws, size_t ws_size, hipStream_t stream) {
    (void)in_sizes; (void)n_in; (void)out_size; (void)ws_size;
    (void)hipFuncSetAttribute((const void*)hedgehog_block, hipFuncAttributeMaxDynamicSharedMemorySize, LDS_BYTES);
    hedgehog_block<<<256, 512, LDS_BYTES, stream>>>((const bf16_t*)d_in[0], (const bf16_t*)d_in[1], (const bf16_t*)d_in[2], (const bf16_t*)d_in[3],
                                                    (const bf16_t*)d_in[5], (const bf16_t*)d_in[6], (const bf16_t*)d_in[7], (bf16_t*)d_out, (char*)d_ws);
}
```

```cpp
#include <hip/hip_runtime.h>
#include <hip/hip_bf16.h>

#define LAS __attribute__((address_space(3)))
#define GAS __attribute__((address_space(1)))
#define DI __device__ __forceinline__
typedef unsigned short bf16_t;
typedef unsigned u32x4 __attribute__((ext_vector_type(4)));
typedef unsigned u32x2 __attribute__((ext_vector_type(2)));
typedef short s16x4 __attribute__((ext_vector_type(4)));
typedef __bf16 bf16x8 __attribute__((ext_vector_type(8)));
typedef __bf16 bf16x2 __attribute__((ext_vector_type(2)));
typedef float f32x16 __attribute__((ext_vector_type(16)));
typedef float f32x4 __attribute__((ext_vector_type(4)));
typedef float f32x2 __attribute__((ext_vector_type(2)));
typedef unsigned long long u64;
typedef __amdgpu_buffer_rsrc_t rsrc_t;

constexpr unsigned WS_F1 = 0x10000, WS_F2 = 0x20000, WS_F3 = 0x30000, WS_F4 = 0x40000, WS_F5 = 0x70000;
constexpr unsigned WS_KVP = 0x80000, KVP_BYTES = 2176;
constexpr unsigned WS_HST = 0x110000;
constexpr unsigned WS_XST = 0x160000, WS_CSG = 0x170000;
constexpr unsigned WS_O = 0x200000, WS_H = 0x400000, WS_ACT = 0x600000, WS_PROF = 0xC00000, WS_BYTES = 0x1000000;
constexpr unsigned L_X = 0, L_Y = 65536, L_XCH = 131072, L_MISC = 147456;
constexpr unsigned L_STAT = L_MISC, L_CS1 = L_MISC + 1024, L_CSQ = L_MISC + 1280, L_WPHI = L_MISC + 1536, L_KVA = L_MISC + 2560, L_CS3 = L_MISC + 4736, L_CNT = L_MISC + 5504, L_DUMMY = L_MISC + 6144, L_HRES = L_MISC + 8192;
constexpr unsigned L_WQ = 65536, L_PHI = 98304, L_VIMG = 114688, SS_BYTES = 24576;
constexpr unsigned LDS_BYTES = 163840;
constexpr unsigned SPIN_CAP = 40000;
constexpr float LOG2E = 1.4426950408889634f;

DI float asf(unsigned v) { return __builtin_bit_cast(float, v); }
DI unsigned asu(float v) { return __builtin_bit_cast(unsigned, v); }
DI unsigned pack2(float lo, float hi) { bf16x2 v = {(__bf16)lo, (__bf16)hi}; return __builtin_bit_cast(unsigned, v); }
DI float blo(unsigned v) { return __builtin_bit_cast(float, v << 16); }
DI float bhi(unsigned v) { return __builtin_bit_cast(float, v & 0xffff0000u); }
DI f32x16 mfma(u32x4 a, u32x4 b, f32x16 c) { return __builtin_amdgcn_mfma_f32_32x32x16_bf16(__builtin_bit_cast(bf16x8, a), __builtin_bit_cast(bf16x8, b), c, 0, 0, 0); }
DI f32x4 mfma16(u32x4 a, u32x4 b, f32x4 c) { return __builtin_amdgcn_mfma_f32_16x16x32_bf16(__builtin_bit_cast(bf16x8, a), __builtin_bit_cast(bf16x8, b), c, 0, 0, 0); }
DI u32x4 tr2(LAS char* lds, unsigned off, unsigned second) {
    const s16x4 lo = __builtin_amdgcn_ds_read_tr16_b64_v4i16((LAS s16x4*)(lds + off));
    const s16x4 hi = __builtin_amdgcn_ds_read_tr16_b64_v4i16((LAS s16x4*)(lds + off + second));
    return __builtin_bit_cast(u32x4, __builtin_shufflevector(lo, hi, 0, 1, 2, 3, 4, 5, 6, 7));
}
DI void dma16(const rsrc_t rs, unsigned ldsaddr, unsigned voff, unsigned soff) {
    asm volatile("s_mov_b32 m0, %0\n\ts_nop 0\n\tbuffer_load_dwordx4 %1, %2, %3 offen lds" ::"s"(ldsaddr), "v"(voff), "s"(rs), "s"(soff) : "memory");
}
#define WAITV(n) asm volatile("s_waitcnt vmcnt(" #n ")" ::: "memory")
#define PIN() do { asm volatile("" ::: "memory"); __builtin_amdgcn_sched_barrier(0); } while (0)
template <int N> DI void waitv() { asm volatile("s_waitcnt vmcnt(%0)" ::"n"(N) : "memory"); }
#define K0 4
#define STAMP2_SS(ss) do { if (ss == 0) STAMP2(0); if (ss == 1) STAMP2(1); if (ss == 2) STAMP2(2); if (ss == 3) STAMP2(3); if (ss == 4) STAMP2(4); if (ss == 5) STAMP2(5); if (ss == 6) STAMP2(6); if (ss == 7) STAMP2(7); } while (0)
#define SBAR() asm volatile("s_barrier" ::: "memory")
#define BAR() asm volatile("s_waitcnt lgkmcnt(0)\n\ts_barrier" ::: "memory")
DI u32x2 both_halves(float v) { const unsigned a = asu(v); const auto r = __builtin_amdgcn_permlane32_swap(a, a, false, false); return (u32x2){r[0], r[1]}; }
DI float halves_add(float v) { const u32x2 r = both_halves(v); return asf(r[0]) + asf(r[1]); }
DI float halves_max(float v) { const u32x2 r = both_halves(v); return fmaxf(asf(r[0]), asf(r[1])); }
DI float halves_min(float v) { const u32x2 r = both_halves(v); return fminf(asf(r[0]), asf(r[1])); }
DI float dpp_ror4(float v) { return asf(__builtin_amdgcn_update_dpp(0, (int)asu(v), 0x124, 0xf, 0xf, true)); }
DI float dpp_ror8(float v) { return asf(__builtin_amdgcn_update_dpp(0, (int)asu(v), 0x128, 0xf, 0xf, true)); }
DI float rows16_add(float v) { const unsigned a = asu(v); const auto r = __builtin_amdgcn_permlane16_swap(a, a, false, false); return asf(r[0]) + asf(r[1]); }
DI float stride4_add(float v) { v += dpp_ror4(v); v += dpp_ror8(v); v = rows16_add(v); return halves_add(v); }
DI float quad_add(float v) {
    v += __builtin_bit_cast(float, __builtin_amdgcn_update_dpp(0, __builtin_bit_cast(int, v), 0xB1, 0xf, 0xf, true));
    v += __builtin_bit_cast(float, __builtin_amdgcn_update_dpp(0, __builtin_bit_cast(int, v), 0x4E, 0xf, 0xf, true));
    return v;
}
DI void st16(const rsrc_t rs, u32x4 v, unsigned voff, unsigned soff) { __builtin_amdgcn_raw_buffer_store_b128(v, rs, voff + soff, 0, 16); }
DI u32x4 ld16(const rsrc_t rs, unsigned voff, unsigned soff) { return __builtin_amdgcn_raw_buffer_load_b128(rs, voff, soff, 16); }
DI unsigned ld4(const rsrc_t rs, unsigned voff, unsigned soff) { return __builtin_amdgcn_raw_buffer_load_b32(rs, voff, soff, 16); }

DI void poll_flags3(const char* ws, unsigned off, u64 tag, int lane) {
    const int l = lane < 11 ? lane : 10;
    const GAS u64* f = (const GAS u64*)(ws + off) + (l < 6 ? l : l + 2) * 32;
#pragma unroll 1
    for (unsigned spin = 0; spin < SPIN_CAP; ++spin) {
        const u64 v = __hip_atomic_load(f, __ATOMIC_RELAXED, __HIP_MEMORY_SCOPE_AGENT);
        if (__all(v == tag)) break;
        __builtin_amdgcn_s_sleep(1);
    }
    asm volatile("" ::: "memory");
}
DI void nohook(int) {}
DI void poll_flags(const char* ws, unsigned off, int n, u64 tag, int lane) {
    const GAS u64* f = (const GAS u64*)(ws + off) + (lane < n ? lane : n - 1) * 32;
#pragma unroll 1
    for (unsigned spin = 0; spin < SPIN_CAP; ++spin) {
        const u64 v = __hip_atomic_load(f, __ATOMIC_RELAXED, __HIP_MEMORY_SCOPE_AGENT);
        if (__all(v == tag)) break;
        __builtin_amdgcn_s_sleep(1);
    }
    asm volatile("" ::: "memory");
}
DI void arrive_publish(LAS char* lds, int cnt, unsigned n, char* ws, unsigned flag_off, u64 tag, int lane) {
    if (lane == 0) {
        const unsigned old = __hip_atomic_fetch_add((LAS unsigned*)(lds + L_CNT) + cnt, 1u, __ATOMIC_RELAXED, __HIP_MEMORY_SCOPE_WORKGROUP);
        if (old == n - 1) __hip_atomic_store((GAS u64*)(ws + flag_off), tag, __ATOMIC_RELAXED, __HIP_MEMORY_SCOPE_AGENT);
    }
}
DI void dual_softmax(const f32x16& p, u32x4& pos, u32x4& neg) {
    float mx = fmaxf(fmaxf(fmaxf(p[0], p[1]), fmaxf(p[2], p[3])), fmaxf(fmaxf(p[4], p[5]), fmaxf(p[6], p[7])));
    float mn = fminf(fminf(fminf(p[0], p[1]), fminf(p[2], p[3])), fminf(fminf(p[4], p[5]), fminf(p[6], p[7])));
    mx = halves_max(mx); mn = halves_min(mn);
    const float cx = -mx * LOG2E, cn = mn * LOG2E;
    float ep[8], en[8], sp = 0.f, sn = 0.f;
#pragma unroll
    for (int r = 0; r < 8; ++r) {
        ep[r] = __builtin_amdgcn_exp2f(__builtin_fmaf(p[r], LOG2E, cx));
        en[r] = __builtin_amdgcn_exp2f(__builtin_fmaf(p[r], -LOG2E, cn));
        sp += ep[r]; sn += en[r];
    }
    const float rp = __builtin_amdgcn_rcpf(halves_add(sp)), rn = __builtin_amdgcn_rcpf(halves_add(sn));
#pragma unroll
    for (int i = 0; i < 4; ++i) { pos[i] = pack2(ep[2 * i] * rp, ep[2 * i + 1] * rp); neg[i] = pack2(en[2 * i] * rn, en[2 * i + 1] * rn); }
}

#define SB() __builtin_amdgcn_sched_barrier(0)
template <int N, int STRIDE, int SECOND, int OFF, int NA, class Hook>
DI void gemm2(LAS char* lds, unsigned b0, unsigned b1, const u32x4 (&act)[NA], f32x16& acc0, f32x16& acc1, Hook hook) {
    u32x4 fb[2][4];
    asm volatile("" : "+v"(b0), "+v"(b1));
    constexpr int NG = (N + 1) / 2;
#pragma unroll
    for (int e = 0; e < 2; ++e) { fb[0][2 * e] = tr2(lds, b0 + e * STRIDE, SECOND); fb[0][2 * e + 1] = tr2(lds, b1 + e * STRIDE, SECOND); }
    SB();
#pragma unroll
    for (int g = 0; g < NG; ++g) {
#pragma unroll
        for (int e = 0; e < 2; ++e) {
            const int i = 2 * g + e, n = i + 2;
            if (i < N) acc0 = mfma(fb[g & 1][2 * e], act[OFF + i], acc0);
            if (n < N) fb[(g + 1) & 1][2 * e] = tr2(lds, b0 + n * STRIDE, SECOND);
            SB();
            if (i < N) acc1 = mfma(fb[g & 1][2 * e + 1], act[OFF + i], acc1);
            if (n < N) fb[(g + 1) & 1][2 * e + 1] = tr2(lds, b1 + n * STRIDE, SECOND);
            SB();
        }
        hook(g);
    }
}
template <int OFF, class Hook>
DI void gemm4(LAS char* lds, unsigned b0, unsigned b1, const u32x4 (&act)[32], f32x16& g0, f32x16& g1, f32x16& u0, f32x16& u1, Hook hook) {
    u32x4 fb[2][4];
    asm volatile("" : "+v"(b0), "+v"(b1));
    fb[0][0] = tr2(lds, b0, 1024); fb[0][1] = tr2(lds, b1, 1024); fb[0][2] = tr2(lds, b0 + 2048, 1024); fb[0][3] = tr2(lds, b1 + 2048, 1024);
    SB();
#pragma unroll
    for (int i = 0; i < 16; ++i) {
        const int o = (i + 1) * 4096;
        g0 = mfma(fb[i & 1][0], act[OFF + i], g0);
        if (i < 15) fb[(i + 1) & 1][0] = tr2(lds, b0 + o, 1024);
        SB();
        g1 = mfma(fb[i & 1][1], act[OFF + i], g1);
        if (i < 15) fb[(i + 1) & 1][1] = tr2(lds, b1 + o, 1024);
        SB();
        u0 = mfma(fb[i & 1][2], act[OFF + i], u0);
        if (i < 15) fb[(i + 1) & 1][2] = tr2(lds, b0 + o + 2048, 1024);
        SB();
        u1 = mfma(fb[i & 1][3], act[OFF + i], u1);
        if (i < 15) fb[(i + 1) & 1][3] = tr2(lds, b1 + o + 2048, 1024);
        SB();
        hook(i);
    }
}
DI void touch_lines(const rsrc_t rs, unsigned ldsaddr, unsigned voff, unsigned soff) {
    asm volatile("s_mov_b32 m0, %0\n\ts_nop 0\n\tbuffer_load_dword %1, %2, %3 offen lds" ::"s"(ldsaddr), "v"(voff), "s"(rs), "s"(soff) : "memory");
}

#define STAMP(i) do {} while (0)
#define CSTAMP(i) do {} while (0)
#define STAMP2(i) do {} while (0)

__global__ void __launch_bounds__(512, 2)
hedgehog_block(const bf16_t* __restrict__ x, const bf16_t* __restrict__ wqkv, const bf16_t* __restrict__ wphi, const bf16_t* __restrict__ wo,
               const bf16_t* __restrict__ wg, const bf16_t* __restrict__ wu, const bf16_t* __restrict__ wd, bf16_t* __restrict__ out, char* __restrict__ ws) {
    extern __shared__ __attribute__((aligned(16))) char smem[];
    LAS char* lds = (LAS char*)smem;
    const unsigned lb = (unsigned)(size_t)lds;
    const int tid = threadIdx.x, lane = tid & 63;
    const int w = __builtin_amdgcn_readfirstlane(tid >> 6);
    const int b = blockIdx.x, xcd = b & 7, slot = b >> 3;
    const int p = 4 * (xcd >> 1) + (slot >> 3), j = 8 * (xcd & 1) + (slot & 7);
    const int pP = 2 * xcd + (slot >> 4), jP = slot & 15, c = slot & 7, half = (slot >> 3) & 1;
    const bool has3 = (j & 7) < (j < 8 ? 6 : 5);
    const int u3 = 32 + (j < 8 ? j : j - 2);
    const int t = lane & 31, h2 = lane >> 5, q = (lane & 15) >> 2, pp = lane & 3, blk = (lane >> 4) & 1;
    const int m = w & 3, a = w >> 2, lw = w & 3;
    const int q4 = slot >> 3;
    const int r1 = (c + 2 * q4) & 7, r2 = (2 * c + 4 * q4) & 15, r3 = (4 * c + 8 * q4) & 31, r4 = (c + 3 * q4) % 11;
    STAMP(0); CSTAMP(30);
    const rsrc_t rsX = __builtin_amdgcn_make_buffer_rsrc((void*)x, 0, 2048 * 512 * 2, 0x00020000);
    const rsrc_t rsQ = __builtin_amdgcn_make_buffer_rsrc((void*)wqkv, 0, 512 * 1536 * 2, 0x00020000);
    const rsrc_t rsW = __builtin_amdgcn_make_buffer_rsrc((void*)ws, 0, WS_BYTES, 0x00020000);
    u64 epoch, tag = 0;
    asm volatile("global_load_dwordx2 %0, %1, off sc1" : "=v"(epoch) : "v"((const GAS u64*)ws) : "memory");
#define TAKE_TAG() do { const u64 e_ = ((u64)(unsigned)__builtin_amdgcn_readfirstlane((unsigned)(epoch >> 32)) << 32) | (unsigned)__builtin_amdgcn_readfirstlane((unsigned)epoch); \
                        tag = e_ + 0x9E3779B97F4A7C15ull; } while (0)
    const u32x4 ones = {0x3F803F80u, 0x3F803F80u, 0x3F803F80u, 0x3F803F80u};
    const unsigned c128 = ((lane & 7) ^ (4 * ((lane >> 4) & 1))) * 16;
    const unsigned vrow = (lane >> 3) * 1024 + c128;
    const unsigned trn = (8 * h2 + q) * 64 + pp * 8;

    const rsrc_t rsG = __builtin_amdgcn_make_buffer_rsrc((void*)wg, 0, 512 * 1376 * 2, 0x00020000);
    const rsrc_t rsU = __builtin_amdgcn_make_buffer_rsrc((void*)wu, 0, 512 * 1376 * 2, 0x00020000);
    const unsigned vgu = (lane >> 2) * 2752 + (lane & 3) * 16, vgu8 = (lane >> 3) * 2752 + c128;
#define ISSUE_STAGE(st, halfbase) do { _Pragma("unroll 4") for (int i_ = 0; i_ < 16; ++i_) { const unsigned so_ = (16 * ((16 * (st) + i_ + r3) & 31) + 8 * (lw & 1)) * 2752 + 128 * j; \
            if (lw & 2) dma16(rsU, lb + (halfbase) + i_ * 4096 + lw * 1024, vgu8, so_); else dma16(rsG, lb + (halfbase) + i_ * 4096 + lw * 1024, vgu8, so_); } } while (0)
#define ISSUE_UNIT(u, halfbase) do { _Pragma("unroll 4") for (int i_ = 0; i_ < 16; ++i_) { const int s_ = 2 * i_ + (lw >> 1); const unsigned so_ = 16 * ((s_ + r3) & 31) * 2752 + 64 * (u); \
            if (lw & 1) dma16(rsU, lb + (halfbase) + (2 * s_ + 1) * 1024, vgu, so_); else dma16(rsG, lb + (halfbase) + (2 * s_) * 1024, vgu, so_); } } while (0)
    u32x4 xf[32];
    float yk[16];
    float mean1, rstd1;
    if (w < 4) {
        {
            const unsigned R = 128 * p + 8 * j + 2 * w;
            const u32x4 v0 = __builtin_amdgcn_raw_buffer_load_b128(rsX, lane * 16, R * 1024, 0), v1 = __builtin_amdgcn_raw_buffer_load_b128(rsX, lane * 16, R * 1024 + 1024, 0);
            if (tid < 16) *(LAS unsigned*)(lds + L_CNT + tid * 4) = 0u;
            f32x2 a1 = {0.f, 0.f}, a2 = {0.f, 0.f}, b1 = {0.f, 0.f}, b2 = {0.f, 0.f};
#pragma unroll
            for (int e = 0; e < 4; ++e) {
                const f32x2 z0 = {blo(v0[e]), bhi(v0[e])}, z1 = {blo(v1[e]), bhi(v1[e])};
                a1 += z0; a2 += z0 * z0; b1 += z1; b2 += z1 * z1;
            }
            const float su0 = stride4_add(quad_add(a1[0] + a1[1])), sq0 = stride4_add(quad_add(a2[0] + a2[1]));
            const float su1 = stride4_add(quad_add(b1[0] + b1[1])), sq1 = stride4_add(quad_add(b2[0] + b2[1]));
            const float su = (lane & 1) ? su1 : su0, sq = (lane & 1) ? sq1 : sq0;
            const float mean = su * (1.0f / 512.0f), var = fmaxf(sq * (1.0f / 512.0f) - mean * mean, 0.f);
            const float rstd = __builtin_amdgcn_rsqf(var + 1e-5f);
            asm volatile("s_waitcnt vmcnt(0)" : "+v"(epoch)::"memory");
            TAKE_TAG();
            if (lane < 2) st16(rsW, (u32x4){asu(mean), asu(rstd), (unsigned)tag, (unsigned)(tag >> 32)}, (R + lane) * 16, WS_XST);
        }
        STAMP(1);
        const unsigned xa0 = (32 * m + t) * 128 + ((h2 ^ ((t >> 1) & 7)) * 16);
        const unsigned twA = 16384 + (blk ? 4096 + 32 : 0) + trn, twB = 16384 + (blk ? 4096 : 32) + trn;
        f32x16 accA = {}, accB = {};
        u32x4 fa[2][2], fb[2][2], xs;
        unsigned bA = twA, bB = twB, bx0 = xa0, bx1 = xa0 ^ 32, bx2 = xa0 ^ 64, bx3 = xa0 ^ 96;
        asm volatile("" : "+v"(bA), "+v"(bB), "+v"(bx0), "+v"(bx1), "+v"(bx2), "+v"(bx3));
#pragma unroll
        for (int ss = 0; ss <= 8; ++ss) {
            if (ss < 8) {
                BAR();
                STAMP2_SS(ss);
                if (ss == 0) STAMP(2);
                if (ss == 6) { xs = ld16(rsW, (128 * p + 32 * m + t) * 16, WS_XST); SB(); }
            }
#pragma unroll
            for (int e = 0; e < 2; ++e) {
                if (ss > 0) accA = mfma(fa[1][e], xf[4 * ss - 2 + e], accA);
                if (ss < 8) { xf[4 * ss + e] = *(LAS u32x4*)(lds + (e ? bx1 : bx0)); fa[0][e] = tr2(lds, bA + e * 1024, 256); }
                SB();
                if (ss > 0) accB = mfma(fb[1][e], xf[4 * ss - 2 + e], accB);
                if (ss < 8) fb[0][e] = tr2(lds, bB + e * 1024, 256);
                SB();
            }
            if (ss < 8) {
#pragma unroll
                for (int e = 0; e < 2; ++e) {
                    accA = mfma(fa[0][e], xf[4 * ss + e], accA);
                    xf[4 * ss + 2 + e] = *(LAS u32x4*)(lds + (e ? bx3 : bx2)); fa[1][e] = tr2(lds, bA + (2 + e) * 1024, 256);
                    SB();
                    accB = mfma(fb[0][e], xf[4 * ss + e], accB);
                    fb[1][e] = tr2(lds, bB + (2 + e) * 1024, 256);
                    SB();
                }
                if (ss < 7) {
                    const unsigned d = ss == 5 ? (unsigned)(-5 * (int)SS_BYTES) : SS_BYTES;
                    bA += d; bB += d; bx0 += d; bx1 += d; bx2 += d; bx3 += d;
                    asm volatile("" : "+v"(bA), "+v"(bB), "+v"(bx0), "+v"(bx1), "+v"(bx2), "+v"(bx3));
                }
            }
        }
        STAMP(3);
#pragma unroll
        for (int g = 0; g < 4; ++g) *(LAS f32x4*)(lds + L_XCH + m * 4096 + g * 1024 + lane * 16) = (f32x4){accB[4 * g], accB[4 * g + 1], accB[4 * g + 2], accB[4 * g + 3]};
#pragma unroll 1
        for (unsigned spin = 0; spin < SPIN_CAP; ++spin) {
            if (__all(xs[2] == (unsigned)tag && xs[3] == (unsigned)(tag >> 32))) break;
            __builtin_amdgcn_s_sleep(1);
            xs = ld16(rsW, (128 * p + 32 * m + t) * 16, WS_XST);
        }
        mean1 = asf(xs[0]); rstd1 = asf(xs[1]);
        if (h2 == 0) *(LAS f32x2*)(lds + L_STAT + (32 * m + t) * 8) = (f32x2){mean1, rstd1};
        BAR();
        STAMP(4);
#pragma unroll
        for (int g = 0; g < 4; ++g) {
            const f32x4 cs = *(LAS f32x4*)(lds + L_CS1 + (8 * g + 4 * h2) * 4);
#pragma unroll
            for (int e = 0; e < 4; ++e) yk[4 * g + e] = rstd1 * __builtin_fmaf(-mean1, cs[e], accA[4 * g + e]);
        }
    } else {
        const unsigned vx0 = (lane >> 3) * 1024 + (((lane & 7) ^ (lane >> 4)) * 16), vx1 = (lane >> 3) * 1024 + (((lane & 7) ^ (4 + (lane >> 4))) * 16);
        const unsigned vwk = (lane >> 2) * 3072 + (lane & 3) * 16, vwv = (lane >> 2) * 3072 + ((lane & 3) ^ 2) * 16;
#define ISSUE_SS(ss, v) do { const unsigned sb_ = lb + ((ss) % 6) * SS_BYTES, ks_ = ((ss) + r1) & 7; \
            const unsigned sx_ = (128 * p + 32 * (v)) * 1024 + ks_ * 128, sw_ = (16 * (v) + 64 * ks_) * 3072 + (512 + 32 * j) * 2; \
            dma16(rsX, sb_ + (4 * (v)) * 1024, vx0, sx_); \
            dma16(rsX, sb_ + (4 * (v) + 1) * 1024, vx1, sx_ + 8 * 1024); \
            dma16(rsQ, sb_ + 16384 + (v) * 1024, vwk, sw_); \
            dma16(rsQ, sb_ + 16384 + 4096 + (v) * 1024, vwv, sw_ + 1024); \
            dma16(rsX, sb_ + (4 * (v) + 2) * 1024, vx0, sx_ + 16 * 1024); \
            dma16(rsX, sb_ + (4 * (v) + 3) * 1024, vx1, sx_ + 24 * 1024); } while (0)
        if (lw == 0) {
            const rsrc_t rsP = __builtin_amdgcn_make_buffer_rsrc((void*)wphi, 0, 32 * 256 * 2, 0x00020000);
            dma16(rsP, lb + L_WPHI, lane * 16, j * 1024);
        }
#pragma unroll
        for (int ss = 0; ss < K0; ++ss) ISSUE_SS(ss, lw);
        STAMP(1);
        f32x4 csacc = {};
        const unsigned tw = 16384 + ((lw & 1) ? 4096 + ((lw >> 1) ^ 1) * 32 : (lw >> 1) * 32) + (8 * (lane >> 4) + q) * 64 + pp * 8;
        constexpr int NL = 8 - K0;
#define P1WAIT(s) (6 * ((((s) + K0 - 1 < 7 ? (s) + K0 - 1 : 7) - (s))) + ((s) > 4 ? 8 : 0))
#pragma unroll
        for (int ss = 0; ss < 8; ++ss) {
            if (ss == 0) { asm volatile("s_waitcnt vmcnt(%1)" : "+v"(epoch) : "n"(P1WAIT(0)) : "memory"); }
            if (ss == 1) waitv<P1WAIT(1)>();
            if (ss == 2) waitv<P1WAIT(2)>();
            if (ss == 3) waitv<P1WAIT(3)>();
            if (ss == 4) waitv<P1WAIT(4)>();
            if (ss == 5) waitv<P1WAIT(5)>();
            if (ss == 6) waitv<P1WAIT(6)>();
            if (ss == 7) waitv<P1WAIT(7)>();
            STAMP2_SS(ss);
            BAR();
            if (ss == 0) { TAKE_TAG(); STAMP(2); }
            unsigned bw = (ss % 6) * SS_BYTES + tw;
            asm volatile("" : "+v"(bw));
            const u32x4 f0 = tr2(lds, bw, 256), f1 = tr2(lds, bw + 2048, 256);
            SB();
            if (ss < NL) ISSUE_SS(K0 + ss, lw);
            if (ss == 4) {
#pragma unroll 4
                for (int i = 0; i < 8; ++i) dma16(rsQ, lb + L_WQ + (8 * lw + i) * 1024, vwk, (16 * ((8 * lw + i + 4 * r1) & 31)) * 3072 + 32 * j * 2);
            }
            if (ss == 7 && b < 86 && lw < 2) {
#pragma unroll 4
                for (int i = 0; i < 16; ++i) {
                    if (b & 1) dma16(rsU, lb + lw * 49152 + i * 1024, vgu, 16 * (16 * lw + i) * 2752 + 64 * (b >> 1));
                    else dma16(rsG, lb + lw * 49152 + i * 1024, vgu, 16 * (16 * lw + i) * 2752 + 64 * (b >> 1));
                }
            }
            csacc = mfma16(f0, ones, csacc);
            csacc = mfma16(f1, ones, csacc);
            SB();
        }
        if ((lane & 15) == 0) *(LAS f32x4*)(lds + L_CS1 + (lw >> 1) * 128 + (lw & 1) * 64 + (lane >> 4) * 16) = csacc;
        STAMP(3);
        BAR();
        STAMP(4);
        const f32x2 st = *(LAS f32x2*)(lds + L_STAT + (32 * m + t) * 8);
        mean1 = st[0]; rstd1 = st[1];
#pragma unroll
        for (int g = 0; g < 4; ++g) {
            const f32x4 cs = *(LAS f32x4*)(lds + L_CS1 + 128 + (8 * g + 4 * h2) * 4);
            const f32x4 v = *(LAS f32x4*)(lds + L_XCH + m * 4096 + g * 1024 + lane * 16);
#pragma unroll
            for (int e = 0; e < 4; ++e) yk[4 * g + e] = rstd1 * __builtin_fmaf(-mean1, cs[e], v[e]);
        }
    }
    const u32x4 aphi = tr2(lds, L_WPHI + a * 512 + (4 * h2 + q) * 32 + pp * 8, 256);
    {
        const u32x4 kf = {pack2(yk[0], yk[1]), pack2(yk[2], yk[3]), pack2(yk[4], yk[5]), pack2(yk[6], yk[7])};
        const f32x16 zero = {};
        const f32x16 pk = mfma(aphi, kf, zero);
        const unsigned sw = (t >> 1) & 7, row = 32 * m + t;
#pragma unroll
        for (int g = 0; g < 2; ++g)
            *(LAS u32x2*)(lds + L_VIMG + row * 64 + (((4 * a + 2 * g + h2) ^ sw) * 8)) = (u32x2){pack2(yk[8 + 4 * g], yk[9 + 4 * g]), pack2(yk[10 + 4 * g], yk[11 + 4 * g])};
        u32x4 pos, neg;
        dual_softmax(pk, pos, neg);
#pragma unroll
        for (int g = 0; g < 2; ++g) {
            *(LAS u32x2*)(lds + L_PHI + a * 8192 + row * 64 + (((2 * g + h2) ^ sw) * 8)) = (u32x2){pos[2 * g], pos[2 * g + 1]};
            *(LAS u32x2*)(lds + L_PHI + a * 8192 + row * 64 + (((4 + 2 * g + h2) ^ sw) * 8)) = (u32x2){neg[2 * g], neg[2 * g + 1]};
        }
    }
    if (w >= 4) { if (b < 86 && lw < 2) waitv<16>(); else waitv<0>(); }
    BAR();
    STAMP(5);
    float yq[8];
    if (w < 4) {
        unsigned tq = L_WQ + trn + blk * 32;
        asm volatile("" : "+v"(tq));
        f32x16 qa = {};
        u32x4 qb[2][4];
#pragma unroll
        for (int s = 0; s < 4; ++s) qb[0][s] = tr2(lds, tq + s * 1024, 256);
        SB();
#pragma unroll
        for (int s = 0; s < 32; ++s) {
            qa = mfma(qb[(s >> 2) & 1][s & 3], xf[s], qa);
            if (s < 28) qb[((s >> 2) + 1) & 1][s & 3] = tr2(lds, tq + (s + 4) * 1024, 256);
            SB();
        }
#pragma unroll
        for (int g = 0; g < 2; ++g) *(LAS f32x4*)(lds + L_XCH + m * 4096 + g * 1024 + lane * 16) = (f32x4){qa[8 + 4 * g], qa[9 + 4 * g], qa[10 + 4 * g], qa[11 + 4 * g]};
        STAMP(6);
        BAR();
#pragma unroll
        for (int g = 0; g < 2; ++g) {
            const f32x4 c0 = *(LAS f32x4*)(lds + L_CSQ + (8 * g + 4 * h2) * 4);
#pragma unroll
            for (int e = 0; e < 4; ++e) yq[4 * g + e] = rstd1 * __builtin_fmaf(-mean1, c0[e], qa[4 * g + e]);
        }
    } else {
        if (lw >= 2) {
            const int ah = lw - 2;
            f32x16 kv = {};
            unsigned aa[2], ab[2];
#pragma unroll
            for (int u = 0; u < 2; ++u) {
                const unsigned R = 8 * h2 + 4 * u + q, sw = (R >> 1) & 7;
                aa[u] = L_PHI + ah * 8192 + R * 64 + (((4 * blk + pp) ^ sw) * 8);
                ab[u] = L_VIMG + R * 64 + (((4 * (ah ^ blk) + pp) ^ sw) * 8);
                asm volatile("" : "+v"(aa[u]), "+v"(ab[u]));
            }
            u32x4 A[8], B[8];
#pragma unroll
            for (int s = 0; s < 8; ++s) {
                const s16x4 a0 = __builtin_amdgcn_ds_read_tr16_b64_v4i16((LAS s16x4*)(lds + aa[0] + s * 1024));
                const s16x4 a1 = __builtin_amdgcn_ds_read_tr16_b64_v4i16((LAS s16x4*)(lds + aa[1] + s * 1024));
                const s16x4 b0 = __builtin_amdgcn_ds_read_tr16_b64_v4i16((LAS s16x4*)(lds + ab[0] + s * 1024));
                const s16x4 b1 = __builtin_amdgcn_ds_read_tr16_b64_v4i16((LAS s16x4*)(lds + ab[1] + s * 1024));
                A[s] = __builtin_bit_cast(u32x4, __builtin_shufflevector(a0, a1, 0, 1, 2, 3, 4, 5, 6, 7));
                B[s] = __builtin_bit_cast(u32x4, __builtin_shufflevector(b0, b1, 0, 1, 2, 3, 4, 5, 6, 7));
                if (s == 3) PIN();
            }
            PIN();
#pragma unroll
            for (int s = 0; s < 8; ++s) {
#pragma unroll
                for (int e = 0; e < 4; ++e) B[s][e] = blk ? 0x3F803F80u : B[s][e];
                kv = mfma(A[s], B[s], kv);
            }
            if (t <= 16) {
                const unsigned base = WS_KVP + (j * 16 + p) * KVP_BYTES + ((ah * 4 + h2) * 17 + t) * 16;
                st16(rsW, (u32x4){pack2(kv[0], kv[1]), pack2(kv[2], kv[3]), pack2(kv[4], kv[5]), pack2(kv[6], kv[7])}, base, 0);
                st16(rsW, (u32x4){pack2(kv[8], kv[9]), pack2(kv[10], kv[11]), pack2(kv[12], kv[13]), pack2(kv[14], kv[15])}, base + 34 * 16, 0);
            }
            WAITV(0);
            arrive_publish(lds, 0, 2, ws, WS_F1 + (j * 16 + p) * 256, tag, lane);
            STAMP(6);
        } else {
            unsigned tq = L_WQ + lw * 32 + (8 * (lane >> 4) + q) * 64 + pp * 8;
            asm volatile("" : "+v"(tq));
            f32x4 qcs = {};
#pragma unroll
            for (int g = 0; g < 4; ++g) {
                u32x4 f[4];
#pragma unroll
                for (int s = 0; s < 4; ++s) f[s] = tr2(lds, tq + (4 * g + s) * 2048, 256);
                PIN();
#pragma unroll
                for (int s = 0; s < 4; ++s) qcs = mfma16(f[s], ones, qcs);
            }
            if ((lane & 15) == 0) *(LAS f32x4*)(lds + L_CSQ + lw * 64 + (lane >> 4) * 16) = qcs;
            if (b < 86) {
                waitv<0>();
                unsigned tc = lw * 49152 + (8 * (lane >> 4) + q) * 64 + pp * 8;
                asm volatile("" : "+v"(tc));
                f32x4 c0 = {}, c1 = {};
#pragma unroll
                for (int g = 0; g < 4; ++g) {
                    u32x4 f[4];
#pragma unroll
                    for (int s = 0; s < 2; ++s) { f[2 * s] = tr2(lds, tc + (2 * g + s) * 2048, 256); f[2 * s + 1] = tr2(lds, tc + (2 * g + s) * 2048 + 32, 256); }
                    PIN();
#pragma unroll
                    for (int s = 0; s < 2; ++s) { c0 = mfma16(f[2 * s], ones, c0); c1 = mfma16(f[2 * s + 1], ones, c1); }
                }
                if ((lane & 15) == 0) {
                    const unsigned o = WS_CSG + (((b >> 1) * 2 + lw) * 64 + (b & 1) * 32 + (lane >> 4) * 4) * 4;
                    st16(rsW, __builtin_bit_cast(u32x4, c0), o, 0);
                    st16(rsW, __builtin_bit_cast(u32x4, c1), o + 64, 0);
                }
            }
            STAMP(6);
        }
        BAR();
        {
            const rsrc_t rsO = __builtin_amdgcn_make_buffer_rsrc((void*)wo, 0, 512 * 512 * 2, 0x00020000);
#pragma unroll 4
            for (int i = 0; i < 16; ++i) dma16(rsO, lb + L_X + (2 * i + (lw >> 1)) * 2048 + (lw & 1) * 1024, vrow, (16 * (16 * (lw >> 1) + ((i + r2) & 15)) + 8 * (lw & 1)) * 1024 + 128 * c);
        }
#pragma unroll
        for (int g = 0; g < 2; ++g) {
            const f32x4 c0 = *(LAS f32x4*)(lds + L_CSQ + (16 + 8 * g + 4 * h2) * 4);
            const f32x4 v = *(LAS f32x4*)(lds + L_XCH + m * 4096 + g * 1024 + lane * 16);
#pragma unroll
            for (int e = 0; e < 4; ++e) yq[4 * g + e] = rstd1 * __builtin_fmaf(-mean1, c0[e], v[e]);
        }
    }
    STAMP(7);
    u32x4 qpos, qneg;
    {
        const u32x4 qf = {pack2(yq[0], yq[1]), pack2(yq[2], yq[3]), pack2(yq[4], yq[5]), pack2(yq[6], yq[7])};
        const f32x16 zero = {};
        const f32x16 pq = mfma(aphi, qf, zero);
        dual_softmax(pq, qpos, qneg);
    }
    STAMP(8);
    if (w == 0) poll_flags(ws, WS_F1 + j * 16 * 256, 16, tag, lane);
    BAR();
    STAMP(9);
    {
        const unsigned kb = WS_KVP + j * 16 * KVP_BYTES;
        float lo = 0.f, hi = 0.f;
        unsigned v[16], z[8];
#pragma unroll
        for (int i = 0; i < 16; ++i) v[i] = ld4(rsW, tid * 4, kb + ((i + 4 * q4) & 15) * KVP_BYTES);
        if (w == 0) {
#pragma unroll
            for (int i = 0; i < 8; ++i) z[i] = ld4(rsW, (512 + t) * 4 + h2 * 8 * KVP_BYTES, kb + i * KVP_BYTES);
        }
        PIN();
#pragma unroll
        for (int i = 0; i < 16; ++i) { lo += blo(v[i]); hi += bhi(v[i]); }
        if (w == 0) {
            float zl = 0.f, zh = 0.f;
#pragma unroll
            for (int i = 0; i < 8; ++i) { zl += blo(z[i]); zh += bhi(z[i]); }
            zl = halves_add(zl); zh = halves_add(zh);
            if (h2 == 0) *(LAS unsigned*)(lds + L_KVA + (512 + t) * 4) = pack2(zl, zh);
        }
        *(LAS unsigned*)(lds + L_KVA + tid * 4) = pack2(lo, hi);
    }
    BAR();
    STAMP(10);
    const int npass = has3 ? 3 : 2;
    const int m2 = w & 1, kh = (w >> 1) & 1;
    const int T2 = 4 * pP + 2 * half + m2;
    const unsigned C0 = 64 * c + 32 * kh;
    {
        const unsigned col = t < 16 ? t : 16;
        const u32x4 kp = *(LAS u32x4*)(lds + L_KVA + ((a * 4 + h2) * 17 + col) * 16);
        const u32x4 kn = *(LAS u32x4*)(lds + L_KVA + ((a * 4 + 2 + h2) * 17 + col) * 16);
        f32x16 o = {};
        o = mfma(kp, qpos, o);
        o = mfma(kn, qneg, o);
        const float r = __builtin_amdgcn_rcpf(o[8] + 1e-6f);
        st16(rsW, (u32x4){pack2(o[0] * r, o[1] * r), pack2(o[2] * r, o[3] * r), pack2(o[4] * r, o[5] * r), pack2(o[6] * r, o[7] * r)},
             WS_O + ((4 * p + m) * 32 + 2 * j + a) * 1024 + lane * 16, 0);
        WAITV(0);
        BAR();
        if (tid == 0) __hip_atomic_store((GAS u64*)(ws + WS_F2 + (p * 16 + j) * 256), tag, __ATOMIC_RELAXED, __HIP_MEMORY_SCOPE_AGENT);
        if (tid == 64 && b < 86) __hip_atomic_store((GAS u64*)(ws + WS_F5 + b * 256), tag, __ATOMIC_RELAXED, __HIP_MEMORY_SCOPE_AGENT);
        SBAR();
    }
    const unsigned trp = (4 * h2 + q) * 64 + blk * 32 + pp * 8;
    const unsigned t128 = (4 * h2 + q) * 128 + (q >> 1) * 64 + blk * 32 + pp * 8;

    if (w >= 4) {
        ISSUE_STAGE(0, L_Y);
        STAMP(11);
    }
    if (w >= 4) {
        const rsrc_t rsD = __builtin_amdgcn_make_buffer_rsrc((void*)wd, 0, 1376 * 512 * 2, 0x00020000);
#define ISSUE_WD(pi, halfbase) do { const int n_ = (pi) == 2 ? 11 : 16; const int k0_ = (pi) == 2 ? 64 + (lw >> 1) * 11 : 32 * (pi) + (lw >> 1) * 16; \
            _Pragma("unroll 1") for (int i_ = 0; i_ < n_; ++i_) { const int ii_ = (pi) == 2 ? (i_ + r4 >= 11 ? i_ + r4 - 11 : i_ + r4) : ((i_ + r2) & 15); \
                dma16(rsD, lb + (halfbase) + (2 * i_ + (lw >> 1)) * 2048 + (lw & 1) * 1024, vrow, (16 * (k0_ + ii_) + 8 * (lw & 1)) * 1024 + 128 * c); } } while (0)
        BAR();
        STAMP(12);
        WAITV(0);
        BAR();
        BAR();
        SBAR();
        ISSUE_STAGE(1, L_X);
        STAMP(13);
        BAR();
        WAITV(0);
        BAR();
        SBAR();
        if (npass == 3) {
            ISSUE_UNIT(u3, L_Y);
            BAR();
            WAITV(0);
            SBAR();
            ISSUE_WD(0, L_X);
            BAR();
            WAITV(0);
            BAR();
            SBAR();
            ISSUE_WD(1, L_Y);
            WAITV(0);
            BAR();
            ISSUE_WD(2, L_X);
            WAITV(0);
            BAR();
        } else {
            ISSUE_WD(0, L_Y);
            BAR();
            SBAR();
            ISSUE_WD(1, L_X);
            WAITV(0);
            BAR();
            SBAR();
            BAR();
            ISSUE_WD(2, L_Y);
            WAITV(0);
            BAR();
        }
        BAR();
    } else {
        const unsigned vo = (32 * T2 + t) * 1024 + (C0 + 8 * h2) * 2;
        u32x4 xr0 = __builtin_amdgcn_raw_buffer_load_b128(rsX, vo, 0, 0);
        u32x4 xr1 = __builtin_amdgcn_raw_buffer_load_b128(rsX, vo + 32, 0, 0);
        PIN();
        STAMP(11);
        if (w == 0) poll_flags(ws, WS_F2 + pP * 16 * 256, 16, tag, lane);
        BAR();
        STAMP(12);
        if (b == 0 && w == 1 && lane == 0) __hip_atomic_store((GAS u64*)ws, tag, __ATOMIC_RELAXED, __HIP_MEMORY_SCOPE_AGENT);
        {
            u32x4 hres0, hres1;
            u32x4 of[16];
#pragma unroll
            for (int i = 0; i < 8; ++i) of[i] = ld16(rsW, lane * 16, WS_O + (T2 * 32 + 16 * kh + ((i + r2) & 15)) * 1024);
            PIN();
            f32x16 a0 = {}, a1 = {};
            gemm2<16, 4096, 1024, 0>(lds, L_X + kh * 2048 + t128, L_X + kh * 2048 + (t128 ^ 64), of, a0, a1, [&](int g) {
                if (g < 4) {
#pragma unroll
                    for (int e = 0; e < 2; ++e) of[8 + 2 * g + e] = ld16(rsW, lane * 16, WS_O + (T2 * 32 + 16 * kh + ((8 + 2 * g + e + r2) & 15)) * 1024);
                    SB();
                } });
            const f32x16 snd = kh ? a0 : a1;
            f32x16 keep = kh ? a1 : a0;
#pragma unroll
            for (int g = 0; g < 4; ++g) *(LAS f32x4*)(lds + L_XCH + w * 4096 + g * 1024 + lane * 16) = (f32x4){snd[4 * g], snd[4 * g + 1], snd[4 * g + 2], snd[4 * g + 3]};
            STAMP(13);
            BAR();
#pragma unroll
            for (int g = 0; g < 4; ++g) {
                const f32x4 v = *(LAS f32x4*)(lds + L_XCH + (w ^ 2) * 4096 + g * 1024 + lane * 16);
#pragma unroll
                for (int e = 0; e < 4; ++e) keep[4 * g + e] += v[e];
            }
            { auto r = __builtin_amdgcn_permlane32_swap(xr0[0], xr0[2], false, false); xr0[0] = r[0]; xr0[2] = r[1];
              r = __builtin_amdgcn_permlane32_swap(xr0[1], xr0[3], false, false); xr0[1] = r[0]; xr0[3] = r[1];
              r = __builtin_amdgcn_permlane32_swap(xr1[0], xr1[2], false, false); xr1[0] = r[0]; xr1[2] = r[1];
              r = __builtin_amdgcn_permlane32_swap(xr1[1], xr1[3], false, false); xr1[1] = r[0]; xr1[3] = r[1]; }
#pragma unroll
            for (int e = 0; e < 4; ++e) {
                hres0[e] = pack2(keep[2 * e] + blo(xr0[e]), keep[2 * e + 1] + bhi(xr0[e]));
                hres1[e] = pack2(keep[8 + 2 * e] + blo(xr1[e]), keep[8 + 2 * e + 1] + bhi(xr1[e]));
            }
            const unsigned hb = WS_H + (T2 * 32 + 4 * c + 2 * kh) * 1024;
            st16(rsW, hres0, lane * 16, hb);
            st16(rsW, hres1, lane * 16, hb + 1024);
            *(LAS u32x4*)(lds + L_HRES + w * 2048 + lane * 16) = hres0;
            *(LAS u32x4*)(lds + L_HRES + w * 2048 + 1024 + lane * 16) = hres1;
            float su = 0.f, sq = 0.f;
#pragma unroll
            for (int e = 0; e < 4; ++e) {
                const float v0 = blo(hres0[e]), v1 = bhi(hres0[e]), v2 = blo(hres1[e]), v3 = bhi(hres1[e]);
                su += (v0 + v1) + (v2 + v3);
                sq = __builtin_fmaf(v0, v0, __builtin_fmaf(v1, v1, __builtin_fmaf(v2, v2, __builtin_fmaf(v3, v3, sq))));
            }
            su = halves_add(su); sq = halves_add(sq);
            __builtin_amdgcn_raw_buffer_store_b32(asu(h2 ? sq : su), rsW, lane * 4 + WS_HST + (T2 * 16 + 2 * c + kh) * 256, 0, 16);
            WAITV(0);
            BAR();
            if (tid == 0) __hip_atomic_store((GAS u64*)(ws + WS_F3 + (pP * 16 + jP) * 256), tag, __ATOMIC_RELAXED, __HIP_MEMORY_SCOPE_AGENT);
            SBAR();
        }
        STAMP(14);
        if (w == 0) poll_flags(ws, WS_F3 + p * 16 * 256, 16, tag, lane);
        if (w == 1) {
            const int un = (lane >> 1) == 2 && npass == 3 ? u3 : 2 * j + ((lane >> 1) & 1);
            const GAS u64* f = (const GAS u64*)(ws + WS_F5) + (2 * un + (lane & 1)) * 32;
#pragma unroll 1
            for (unsigned spin = 0; spin < SPIN_CAP; ++spin) {
                const u64 v = __hip_atomic_load(f, __ATOMIC_RELAXED, __HIP_MEMORY_SCOPE_AGENT);
                if (__all(v == tag)) break;
                __builtin_amdgcn_s_sleep(1);
            }
            asm volatile("" ::: "memory");
            unsigned cq[3][2];
#pragma unroll
            for (int pi = 0; pi < 3; ++pi)
#pragma unroll
                for (int k = 0; k < 2; ++k) cq[pi][k] = ld4(rsW, lane * 4, WS_CSG + ((pi < 2 ? 2 * j + pi : (npass == 3 ? u3 : 0)) * 2 + k) * 256);
            PIN();
#pragma unroll
            for (int pi = 0; pi < 3; ++pi) *(LAS float*)(lds + L_CS3 + (pi * 64 + lane) * 4) = asf(cq[pi][0]) + asf(cq[pi][1]);
        }
        BAR();
        STAMP(15);
        {
            const int T = 4 * p + w;
            u32x4 hf[32];
            float sv[16];
#pragma unroll
            for (int i = 0; i < 16; ++i) sv[i] = asf(ld4(rsW, lane * 4, WS_HST + (T * 16 + i) * 256));
#pragma unroll
            for (int s = 0; s < 8; ++s) hf[s] = ld16(rsW, lane * 16, WS_H + (T * 32 + ((s + r3) & 31)) * 1024);
            PIN();
            float mean, r2;
            auto act_out = [&](const f32x16& ag, const f32x16& au, int cs, unsigned ks) {
                float y[16];
#pragma unroll
                for (int g = 0; g < 4; ++g) {
                    const f32x4 cg = *(LAS f32x4*)(lds + L_CS3 + (cs * 64 + 8 * g + 4 * h2) * 4), cu = *(LAS f32x4*)(lds + L_CS3 + (cs * 64 + 32 + 8 * g + 4 * h2) * 4);
#pragma unroll
                    for (int e = 0; e < 4; ++e) y[4 * g + e] = (__builtin_fmaf(-mean, cg[e], ag[4 * g + e]) * r2) * __builtin_fmaf(-mean, cu[e], au[4 * g + e]);
                }
                const unsigned ab = WS_ACT + (T * 86 + ks) * 1024;
                st16(rsW, (u32x4){pack2(y[0], y[1]), pack2(y[2], y[3]), pack2(y[4], y[5]), pack2(y[6], y[7])}, lane * 16, ab);
                st16(rsW, (u32x4){pack2(y[8], y[9]), pack2(y[10], y[11]), pack2(y[12], y[13]), pack2(y[14], y[15])}, lane * 16, ab + 1024);
            };
            {
                f32x16 g0 = {}, g1 = {}, u0 = {}, u1 = {};
                gemm4<0>(lds, L_Y + t128, L_Y + (t128 ^ 64), hf, g0, g1, u0, u1, [&](int i) {
                    hf[8 + i] = ld16(rsW, lane * 16, WS_H + (T * 32 + ((8 + i + r3) & 31)) * 1024);
                    if (i == 1) {
                        float tot = 0.f;
#pragma unroll
                        for (int k = 0; k < 16; ++k) tot += sv[k];
                        const u32x2 r = both_halves(tot);
                        mean = asf(r[0]) * (1.0f / 512.0f);
                        const float var = fmaxf(asf(r[1]) * (1.0f / 512.0f) - mean * mean, 0.f);
                        r2 = __builtin_amdgcn_rcpf(var + 1e-5f);
                        asm volatile("" : "+v"(mean), "+v"(r2));
                    }
                    SB(); });
                BAR();
                STAMP(16);
                gemm4<16>(lds, L_X + t128, L_X + (t128 ^ 64), hf, g0, g1, u0, u1, [&](int i) {
                    if (i < 8) { hf[24 + i] = ld16(rsW, lane * 16, WS_H + (T * 32 + ((24 + i + r3) & 31)) * 1024); SB(); }
                    if (i == 7) { SBAR(); SB(); } });
                BAR();
                STAMP(17);
                act_out(g0, u0, 0, 4 * j);
                act_out(g1, u1, 1, 4 * j + 2);
                SBAR();
            }
            if (npass == 3) {
                f32x16 ag = {}, au = {};
                gemm2<32, 2048, 512, 0>(lds, L_Y + trp, L_Y + 1024 + trp, hf, ag, au, [&](int g) {
                    if (g == 5) { WAITV(0); arrive_publish(lds, 3, 4, ws, WS_F4 + (p * 16 + j) * 256, tag, lane); PIN(); } });
                BAR();
                STAMP(18);
                act_out(ag, au, 2, 2 * u3);
                WAITV(0); arrive_publish(lds, 4, 4, ws, WS_F4 + 0x10000 + (p * 16 + j) * 256, tag, lane);
            } else {
                WAITV(0); arrive_publish(lds, 3, 4, ws, WS_F4 + (p * 16 + j) * 256, tag, lane);
            }
        }
        STAMP(19);
        {
            f32x16 a0 = {}, a1 = {};
            const unsigned h0 = npass == 3 ? L_X : L_Y, h1 = npass == 3 ? L_Y : L_X;
            if (w == 0) poll_flags(ws, WS_F4 + pP * 16 * 256, 16, tag, lane);
            BAR();
            STAMP(20);
            u32x4 af[43];
            auto ld_act = [&](int i) {
                const int ks = i < 32 ? 32 * (i >> 4) + kh * 16 + ((i + r2) & 15) : 64 + kh * 11 + (i - 32 + r4 >= 11 ? i - 32 + r4 - 11 : i - 32 + r4);
                af[i] = ld16(rsW, lane * 16, WS_ACT + (T2 * 86 + ks) * 1024); };
#pragma unroll
            for (int i = 0; i < 8; ++i) ld_act(i);
            SBAR();
            gemm2<16, 4096, 1024, 0>(lds, h0 + kh * 2048 + t128, h0 + kh * 2048 + (t128 ^ 64), af, a0, a1, [&](int g) { ld_act(8 + 2 * g); ld_act(9 + 2 * g); SB(); });
            STAMP(21);
            if (w == 0) poll_flags3(ws, WS_F4 + 0x10000 + pP * 16 * 256, tag, lane);
            BAR();
            STAMP(22);
            gemm2<16, 4096, 1024, 16>(lds, h1 + kh * 2048 + t128, h1 + kh * 2048 + (t128 ^ 64), af, a0, a1, [&](int g) { ld_act(24 + 2 * g); ld_act(25 + 2 * g); SB(); });
            BAR();
            STAMP(23);
            gemm2<11, 4096, 1024, 32>(lds, h0 + kh * 2048 + t128, h0 + kh * 2048 + (t128 ^ 64), af, a0, a1, [&](int g) { if (g < 2) { ld_act(40 + 2 * g < 43 ? 40 + 2 * g : 42); if (g == 0) ld_act(41); SB(); } });
            const f32x16 snd = kh ? a0 : a1;
            f32x16 keep = kh ? a1 : a0;
#pragma unroll
            for (int g = 0; g < 4; ++g) *(LAS f32x4*)(lds + L_XCH + w * 4096 + g * 1024 + lane * 16) = (f32x4){snd[4 * g], snd[4 * g + 1], snd[4 * g + 2], snd[4 * g + 3]};
            BAR();
            STAMP(24);
#pragma unroll
            for (int g = 0; g < 4; ++g) {
                const f32x4 v = *(LAS f32x4*)(lds + L_XCH + (w ^ 2) * 4096 + g * 1024 + lane * 16);
#pragma unroll
                for (int e = 0; e < 4; ++e) keep[4 * g + e] += v[e];
            }
            const u32x4 hres0 = *(LAS u32x4*)(lds + L_HRES + w * 2048 + lane * 16), hres1 = *(LAS u32x4*)(lds + L_HRES + w * 2048 + 1024 + lane * 16);
            u32x4 o0, o1;
#pragma unroll
            for (int e = 0; e < 4; ++e) {
                o0[e] = pack2(keep[2 * e] + blo(hres0[e]), keep[2 * e + 1] + bhi(hres0[e]));
                o1[e] = pack2(keep[8 + 2 * e] + blo(hres1[e]), keep[8 + 2 * e + 1] + bhi(hres1[e]));
            }
            { auto r = __builtin_amdgcn_permlane32_swap(o0[0], o0[2], false, false); o0[0] = r[0]; o0[2] = r[1];
              r = __builtin_amdgcn_permlane32_swap(o0[1], o0[3], false, false); o0[1] = r[0]; o0[3] = r[1];
              r = __builtin_amdgcn_permlane32_swap(o1[0], o1[2], false, false); o1[0] = r[0]; o1[2] = r[1];
              r = __builtin_amdgcn_permlane32_swap(o1[1], o1[3], false, false); o1[1] = r[0]; o1[3] = r[1]; }
            const rsrc_t rsOut = __builtin_amdgcn_make_buffer_rsrc((void*)out, 0, 2048 * 512 * 2, 0x00020000);
            int tz = threadIdx.x; asm volatile("" : "+v"(tz));
            const unsigned vz = (32 * T2 + (tz & 31)) * 1024 + (C0 + 8 * ((tz >> 5) & 1)) * 2;
            __builtin_amdgcn_raw_buffer_store_b128(o0, rsOut, vz, 0, 0);
            __builtin_amdgcn_raw_buffer_store_b128(o1, rsOut, vz + 32, 0, 0);
        }
        STAMP(25); CSTAMP(31);
    }
}

extern "C" void kernel_launch(void* const* d_in, const int* in_sizes, int n_in, void* d_out, int out_size, void* d_ws, size_t ws_size, hipStream_t stream) {
    (void)in_sizes; (void)n_in; (void)out_size; (void)ws_size;
    (void)hipFuncSetAttribute((const void*)hedgehog_block, hipFuncAttributeMaxDynamicSharedMemorySize, LDS_BYTES);
    hedgehog_block<<<256, 512, LDS_BYTES, stream>>>((const bf16_t*)d_in[0], (const bf16_t*)d_in[1], (const bf16_t*)d_in[2], (const bf16_t*)d_in[3],
                                                    (const bf16_t*)d_in[5], (const bf16_t*)d_in[6], (const bf16_t*)d_in[7], (bf16_t*)d_out, (char*)d_ws);
}
```

```cpp
#include <hip/hip_runtime.h>
#include <hip/hip_bf16.h>

#define LAS __attribute__((address_space(3)))
#define GAS __attribute__((address_space(1)))
#define DI __device__ __forceinline__
typedef unsigned short bf16_t;
typedef unsigned u32x4 __attribute__((ext_vector_type(4)));
typedef unsigned u32x2 __attribute__((ext_vector_type(2)));
typedef short s16x4 __attribute__((ext_vector_type(4)));
typedef __bf16 bf16x8 __attribute__((ext_vector_type(8)));
typedef __bf16 bf16x2 __attribute__((ext_vector_type(2)));
typedef float f32x16 __attribute__((ext_vector_type(16)));
typedef float f32x4 __attribute__((ext_vector_type(4)));
typedef float f32x2 __attribute__((ext_vector_type(2)));
typedef unsigned long long u64;
typedef __amdgpu_buffer_rsrc_t rsrc_t;

constexpr unsigned WS_F1 = 0x10000, WS_F2 = 0x20000, WS_F3 = 0x30000, WS_F4 = 0x40000, WS_F5 = 0x70000;
constexpr unsigned WS_KVP = 0x80000, KVP_BYTES = 2176;
constexpr unsigned WS_HST = 0x110000;
constexpr unsigned WS_XST = 0x160000, WS_CSG = 0x170000;
constexpr unsigned WS_O = 0x200000, WS_H = 0x400000, WS_ACT = 0x600000, WS_PROF = 0xC00000, WS_BYTES = 0x1000000;
constexpr unsigned L_X = 0, L_Y = 65536, L_XCH = 131072, L_MISC = 147456;
constexpr unsigned L_STAT = L_MISC, L_CS1 = L_MISC + 1024, L_CSQ = L_MISC + 1280, L_WPHI = L_MISC + 1536, L_KVA = L_MISC + 2560, L_CS3 = L_MISC + 4736, L_CNT = L_MISC + 5504, L_DUMMY = L_MISC + 6144, L_HRES = L_MISC + 8192;
constexpr unsigned L_WQ = 65536, L_PHI = 98304, L_VIMG = 114688, SS_BYTES = 24576;
constexpr unsigned LDS_BYTES = 163840;
constexpr unsigned SPIN_CAP = 40000;
constexpr float LOG2E = 1.4426950408889634f;

DI float asf(unsigned v) { return __builtin_bit_cast(float, v); }
DI unsigned asu(float v) { return __builtin_bit_cast(unsigned, v); }
DI unsigned pack2(float lo, float hi) { bf16x2 v = {(__bf16)lo, (__bf16)hi}; return __builtin_bit_cast(unsigned, v); }
DI float blo(unsigned v) { return __builtin_bit_cast(float, v << 16); }
DI float bhi(unsigned v) { return __builtin_bit_cast(float, v & 0xffff0000u); }
DI f32x16 mfma(u32x4 a, u32x4 b, f32x16 c) { return __builtin_amdgcn_mfma_f32_32x32x16_bf16(__builtin_bit_cast(bf16x8, a), __builtin_bit_cast(bf16x8, b), c, 0, 0, 0); }
DI f32x4 mfma16(u32x4 a, u32x4 b, f32x4 c) { return __builtin_amdgcn_mfma_f32_16x16x32_bf16(__builtin_bit_cast(bf16x8, a), __builtin_bit_cast(bf16x8, b), c, 0, 0, 0); }
DI u32x4 tr2(LAS char* lds, unsigned off, unsigned second) {
    const s16x4 lo = __builtin_amdgcn_ds_read_tr16_b64_v4i16((LAS s16x4*)(lds + off));
    const s16x4 hi = __builtin_amdgcn_ds_read_tr16_b64_v4i16((LAS s16x4*)(lds + off + second));
    return __builtin_bit_cast(u32x4, __builtin_shufflevector(lo, hi, 0, 1, 2, 3, 4, 5, 6, 7));
}
DI void dma16(const rsrc_t rs, unsigned ldsaddr, unsigned voff, unsigned soff) {
    asm volatile("s_mov_b32 m0, %0\n\ts_nop 0\n\tbuffer_load_dwordx4 %1, %2, %3 offen lds" ::"s"(ldsaddr), "v"(voff), "s"(rs), "s"(soff) : "memory");
}
#define WAITV(n) asm volatile("s_waitcnt vmcnt(" #n ")" ::: "memory")
#define PIN() do { asm volatile("" ::: "memory"); __builtin_amdgcn_sched_barrier(0); } while (0)
template <int N> DI void waitv() { asm volatile("s_waitcnt vmcnt(%0)" ::"n"(N) : "memory"); }
#define K0 4
#define STAMP2_SS(ss) do { if (ss == 0) STAMP2(0); if (ss == 1) STAMP2(1); if (ss == 2) STAMP2(2); if (ss == 3) STAMP2(3); if (ss == 4) STAMP2(4); if (ss == 5) STAMP2(5); if (ss == 6) STAMP2(6); if (ss == 7) STAMP2(7); } while (0)
#define SBAR() asm volatile("s_barrier" ::: "memory")
#define BAR() asm volatile("s_waitcnt lgkmcnt(0)\n\ts_barrier" ::: "memory")
DI u32x2 both_halves(float v) { const unsigned a = asu(v); const auto r = __builtin_amdgcn_permlane32_swap(a, a, false, false); return (u32x2){r[0], r[1]}; }
DI float halves_add(float v) { const u32x2 r = both_halves(v); return asf(r[0]) + asf(r[1]); }
DI float halves_max(float v) { const u32x2 r = both_halves(v); return fmaxf(asf(r[0]), asf(r[1])); }
DI float halves_min(float v) { const u32x2 r = both_halves(v); return fminf(asf(r[0]), asf(r[1])); }
DI float dpp_ror4(float v) { return asf(__builtin_amdgcn_update_dpp(0, (int)asu(v), 0x124, 0xf, 0xf, true)); }
DI float dpp_ror8(float v) { return asf(__builtin_amdgcn_update_dpp(0, (int)asu(v), 0x128, 0xf, 0xf, true)); }
DI float rows16_add(float v) { const unsigned a = asu(v); const auto r = __builtin_amdgcn_permlane16_swap(a, a, false, false); return asf(r[0]) + asf(r[1]); }
DI float stride4_add(float v) { v += dpp_ror4(v); v += dpp_ror8(v); v = rows16_add(v); return halves_add(v); }
DI float quad_add(float v) {
    v += __builtin_bit_cast(float, __builtin_amdgcn_update_dpp(0, __builtin_bit_cast(int, v), 0xB1, 0xf, 0xf, true));
    v += __builtin_bit_cast(float, __builtin_amdgcn_update_dpp(0, __builtin_bit_cast(int, v), 0x4E, 0xf, 0xf, true));
    return v;
}
DI void st16(const rsrc_t rs, u32x4 v, unsigned voff, unsigned soff) { __builtin_amdgcn_raw_buffer_store_b128(v, rs, voff + soff, 0, 16); }
DI u32x4 ld16(const rsrc_t rs, unsigned voff, unsigned soff) { return __builtin_amdgcn_raw_buffer_load_b128(rs, voff, soff, 16); }
DI unsigned ld4(const rsrc_t rs, unsigned voff, unsigned soff) { return __builtin_amdgcn_raw_buffer_load_b32(rs, voff, soff, 16); }

DI void poll_flags3(const char* ws, unsigned off, u64 tag, int lane) {
    const int l = lane < 11 ? lane : 10;
    const GAS u64* f = (const GAS u64*)(ws + off) + (l < 6 ? l : l + 2) * 32;
#pragma unroll 1
    for (unsigned spin = 0; spin < SPIN_CAP; ++spin) {
        const u64 v = __hip_atomic_load(f, __ATOMIC_RELAXED, __HIP_MEMORY_SCOPE_AGENT);
        if (__all(v == tag)) break;
        __builtin_amdgcn_s_sleep(1);
    }
    asm volatile("" ::: "memory");
}
DI void nohook(int) {}
DI void poll_flags(const char* ws, unsigned off, int n, u64 tag, int lane) {
    const GAS u64* f = (const GAS u64*)(ws + off) + (lane < n ? lane : n - 1) * 32;
#pragma unroll 1
    for (unsigned spin = 0; spin < SPIN_CAP; ++spin) {
        const u64 v = __hip_atomic_load(f, __ATOMIC_RELAXED, __HIP_MEMORY_SCOPE_AGENT);
        if (__all(v == tag)) break;
        __builtin_amdgcn_s_sleep(1);
    }
    asm volatile("" ::: "memory");
}
DI void arrive_publish(LAS char* lds, int cnt, unsigned n, char* ws, unsigned flag_off, u64 tag, int lane) {
    if (lane == 0) {
        const unsigned old = __hip_atomic_fetch_add((LAS unsigned*)(lds + L_CNT) + cnt, 1u, __ATOMIC_RELAXED, __HIP_MEMORY_SCOPE_WORKGROUP);
        if (old == n - 1) __hip_atomic_store((GAS u64*)(ws + flag_off), tag, __ATOMIC_RELAXED, __HIP_MEMORY_SCOPE_AGENT);
    }
}
DI void dual_softmax(const f32x16& p, u32x4& pos, u32x4& neg) {
    float mx = fmaxf(fmaxf(fmaxf(p[0], p[1]), fmaxf(p[2], p[3])), fmaxf(fmaxf(p[4], p[5]), fmaxf(p[6], p[7])));
    float mn = fminf(fminf(fminf(p[0], p[1]), fminf(p[2], p[3])), fminf(fminf(p[4], p[5]), fminf(p[6], p[7])));
    mx = halves_max(mx); mn = halves_min(mn);
    const float cx = -mx * LOG2E, cn = mn * LOG2E;
    float ep[8], en[8], sp = 0.f, sn = 0.f;
#pragma unroll
    for (int r = 0; r < 8; ++r) {
        ep[r] = __builtin_amdgcn_exp2f(__builtin_fmaf(p[r], LOG2E, cx));
        en[r] = __builtin_amdgcn_exp2f(__builtin_fmaf(p[r], -LOG2E, cn));
        sp += ep[r]; sn += en[r];
    }
    const float rp = __builtin_amdgcn_rcpf(halves_add(sp)), rn = __builtin_amdgcn_rcpf(halves_add(sn));
#pragma unroll
    for (int i = 0; i < 4; ++i) { pos[i] = pack2(ep[2 * i] * rp, ep[2 * i + 1] * rp); neg[i] = pack2(en[2 * i] * rn, en[2 * i + 1] * rn); }
}

#define SB() __builtin_amdgcn_sched_barrier(0)
template <int N, int STRIDE, int SECOND, int OFF, int NA, class Hook>
DI void gemm2(LAS char* lds, unsigned b0, unsigned b1, const u32x4 (&act)[NA], f32x16& acc0, f32x16& acc1, Hook hook) {
    u32x4 fb[2][4];
    asm volatile("" : "+v"(b0), "+v"(b1));
    constexpr int NG = (N + 1) / 2;
#pragma unroll
    for (int e = 0; e < 2; ++e) { fb[0][2 * e] = tr2(lds, b0 + e * STRIDE, SECOND); fb[0][2 * e + 1] = tr2(lds, b1 + e * STRIDE, SECOND); }
    SB();
#pragma unroll
    for (int g = 0; g < NG; ++g) {
#pragma unroll
        for (int e = 0; e < 2; ++e) {
            const int i = 2 * g + e, n = i + 2;
            if (i < N) acc0 = mfma(fb[g & 1][2 * e], act[OFF + i], acc0);
            if (n < N) fb[(g + 1) & 1][2 * e] = tr2(lds, b0 + n * STRIDE, SECOND);
            SB();
            if (i < N) acc1 = mfma(fb[g & 1][2 * e + 1], act[OFF + i], acc1);
            if (n < N) fb[(g + 1) & 1][2 * e + 1] = tr2(lds, b1 + n * STRIDE, SECOND);
            SB();
        }
        hook(g);
    }
}
template <int OFF, class Hook>
DI void gemm4(LAS char* lds, unsigned b0, unsigned b1, const u32x4 (&act)[32], f32x16& g0, f32x16& g1, f32x16& u0, f32x16& u1, Hook hook) {
    u32x4 fb[2][4];
    asm volatile("" : "+v"(b0), "+v"(b1));
    fb[0][0] = tr2(lds, b0, 1024); fb[0][1] = tr2(lds, b1, 1024); fb[0][2] = tr2(lds, b0 + 2048, 1024); fb[0][3] = tr2(lds, b1 + 2048, 1024);
    SB();
#pragma unroll
    for (int i = 0; i < 16; ++i) {
        const int o = (i + 1) * 4096;
        g0 = mfma(fb[i & 1][0], act[OFF + i], g0);
        if (i < 15) fb[(i + 1) & 1][0] = tr2(lds, b0 + o, 1024);
        SB();
        g1 = mfma(fb[i & 1][1], act[OFF + i], g1);
        if (i < 15) fb[(i + 1) & 1][1] = tr2(lds, b1 + o, 1024);
        SB();
        u0 = mfma(fb[i & 1][2], act[OFF + i], u0);
        if (i < 15) fb[(i + 1) & 1][2] = tr2(lds, b0 + o + 2048, 1024);
        SB();
        u1 = mfma(fb[i & 1][3], act[OFF + i], u1);
        if (i < 15) fb[(i + 1) & 1][3] = tr2(lds, b1 + o + 2048, 1024);
        SB();
        hook(i);
    }
}
DI void touch_lines(const rsrc_t rs, unsigned ldsaddr, unsigned voff, unsigned soff) {
    asm volatile("s_mov_b32 m0, %0\n\ts_nop 0\n\tbuffer_load_dword %1, %2, %3 offen lds" ::"s"(ldsaddr), "v"(voff), "s"(rs), "s"(soff) : "memory");
}

#define STAMP(i) do {} while (0)
#define CSTAMP(i) do {} while (0)
#define STAMP2(i) do {} while (0)

__global__ void __launch_bounds__(512, 2)
hedgehog_block(const bf16_t* __restrict__ x, const bf16_t* __restrict__ wqkv, const bf16_t* __restrict__ wphi, const bf16_t* __restrict__ wo,
               const bf16_t* __restrict__ wg, const bf16_t* __restrict__ wu, const bf16_t* __restrict__ wd, bf16_t* __restrict__ out, char* __restrict__ ws) {
    extern __shared__ __attribute__((aligned(16))) char smem[];
    LAS char* lds = (LAS char*)smem;
    const unsigned lb = (unsigned)(size_t)lds;
    const int tid = threadIdx.x, lane = tid & 63;
    const int w = __builtin_amdgcn_readfirstlane(tid >> 6);
    const int b = blockIdx.x, xcd = b & 7, slot = b >> 3;
    const int p = 4 * (xcd >> 1) + (slot >> 3), j = 8 * (xcd & 1) + (slot & 7);
    const int pP = 2 * xcd + (slot >> 4), jP = slot & 15, c = slot & 7, half = (slot >> 3) & 1;
    const bool has3 = (j & 7) < (j < 8 ? 6 : 5);
    const int u3 = 32 + (j < 8 ? j : j - 2);
    const int t = lane & 31, h2 = lane >> 5, q = (lane & 15) >> 2, pp = lane & 3, blk = (lane >> 4) & 1;
    const int m = w & 3, a = w >> 2, lw = w & 3;
    const int q4 = slot >> 3;
    const int r1 = (c + 2 * q4) & 7, r2 = (2 * c + 4 * q4) & 15, r3 = (4 * c + 8 * q4) & 31, r4 = (c + 3 * q4) % 11;
    STAMP(0); CSTAMP(30);
    const rsrc_t rsX = __builtin_amdgcn_make_buffer_rsrc((void*)x, 0, 2048 * 512 * 2, 0x00020000);
    const rsrc_t rsQ = __builtin_amdgcn_make_buffer_rsrc((void*)wqkv, 0, 512 * 1536 * 2, 0x00020000);
    const rsrc_t rsW = __builtin_amdgcn_make_buffer_rsrc((void*)ws, 0, WS_BYTES, 0x00020000);
    u64 epoch, tag = 0;
    asm volatile("global_load_dwordx2 %0, %1, off sc1" : "=v"(epoch) : "v"((const GAS u64*)ws) : "memory");
#define TAKE_TAG() do { const u64 e_ = ((u64)(unsigned)__builtin_amdgcn_readfirstlane((unsigned)(epoch >> 32)) << 32) | (unsigned)__builtin_amdgcn_readfirstlane((unsigned)epoch); \
                        tag = e_ + 0x9E3779B97F4A7C15ull; } while (0)
    const u32x4 ones = {0x3F803F80u, 0x3F803F80u, 0x3F803F80u, 0x3F803F80u};
    const unsigned c128 = ((lane & 7) ^ (4 * ((lane >> 4) & 1))) * 16;
    const unsigned vrow = (lane >> 3) * 1024 + c128;
    const unsigned trn = (8 * h2 + q) * 64 + pp * 8;

    const rsrc_t rsG = __builtin_amdgcn_make_buffer_rsrc((void*)wg, 0, 512 * 1376 * 2, 0x00020000);
    const rsrc_t rsU = __builtin_amdgcn_make_buffer_rsrc((void*)wu, 0, 512 * 1376 * 2, 0x00020000);
    const unsigned vgu = (lane >> 2) * 2752 + (lane & 3) * 16, vgu8 = (lane >> 3) * 2752 + c128;
#define ISSUE_STAGE(st, halfbase) do { _Pragma("unroll 4") for (int i_ = 0; i_ < 16; ++i_) { const unsigned so_ = (16 * ((16 * (st) + i_ + r3) & 31) + 8 * (lw & 1)) * 2752 + 128 * j; \
            if (lw & 2) dma16(rsU, lb + (halfbase) + i_ * 4096 + lw * 1024, vgu8, so_); else dma16(rsG, lb + (halfbase) + i_ * 4096 + lw * 1024, vgu8, so_); } } while (0)
#define ISSUE_UNIT(u, halfbase) do { _Pragma("unroll 4") for (int i_ = 0; i_ < 16; ++i_) { const int s_ = 2 * i_ + (lw >> 1); const unsigned so_ = 16 * ((s_ + r3) & 31) * 2752 + 64 * (u); \
            if (lw & 1) dma16(rsU, lb + (halfbase) + (2 * s_ + 1) * 1024, vgu, so_); else dma16(rsG, lb + (halfbase) + (2 * s_) * 1024, vgu, so_); } } while (0)
    u32x4 xf[32];
    float yk[16];
    float mean1, rstd1;
    if (w < 4) {
        {
            const unsigned R = 128 * p + 8 * j + 2 * w;
            const u32x4 v0 = __builtin_amdgcn_raw_buffer_load_b128(rsX, lane * 16, R * 1024, 0), v1 = __builtin_amdgcn_raw_buffer_load_b128(rsX, lane * 16, R * 1024 + 1024, 0);
            if (tid < 16) *(LAS unsigned*)(lds + L_CNT + tid * 4) = 0u;
            f32x2 a1 = {0.f, 0.f}, a2 = {0.f, 0.f}, b1 = {0.f, 0.f}, b2 = {0.f, 0.f};
#pragma unroll
            for (int e = 0; e < 4; ++e) {
                const f32x2 z0 = {blo(v0[e]), bhi(v0[e])}, z1 = {blo(v1[e]), bhi(v1[e])};
                a1 += z0; a2 += z0 * z0; b1 += z1; b2 += z1 * z1;
            }
            const float su0 = stride4_add(quad_add(a1[0] + a1[1])), sq0 = stride4_add(quad_add(a2[0] + a2[1]));
            const float su1 = stride4_add(quad_add(b1[0] + b1[1])), sq1 = stride4_add(quad_add(b2[0] + b2[1]));
            const float su = (lane & 1) ? su1 : su0, sq = (lane & 1) ? sq1 : sq0;
            const float mean = su * (1.0f / 512.0f), var = fmaxf(sq * (1.0f / 512.0f) - mean * mean, 0.f);
            const float rstd = __builtin_amdgcn_rsqf(var + 1e-5f);
            asm volatile("s_waitcnt vmcnt(0)" : "+v"(epoch)::"memory");
            TAKE_TAG();
            if (lane < 2) st16(rsW, (u32x4){asu(mean), asu(rstd), (unsigned)tag, (unsigned)(tag >> 32)}, (R + lane) * 16, WS_XST);
        }
        STAMP(1);
        const unsigned xa0 = (32 * m + t) * 128 + ((h2 ^ ((t >> 1) & 7)) * 16);
        const unsigned twA = 16384 + (blk ? 4096 + 32 : 0) + trn, twB = 16384 + (blk ? 4096 : 32) + trn;
        f32x16 accA = {}, accB = {};
        u32x4 fa[2][2], fb[2][2], xs;
        unsigned bA = twA, bB = twB, bx0 = xa0, bx1 = xa0 ^ 32, bx2 = xa0 ^ 64, bx3 = xa0 ^ 96;
        asm volatile("" : "+v"(bA), "+v"(bB), "+v"(bx0), "+v"(bx1), "+v"(bx2), "+v"(bx3));
#pragma unroll
        for (int ss = 0; ss <= 8; ++ss) {
            if (ss < 8) {
                BAR();
                STAMP2_SS(ss);
                if (ss == 0) STAMP(2);
                if (ss == 6) { xs = ld16(rsW, (128 * p + 32 * m + t) * 16, WS_XST); SB(); }
            }
#pragma unroll
            for (int e = 0; e < 2; ++e) {
                if (ss > 0) accA = mfma(fa[1][e], xf[4 * ss - 2 + e], accA);
                if (ss < 8) { xf[4 * ss + e] = *(LAS u32x4*)(lds + (e ? bx1 : bx0)); fa[0][e] = tr2(lds, bA + e * 1024, 256); }
                SB();
                if (ss > 0) accB = mfma(fb[1][e], xf[4 * ss - 2 + e], accB);
                if (ss < 8) fb[0][e] = tr2(lds, bB + e * 1024, 256);
                SB();
            }
            if (ss < 8) {
#pragma unroll
                for (int e = 0; e < 2; ++e) {
                    accA = mfma(fa[0][e], xf[4 * ss + e], accA);
                    xf[4 * ss + 2 + e] = *(LAS u32x4*)(lds + (e ? bx3 : bx2)); fa[1][e] = tr2(lds, bA + (2 + e) * 1024, 256);
                    SB();
                    accB = mfma(fb[0][e], xf[4 * ss + e], accB);
                    fb[1][e] = tr2(lds, bB + (2 + e) * 1024, 256);
                    SB();
                }
                if (ss < 7) {
                    const unsigned d = ss == 5 ? (unsigned)(-5 * (int)SS_BYTES) : SS_BYTES;
                    bA += d; bB += d; bx0 += d; bx1 += d; bx2 += d; bx3 += d;
                    asm volatile("" : "+v"(bA), "+v"(bB), "+v"(bx0), "+v"(bx1), "+v"(bx2), "+v"(bx3));
                }
            }
        }
        STAMP(3);
#pragma unroll
        for (int g = 0; g < 4; ++g) *(LAS f32x4*)(lds + L_XCH + m * 4096 + g * 1024 + lane * 16) = (f32x4){accB[4 * g], accB[4 * g + 1], accB[4 * g + 2], accB[4 * g + 3]};
#pragma unroll 1
        for (unsigned spin = 0; spin < SPIN_CAP; ++spin) {
            if (__all(xs[2] == (unsigned)tag && xs[3] == (unsigned)(tag >> 32))) break;
            __builtin_amdgcn_s_sleep(1);
            xs = ld16(rsW, (128 * p + 32 * m + t) * 16, WS_XST);
        }
        mean1 = asf(xs[0]); rstd1 = asf(xs[1]);
        if (h2 == 0) *(LAS f32x2*)(lds + L_STAT + (32 * m + t) * 8) = (f32x2){mean1, rstd1};
        BAR();
        STAMP(4);
#pragma unroll
        for (int g = 0; g < 4; ++g) {
            const f32x4 cs = *(LAS f32x4*)(lds + L_CS1 + (8 * g + 4 * h2) * 4);
#pragma unroll
            for (int e = 0; e < 4; ++e) yk[4 * g + e] = rstd1 * __builtin_fmaf(-mean1, cs[e], accA[4 * g + e]);
        }
    } else {
        const unsigned vx0 = (lane >> 3) * 1024 + (((lane & 7) ^ (lane >> 4)) * 16), vx1 = (lane >> 3) * 1024 + (((lane & 7) ^ (4 + (lane >> 4))) * 16);
        const unsigned vwk = (lane >> 2) * 3072 + (lane & 3) * 16, vwv = (lane >> 2) * 3072 + ((lane & 3) ^ 2) * 16;
#define ISSUE_SS(ss, v) do { const unsigned sb_ = lb + ((ss) % 6) * SS_BYTES, ks_ = ((ss) + r1) & 7; \
            const unsigned sx_ = (128 * p + 32 * (v)) * 1024 + ks_ * 128, sw_ = (16 * (v) + 64 * ks_) * 3072 + (512 + 32 * j) * 2; \
            dma16(rsX, sb_ + (4 * (v)) * 1024, vx0, sx_); \
            dma16(rsX, sb_ + (4 * (v) + 1) * 1024, vx1, sx_ + 8 * 1024); \
            dma16(rsQ, sb_ + 16384 + (v) * 1024, vwk, sw_); \
            dma16(rsQ, sb_ + 16384 + 4096 + (v) * 1024, vwv, sw_ + 1024); \
            dma16(rsX, sb_ + (4 * (v) + 2) * 1024, vx0, sx_ + 16 * 1024); \
            dma16(rsX, sb_ + (4 * (v) + 3) * 1024, vx1, sx_ + 24 * 1024); } while (0)
        if (lw == 0) {
            const rsrc_t rsP = __builtin_amdgcn_make_buffer_rsrc((void*)wphi, 0, 32 * 256 * 2, 0x00020000);
            dma16(rsP, lb + L_WPHI, lane * 16, j * 1024);
        }
#pragma unroll
        for (int ss = 0; ss < K0; ++ss) ISSUE_SS(ss, lw);
        STAMP(1);
        f32x4 csacc = {};
        const unsigned tw = 16384 + ((lw & 1) ? 4096 + ((lw >> 1) ^ 1) * 32 : (lw >> 1) * 32) + (8 * (lane >> 4) + q) * 64 + pp * 8;
        constexpr int NL = 8 - K0;
#define P1WAIT(s) (6 * ((((s) + K0 - 1 < 7 ? (s) + K0 - 1 : 7) - (s))) + ((s) > 4 ? 8 : 0))
#pragma unroll
        for (int ss = 0; ss < 8; ++ss) {
            if (ss == 0) { asm volatile("s_waitcnt vmcnt(%1)" : "+v"(epoch) : "n"(P1WAIT(0)) : "memory"); }
            if (ss == 1) waitv<P1WAIT(1)>();
            if (ss == 2) waitv<P1WAIT(2)>();
            if (ss == 3) waitv<P1WAIT(3)>();
            if (ss == 4) waitv<P1WAIT(4)>();
            if (ss == 5) waitv<P1WAIT(5)>();
            if (ss == 6) waitv<P1WAIT(6)>();
            if (ss == 7) waitv<P1WAIT(7)>();
            STAMP2_SS(ss);
            BAR();
            if (ss == 0) { TAKE_TAG(); STAMP(2); }
            unsigned bw = (ss % 6) * SS_BYTES + tw;
            asm volatile("" : "+v"(bw));
            const u32x4 f0 = tr2(lds, bw, 256), f1 = tr2(lds, bw + 2048, 256);
            SB();
            if (ss < NL) ISSUE_SS(K0 + ss, lw);
            if (ss == 4) {
#pragma unroll 4
                for (int i = 0; i < 8; ++i) dma16(rsQ, lb + L_WQ + (8 * lw + i) * 1024, vwk, (16 * ((8 * lw + i + 4 * r1) & 31)) * 3072 + 32 * j * 2);
            }
            if (ss == 7 && b < 86 && lw < 2) {
#pragma unroll 4
                for (int i = 0; i < 16; ++i) {
                    if (b & 1) dma16(rsU, lb + lw * 49152 + i * 1024, vgu, 16 * (16 * lw + i) * 2752 + 64 * (b >> 1));
                    else dma16(rsG, lb + lw * 49152 + i * 1024, vgu, 16 * (16 * lw + i) * 2752 + 64 * (b >> 1));
                }
            }
            csacc = mfma16(f0, ones, csacc);
            csacc = mfma16(f1, ones, csacc);
            SB();
        }
        if ((lane & 15) == 0) *(LAS f32x4*)(lds + L_CS1 + (lw >> 1) * 128 + (lw & 1) * 64 + (lane >> 4) * 16) = csacc;
        STAMP(3);
        BAR();
        STAMP(4);
        const f32x2 st = *(LAS f32x2*)(lds + L_STAT + (32 * m + t) * 8);
        mean1 = st[0]; rstd1 = st[1];
#pragma unroll
        for (int g = 0; g < 4; ++g) {
            const f32x4 cs = *(LAS f32x4*)(lds + L_CS1 + 128 + (8 * g + 4 * h2) * 4);
            const f32x4 v = *(LAS f32x4*)(lds + L_XCH + m * 4096 + g * 1024 + lane * 16);
#pragma unroll
            for (int e = 0; e < 4; ++e) yk[4 * g + e] = rstd1 * __builtin_fmaf(-mean1, cs[e], v[e]);
        }
    }
    const u32x4 aphi = tr2(lds, L_WPHI + a * 512 + (4 * h2 + q) * 32 + pp * 8, 256);
    {
        const u32x4 kf = {pack2(yk[0], yk[1]), pack2(yk[2], yk[3]), pack2(yk[4], yk[5]), pack2(yk[6], yk[7])};
        const f32x16 zero = {};
        const f32x16 pk = mfma(aphi, kf, zero);
        const unsigned sw = (t >> 1) & 7, row = 32 * m + t;
#pragma unroll
        for (int g = 0; g < 2; ++g)
            *(LAS u32x2*)(lds + L_VIMG + row * 64 + (((4 * a + 2 * g + h2) ^ sw) * 8)) = (u32x2){pack2(yk[8 + 4 * g], yk[9 + 4 * g]), pack2(yk[10 + 4 * g], yk[11 + 4 * g])};
        u32x4 pos, neg;
        dual_softmax(pk, pos, neg);
#pragma unroll
        for (int g = 0; g < 2; ++g) {
            *(LAS u32x2*)(lds + L_PHI + a * 8192 + row * 64 + (((2 * g + h2) ^ sw) * 8)) = (u32x2){pos[2 * g], pos[2 * g + 1]};
            *(LAS u32x2*)(lds + L_PHI + a * 8192 + row * 64 + (((4 + 2 * g + h2) ^ sw) * 8)) = (u32x2){neg[2 * g], neg[2 * g + 1]};
        }
    }
    if (w >= 4) { if (b < 86 && lw < 2) waitv<16>(); else waitv<0>(); }
    BAR();
    STAMP(5);
    float yq[8];
    if (w < 4) {
        unsigned tq = L_WQ + trn + blk * 32;
        asm volatile("" : "+v"(tq));
        f32x16 qa = {};
        u32x4 qb[2][4];
#pragma unroll
        for (int s = 0; s < 4; ++s) qb[0][s] = tr2(lds, tq + s * 1024, 256);
        SB();
#pragma unroll
        for (int s = 0; s < 32; ++s) {
            qa = mfma(qb[(s >> 2) & 1][s & 3], xf[s], qa);
            if (s < 28) qb[((s >> 2) + 1) & 1][s & 3] = tr2(lds, tq + (s + 4) * 1024, 256);
            SB();
        }
#pragma unroll
        for (int g = 0; g < 2; ++g) *(LAS f32x4*)(lds + L_XCH + m * 4096 + g * 1024 + lane * 16) = (f32x4){qa[8 + 4 * g], qa[9 + 4 * g], qa[10 + 4 * g], qa[11 + 4 * g]};
        STAMP(6);
        BAR();
#pragma unroll
        for (int g = 0; g < 2; ++g) {
            const f32x4 c0 = *(LAS f32x4*)(lds + L_CSQ + (8 * g + 4 * h2) * 4);
#pragma unroll
            for (int e = 0; e < 4; ++e) yq[4 * g + e] = rstd1 * __builtin_fmaf(-mean1, c0[e], qa[4 * g + e]);
        }
    } else {
        if (lw >= 2) {
            const int ah = lw - 2;
            f32x16 kv = {};
            unsigned aa[2], ab[2];
#pragma unroll
            for (int u = 0; u < 2; ++u) {
                const unsigned R = 8 * h2 + 4 * u + q, sw = (R >> 1) & 7;
                aa[u] = L_PHI + ah * 8192 + R * 64 + (((4 * blk + pp) ^ sw) * 8);
                ab[u] = L_VIMG + R * 64 + (((4 * (ah ^ blk) + pp) ^ sw) * 8);
                asm volatile("" : "+v"(aa[u]), "+v"(ab[u]));
            }
            u32x4 A[8], B[8];
#pragma unroll
            for (int s = 0; s < 8; ++s) {
                const s16x4 a0 = __builtin_amdgcn_ds_read_tr16_b64_v4i16((LAS s16x4*)(lds + aa[0] + s * 1024));
                const s16x4 a1 = __builtin_amdgcn_ds_read_tr16_b64_v4i16((LAS s16x4*)(lds + aa[1] + s * 1024));
                const s16x4 b0 = __builtin_amdgcn_ds_read_tr16_b64_v4i16((LAS s16x4*)(lds + ab[0] + s * 1024));
                const s16x4 b1 = __builtin_amdgcn_ds_read_tr16_b64_v4i16((LAS s16x4*)(lds + ab[1] + s * 1024));
                A[s] = __builtin_bit_cast(u32x4, __builtin_shufflevector(a0, a1, 0, 1, 2, 3, 4, 5, 6, 7));
                B[s] = __builtin_bit_cast(u32x4, __builtin_shufflevector(b0, b1, 0, 1, 2, 3, 4, 5, 6, 7));
                if (s == 3) PIN();
            }
            PIN();
#pragma unroll
            for (int s = 0; s < 8; ++s) {
#pragma unroll
                for (int e = 0; e < 4; ++e) B[s][e] = blk ? 0x3F803F80u : B[s][e];
                kv = mfma(A[s], B[s], kv);
            }
            if (t <= 16) {
                const unsigned base = WS_KVP + (j * 16 + p) * KVP_BYTES + ((ah * 4 + h2) * 17 + t) * 16;
                st16(rsW, (u32x4){pack2(kv[0], kv[1]), pack2(kv[2], kv[3]), pack2(kv[4], kv[5]), pack2(kv[6], kv[7])}, base, 0);
                st16(rsW, (u32x4){pack2(kv[8], kv[9]), pack2(kv[10], kv[11]), pack2(kv[12], kv[13]), pack2(kv[14], kv[15])}, base + 34 * 16, 0);
            }
            WAITV(0);
            arrive_publish(lds, 0, 2, ws, WS_F1 + (j * 16 + p) * 256, tag, lane);
            STAMP(6);
        } else {
            unsigned tq = L_WQ + lw * 32 + (8 * (lane >> 4) + q) * 64 + pp * 8;
            asm volatile("" : "+v"(tq));
            f32x4 qcs = {};
#pragma unroll
            for (int g = 0; g < 4; ++g) {
                u32x4 f[4];
#pragma unroll
                for (int s = 0; s < 4; ++s) f[s] = tr2(lds, tq + (4 * g + s) * 2048, 256);
                PIN();
#pragma unroll
                for (int s = 0; s < 4; ++s) qcs = mfma16(f[s], ones, qcs);
            }
            if ((lane & 15) == 0) *(LAS f32x4*)(lds + L_CSQ + lw * 64 + (lane >> 4) * 16) = qcs;
            if (b < 86) {
                waitv<0>();
                unsigned tc = lw * 49152 + (8 * (lane >> 4) + q) * 64 + pp * 8;
                asm volatile("" : "+v"(tc));
                f32x4 c0 = {}, c1 = {};
#pragma unroll
                for (int g = 0; g < 4; ++g) {
                    u32x4 f[4];
#pragma unroll
                    for (int s = 0; s < 2; ++s) { f[2 * s] = tr2(lds, tc + (2 * g + s) * 2048, 256); f[2 * s + 1] = tr2(lds, tc + (2 * g + s) * 2048 + 32, 256); }
                    PIN();
#pragma unroll
                    for (int s = 0; s < 2; ++s) { c0 = mfma16(f[2 * s], ones, c0); c1 = mfma16(f[2 * s + 1], ones, c1); }
                }
                if ((lane & 15) == 0) {
                    const unsigned o = WS_CSG + (((b >> 1) * 2 + lw) * 64 + (b & 1) * 32 + (lane >> 4) * 4) * 4;
                    st16(rsW, __builtin_bit_cast(u32x4, c0), o, 0);
                    st16(rsW, __builtin_bit_cast(u32x4, c1), o + 64, 0);
                }
            }
            STAMP(6);
        }
        BAR();
        {
            const rsrc_t rsO = __builtin_amdgcn_make_buffer_rsrc((void*)wo, 0, 512 * 512 * 2, 0x00020000);
#pragma unroll 4
            for (int i = 0; i < 16; ++i) dma16(rsO, lb + L_X + (2 * i + (lw >> 1)) * 2048 + (lw & 1) * 1024, vrow, (16 * (16 * (lw >> 1) + ((i + r2) & 15)) + 8 * (lw & 1)) * 1024 + 128 * c);
        }
#pragma unroll
        for (int g = 0; g < 2; ++g) {
            const f32x4 c0 = *(LAS f32x4*)(lds + L_CSQ + (16 + 8 * g + 4 * h2) * 4);
            const f32x4 v = *(LAS f32x4*)(lds + L_XCH + m * 4096 + g * 1024 + lane * 16);
#pragma unroll
            for (int e = 0; e < 4; ++e) yq[4 * g + e] = rstd1 * __builtin_fmaf(-mean1, c0[e], v[e]);
        }
    }
    STAMP(7);
    u32x4 qpos, qneg;
    {
        const u32x4 qf = {pack2(yq[0], yq[1]), pack2(yq[2], yq[3]), pack2(yq[4], yq[5]), pack2(yq[6], yq[7])};
        const f32x16 zero = {};
        const f32x16 pq = mfma(aphi, qf, zero);
        dual_softmax(pq, qpos, qneg);
    }
    STAMP(8);
    if (w == 0) poll_flags(ws, WS_F1 + j * 16 * 256, 16, tag, lane);
    BAR();
    STAMP(9);
    {
        const unsigned kb = WS_KVP + j * 16 * KVP_BYTES;
        float lo = 0.f, hi = 0.f;
        unsigned v[16], z[8];
#pragma unroll
        for (int i = 0; i < 16; ++i) v[i] = ld4(rsW, tid * 4, kb + ((i + 4 * q4) & 15) * KVP_BYTES);
        if (w == 0) {
#pragma unroll
            for (int i = 0; i < 8; ++i) z[i] = ld4(rsW, (512 + t) * 4 + h2 * 8 * KVP_BYTES, kb + i * KVP_BYTES);
        }
        PIN();
#pragma unroll
        for (int i = 0; i < 16; ++i) { lo += blo(v[i]); hi += bhi(v[i]); }
        if (w == 0) {
            float zl = 0.f, zh = 0.f;
#pragma unroll
            for (int i = 0; i < 8; ++i) { zl += blo(z[i]); zh += bhi(z[i]); }
            zl = halves_add(zl); zh = halves_add(zh);
            if (h2 == 0) *(LAS unsigned*)(lds + L_KVA + (512 + t) * 4) = pack2(zl, zh);
        }
        *(LAS unsigned*)(lds + L_KVA + tid * 4) = pack2(lo, hi);
    }
    BAR();
    STAMP(10);
    const int npass = has3 ? 3 : 2;
    const int m2 = w & 1, kh = (w >> 1) & 1;
    const int T2 = 4 * pP + 2 * half + m2;
    const unsigned C0 = 64 * c + 32 * kh;
    {
        const unsigned col = t < 16 ? t : 16;
        const u32x4 kp = *(LAS u32x4*)(lds + L_KVA + ((a * 4 + h2) * 17 + col) * 16);
        const u32x4 kn = *(LAS u32x4*)(lds + L_KVA + ((a * 4 + 2 + h2) * 17 + col) * 16);
        f32x16 o = {};
        o = mfma(kp, qpos, o);
        o = mfma(kn, qneg, o);
        const float r = __builtin_amdgcn_rcpf(o[8] + 1e-6f);
        st16(rsW, (u32x4){pack2(o[0] * r, o[1] * r), pack2(o[2] * r, o[3] * r), pack2(o[4] * r, o[5] * r), pack2(o[6] * r, o[7] * r)},
             WS_O + ((4 * p + m) * 32 + 2 * j + a) * 1024 + lane * 16, 0);
        WAITV(0);
        BAR();
        if (tid == 0) __hip_atomic_store((GAS u64*)(ws + WS_F2 + (p * 16 + j) * 256), tag, __ATOMIC_RELAXED, __HIP_MEMORY_SCOPE_AGENT);
        if (tid == 64 && b < 86) __hip_atomic_store((GAS u64*)(ws + WS_F5 + b * 256), tag, __ATOMIC_RELAXED, __HIP_MEMORY_SCOPE_AGENT);
        SBAR();
    }
    const unsigned trp = (4 * h2 + q) * 64 + blk * 32 + pp * 8;
    const unsigned t128 = (4 * h2 + q) * 128 + (q >> 1) * 64 + blk * 32 + pp * 8;

    if (w >= 4) {
        ISSUE_STAGE(0, L_Y);
        STAMP(11);
    }
    if (w >= 4) {
        const rsrc_t rsD = __builtin_amdgcn_make_buffer_rsrc((void*)wd, 0, 1376 * 512 * 2, 0x00020000);
#define ISSUE_WD(pi, halfbase) do { const int n_ = (pi) == 2 ? 11 : 16; const int k0_ = (pi) == 2 ? 64 + (lw >> 1) * 11 : 32 * (pi) + (lw >> 1) * 16; \
            _Pragma("unroll 1") for (int i_ = 0; i_ < n_; ++i_) { const int ii_ = (pi) == 2 ? (i_ + r4 >= 11 ? i_ + r4 - 11 : i_ + r4) : ((i_ + r2) & 15); \
                dma16(rsD, lb + (halfbase) + (2 * i_ + (lw >> 1)) * 2048 + (lw & 1) * 1024, vrow, (16 * (k0_ + ii_) + 8 * (lw & 1)) * 1024 + 128 * c); } } while (0)
        BAR();
        STAMP(12);
        WAITV(0);
        BAR();
        BAR();
        SBAR();
        ISSUE_STAGE(1, L_X);
        STAMP(13);
        BAR();
        WAITV(0);
        BAR();
        SBAR();
        if (npass == 3) {
            ISSUE_UNIT(u3, L_Y);
            BAR();
            WAITV(0);
            SBAR();
            ISSUE_WD(0, L_X);
            BAR();
            WAITV(0);
            BAR();
            SBAR();
            ISSUE_WD(1, L_Y);
            WAITV(0);
            BAR();
            ISSUE_WD(2, L_X);
            WAITV(0);
            BAR();
        } else {
            ISSUE_WD(0, L_Y);
            BAR();
            SBAR();
            ISSUE_WD(1, L_X);
            WAITV(0);
            BAR();
            SBAR();
            BAR();
            ISSUE_WD(2, L_Y);
            WAITV(0);
            BAR();
        }
        BAR();
    } else {
        const unsigned vo = (32 * T2 + t) * 1024 + (C0 + 8 * h2) * 2;
        u32x4 xr0 = __builtin_amdgcn_raw_buffer_load_b128(rsX, vo, 0, 0);
        u32x4 xr1 = __builtin_amdgcn_raw_buffer_load_b128(rsX, vo + 32, 0, 0);
        PIN();
        STAMP(11);
        if (w == 0) poll_flags(ws, WS_F2 + pP * 16 * 256, 16, tag, lane);
        BAR();
        STAMP(12);
        if (b == 0 && w == 1 && lane == 0) __hip_atomic_store((GAS u64*)ws, tag, __ATOMIC_RELAXED, __HIP_MEMORY_SCOPE_AGENT);
        {
            u32x4 hres0, hres1;
            u32x4 of[16];
#pragma unroll
            for (int i = 0; i < 8; ++i) of[i] = ld16(rsW, lane * 16, WS_O + (T2 * 32 + 16 * kh + ((i + r2) & 15)) * 1024);
            PIN();
            f32x16 a0 = {}, a1 = {};
            gemm2<16, 4096, 1024, 0>(lds, L_X + kh * 2048 + t128, L_X + kh * 2048 + (t128 ^ 64), of, a0, a1, [&](int g) {
                if (g < 4) {
#pragma unroll
                    for (int e = 0; e < 2; ++e) of[8 + 2 * g + e] = ld16(rsW, lane * 16, WS_O + (T2 * 32 + 16 * kh + ((8 + 2 * g + e + r2) & 15)) * 1024);
                    SB();
                } });
            const f32x16 snd = kh ? a0 : a1;
            f32x16 keep = kh ? a1 : a0;
#pragma unroll
            for (int g = 0; g < 4; ++g) *(LAS f32x4*)(lds + L_XCH + w * 4096 + g * 1024 + lane * 16) = (f32x4){snd[4 * g], snd[4 * g + 1], snd[4 * g + 2], snd[4 * g + 3]};
            STAMP(13);
            BAR();
#pragma unroll
            for (int g = 0; g < 4; ++g) {
                const f32x4 v = *(LAS f32x4*)(lds + L_XCH + (w ^ 2) * 4096 + g * 1024 + lane * 16);
#pragma unroll
                for (int e = 0; e < 4; ++e) keep[4 * g + e] += v[e];
            }
            { auto r = __builtin_amdgcn_permlane32_swap(xr0[0], xr0[2], false, false); xr0[0] = r[0]; xr0[2] = r[1];
              r = __builtin_amdgcn_permlane32_swap(xr0[1], xr0[3], false, false); xr0[1] = r[0]; xr0[3] = r[1];
              r = __builtin_amdgcn_permlane32_swap(xr1[0], xr1[2], false, false); xr1[0] = r[0]; xr1[2] = r[1];
              r = __builtin_amdgcn_permlane32_swap(xr1[1], xr1[3], false, false); xr1[1] = r[0]; xr1[3] = r[1]; }
#pragma unroll
            for (int e = 0; e < 4; ++e) {
                hres0[e] = pack2(keep[2 * e] + blo(xr0[e]), keep[2 * e + 1] + bhi(xr0[e]));
                hres1[e] = pack2(keep[8 + 2 * e] + blo(xr1[e]), keep[8 + 2 * e + 1] + bhi(xr1[e]));
            }
            const unsigned hb = WS_H + (T2 * 32 + 4 * c + 2 * kh) * 1024;
            st16(rsW, hres0, lane * 16, hb);
            st16(rsW, hres1, lane * 16, hb + 1024);
            *(LAS u32x4*)(lds + L_HRES + w * 2048 + lane * 16) = hres0;
            *(LAS u32x4*)(lds + L_HRES + w * 2048 + 1024 + lane * 16) = hres1;
            float su = 0.f, sq = 0.f;
#pragma unroll
            for (int e = 0; e < 4; ++e) {
                const float v0 = blo(hres0[e]), v1 = bhi(hres0[e]), v2 = blo(hres1[e]), v3 = bhi(hres1[e]);
                su += (v0 + v1) + (v2 + v3);
                sq = __builtin_fmaf(v0, v0, __builtin_fmaf(v1, v1, __builtin_fmaf(v2, v2, __builtin_fmaf(v3, v3, sq))));
            }
            su = halves_add(su); sq = halves_add(sq);
            __builtin_amdgcn_raw_buffer_store_b32(asu(h2 ? sq : su), rsW, lane * 4 + WS_HST + (T2 * 16 + 2 * c + kh) * 256, 0, 16);
            WAITV(0);
            BAR();
            if (tid == 0) __hip_atomic_store((GAS u64*)(ws + WS_F3 + (pP * 16 + jP) * 256), tag, __ATOMIC_RELAXED, __HIP_MEMORY_SCOPE_AGENT);
            SBAR();
        }
        STAMP(14);
        if (w == 0) poll_flags(ws, WS_F3 + p * 16 * 256, 16, tag, lane);
        if (w == 1) {
            const int un = (lane >> 1) == 2 && npass == 3 ? u3 : 2 * j + ((lane >> 1) & 1);
            const GAS u64* f = (const GAS u64*)(ws + WS_F5) + (2 * un + (lane & 1)) * 32;
#pragma unroll 1
            for (unsigned spin = 0; spin < SPIN_CAP; ++spin) {
                const u64 v = __hip_atomic_load(f, __ATOMIC_RELAXED, __HIP_MEMORY_SCOPE_AGENT);
                if (__all(v == tag)) break;
                __builtin_amdgcn_s_sleep(1);
            }
            asm volatile("" ::: "memory");
            unsigned cq[3][2];
#pragma unroll
            for (int pi = 0; pi < 3; ++pi)
#pragma unroll
                for (int k = 0; k < 2; ++k) cq[pi][k] = ld4(rsW, lane * 4, WS_CSG + ((pi < 2 ? 2 * j + pi : (npass == 3 ? u3 : 0)) * 2 + k) * 256);
            PIN();
#pragma unroll
            for (int pi = 0; pi < 3; ++pi) *(LAS float*)(lds + L_CS3 + (pi * 64 + lane) * 4) = asf(cq[pi][0]) + asf(cq[pi][1]);
        }
        BAR();
        STAMP(15);
        {
            const int T = 4 * p + w;
            u32x4 hf[32];
            float sv[16];
#pragma unroll
            for (int i = 0; i < 16; ++i) sv[i] = asf(ld4(rsW, lane * 4, WS_HST + (T * 16 + i) * 256));
#pragma unroll
            for (int s = 0; s < 8; ++s) hf[s] = ld16(rsW, lane * 16, WS_H + (T * 32 + ((s + r3) & 31)) * 1024);
            PIN();
            float mean, r2;
            auto act_out = [&](const f32x16& ag, const f32x16& au, int cs, unsigned ks) {
                float y[16];
#pragma unroll
                for (int g = 0; g < 4; ++g) {
                    const f32x4 cg = *(LAS f32x4*)(lds + L_CS3 + (cs * 64 + 8 * g + 4 * h2) * 4), cu = *(LAS f32x4*)(lds + L_CS3 + (cs * 64 + 32 + 8 * g + 4 * h2) * 4);
#pragma unroll
                    for (int e = 0; e < 4; ++e) y[4 * g + e] = (__builtin_fmaf(-mean, cg[e], ag[4 * g + e]) * r2) * __builtin_fmaf(-mean, cu[e], au[4 * g + e]);
                }
                const unsigned ab = WS_ACT + (T * 86 + ks) * 1024;
                st16(rsW, (u32x4){pack2(y[0], y[1]), pack2(y[2], y[3]), pack2(y[4], y[5]), pack2(y[6], y[7])}, lane * 16, ab);
                st16(rsW, (u32x4){pack2(y[8], y[9]), pack2(y[10], y[11]), pack2(y[12], y[13]), pack2(y[14], y[15])}, lane * 16, ab + 1024);
            };
            {
                f32x16 g0 = {}, g1 = {}, u0 = {}, u1 = {};
                gemm4<0>(lds, L_Y + t128, L_Y + (t128 ^ 64), hf, g0, g1, u0, u1, [&](int i) {
                    hf[8 + i] = ld16(rsW, lane * 16, WS_H + (T * 32 + ((8 + i + r3) & 31)) * 1024);
                    if (i == 1) {
                        float tot = 0.f;
#pragma unroll
                        for (int k = 0; k < 16; ++k) tot += sv[k];
                        const u32x2 r = both_halves(tot);
                        mean = asf(r[0]) * (1.0f / 512.0f);
                        const float var = fmaxf(asf(r[1]) * (1.0f / 512.0f) - mean * mean, 0.f);
                        r2 = __builtin_amdgcn_rcpf(var + 1e-5f);
                        asm volatile("" : "+v"(mean), "+v"(r2));
                    }
                    SB(); });
                BAR();
                STAMP(16);
                gemm4<16>(lds, L_X + t128, L_X + (t128 ^ 64), hf, g0, g1, u0, u1, [&](int i) {
                    if (i < 8) { hf[24 + i] = ld16(rsW, lane * 16, WS_H + (T * 32 + ((24 + i + r3) & 31)) * 1024); SB(); }
                    if (i == 7) { SBAR(); SB(); } });
                BAR();
                STAMP(17);
                act_out(g0, u0, 0, 4 * j);
                act_out(g1, u1, 1, 4 * j + 2);
                SBAR();
            }
            if (npass == 3) {
                f32x16 ag = {}, au = {};
                gemm2<32, 2048, 512, 0>(lds, L_Y + trp, L_Y + 1024 + trp, hf, ag, au, [&](int g) {
                    if (g == 5) { WAITV(0); arrive_publish(lds, 3, 4, ws, WS_F4 + (p * 16 + j) * 256, tag, lane); PIN(); } });
                BAR();
                STAMP(18);
                act_out(ag, au, 2, 2 * u3);
                WAITV(0); arrive_publish(lds, 4, 4, ws, WS_F4 + 0x10000 + (p * 16 + j) * 256, tag, lane);
            } else {
                WAITV(0); arrive_publish(lds, 3, 4, ws, WS_F4 + (p * 16 + j) * 256, tag, lane);
            }
        }
        STAMP(19);
        {
            f32x16 a0 = {}, a1 = {};
            const unsigned h0 = npass == 3 ? L_X : L_Y, h1 = npass == 3 ? L_Y : L_X;
            if (w == 0) poll_flags(ws, WS_F4 + pP * 16 * 256, 16, tag, lane);
            BAR();
            STAMP(20);
            u32x4 af[43];
            auto ld_act = [&](int i) {
                const int ks = i < 32 ? 32 * (i >> 4) + kh * 16 + ((i + r2) & 15) : 64 + kh * 11 + (i - 32 + r4 >= 11 ? i - 32 + r4 - 11 : i - 32 + r4);
                af[i] = ld16(rsW, lane * 16, WS_ACT + (T2 * 86 + ks) * 1024); };
#pragma unroll
            for (int i = 0; i < 8; ++i) ld_act(i);
            SBAR();
            gemm2<16, 4096, 1024, 0>(lds, h0 + kh * 2048 + t128, h0 + kh * 2048 + (t128 ^ 64), af, a0, a1, [&](int g) { ld_act(8 + 2 * g); ld_act(9 + 2 * g); SB(); });
            STAMP(21);
            if (w == 0) poll_flags3(ws, WS_F4 + 0x10000 + pP * 16 * 256, tag, lane);
            BAR();
            STAMP(22);
            gemm2<16, 4096, 1024, 16>(lds, h1 + kh * 2048 + t128, h1 + kh * 2048 + (t128 ^ 64), af, a0, a1, [&](int g) { ld_act(24 + 2 * g); ld_act(25 + 2 * g); SB(); });
            BAR();
            STAMP(23);
            gemm2<11, 4096, 1024, 32>(lds, h0 + kh * 2048 + t128, h0 + kh * 2048 + (t128 ^ 64), af, a0, a1, [&](int g) { if (g < 2) { ld_act(40 + 2 * g < 43 ? 40 + 2 * g : 42); if (g == 0) ld_act(41); SB(); } });
            const f32x16 snd = kh ? a0 : a1;
            f32x16 keep = kh ? a1 : a0;
#pragma unroll
            for (int g = 0; g < 4; ++g) *(LAS f32x4*)(lds + L_XCH + w * 4096 + g * 1024 + lane * 16) = (f32x4){snd[4 * g], snd[4 * g + 1], snd[4 * g + 2], snd[4 * g + 3]};
            BAR();
            STAMP(24);
#pragma unroll
            for (int g = 0; g < 4; ++g) {
                const f32x4 v = *(LAS f32x4*)(lds + L_XCH + (w ^ 2) * 4096 + g * 1024 + lane * 16);
#pragma unroll
                for (int e = 0; e < 4; ++e) keep[4 * g + e] += v[e];
            }
            const u32x4 hres0 = *(LAS u32x4*)(lds + L_HRES + w * 2048 + lane * 16), hres1 = *(LAS u32x4*)(lds + L_HRES + w * 2048 + 1024 + lane * 16);
            u32x4 o0, o1;
#pragma unroll
            for (int e = 0; e < 4; ++e) {
                o0[e] = pack2(keep[2 * e] + blo(hres0[e]), keep[2 * e + 1] + bhi(hres0[e]));
                o1[e] = pack2(keep[8 + 2 * e] + blo(hres1[e]), keep[8 + 2 * e + 1] + bhi(hres1[e]));
            }
            { auto r = __builtin_amdgcn_permlane32_swap(o0[0], o0[2], false, false); o0[0] = r[0]; o0[2] = r[1];
              r = __builtin_amdgcn_permlane32_swap(o0[1], o0[3], false, false); o0[1] = r[0]; o0[3] = r[1];
              r = __builtin_amdgcn_permlane32_swap(o1[0], o1[2], false, false); o1[0] = r[0]; o1[2] = r[1];
              r = __builtin_amdgcn_permlane32_swap(o1[1], o1[3], false, false); o1[1] = r[0]; o1[3] = r[1]; }
            const rsrc_t rsOut = __builtin_amdgcn_make_buffer_rsrc((void*)out, 0, 2048 * 512 * 2, 0x00020000);
            int tz = threadIdx.x; asm volatile("" : "+v"(tz));
            const unsigned vz = (32 * T2 + (tz & 31)) * 1024 + (C0 + 8 * ((tz >> 5) & 1)) * 2;
            __builtin_amdgcn_raw_buffer_store_b128(o0, rsOut, vz, 0, 0);
            __builtin_amdgcn_raw_buffer_store_b128(o1, rsOut, vz + 32, 0, 0);
        }
        STAMP(25); CSTAMP(31);
    }
    if (tag == 1ull) {
        float z = (float)lane;
        asm volatile(".rept 8192\n\tv_fma_f32 %0, %0, %0, %0\n\t.endr" : "+v"(z));
        ((GAS float*)ws)[lane] = z;
    }
}

extern "C" void kernel_launch(void* const* d_in, const int* in_sizes, int n_in, void* d_out, int out_size, void* d_ws, size_t ws_size, hipStream_t stream) {
    (void)in_sizes; (void)n_in; (void)out_size; (void)ws_size;
    (void)hipFuncSetAttribute((const void*)hedgehog_block, hipFuncAttributeMaxDynamicSharedMemorySize, LDS_BYTES);
    hedgehog_block<<<256, 512, LDS_BYTES, stream>>>((const bf16_t*)d_in[0], (const bf16_t*)d_in[1], (const bf16_t*)d_in[2], (const bf16_t*)d_in[3],
                                                    (const bf16_t*)d_in[5], (const bf16_t*)d_in[6], (const bf16_t*)d_in[7], (bf16_t*)d_out, (char*)d_ws);
}
```

```cpp
#include <hip/hip_runtime.h>
#include <hip/hip_bf16.h>

#define LAS __attribute__((address_space(3)))
#define GAS __attribute__((address_space(1)))
#define DI __device__ __forceinline__
typedef unsigned short bf16_t;
typedef unsigned u32x4 __attribute__((ext_vector_type(4)));
typedef unsigned u32x2 __attribute__((ext_vector_type(2)));
typedef short s16x4 __attribute__((ext_vector_type(4)));
typedef __bf16 bf16x8 __attribute__((ext_vector_type(8)));
typedef __bf16 bf16x2 __attribute__((ext_vector_type(2)));
typedef float f32x16 __attribute__((ext_vector_type(16)));
typedef float f32x4 __attribute__((ext_vector_type(4)));
typedef float f32x2 __attribute__((ext_vector_type(2)));
typedef unsigned long long u64;
typedef __amdgpu_buffer_rsrc_t rsrc_t;

constexpr unsigned WS_F1 = 0x10000, WS_F2 = 0x20000, WS_F3 = 0x30000, WS_F4 = 0x40000;
constexpr unsigned WS_KVP = 0x80000, KVP_BYTES = 2176;
constexpr unsigned WS_HST = 0x110000;
constexpr unsigned WS_XST = 0x160000;
constexpr unsigned WS_O = 0x200000, WS_H = 0x400000, WS_ACT = 0x600000, WS_PROF = 0xC00000, WS_BYTES = 0x1000000;
constexpr unsigned L_X = 0, L_Y = 65536, L_XCH = 131072, L_MISC = 147456;
constexpr unsigned L_STAT = L_MISC, L_CS1 = L_MISC + 1024, L_CSQ = L_MISC + 1280, L_WPHI = L_MISC + 1536, L_KVA = L_MISC + 2560, L_CS3 = L_MISC + 4736, L_CNT = L_MISC + 5504, L_TAG = L_MISC + 6016, L_DUMMY = L_MISC + 6144, L_HRES = L_MISC + 8192;
constexpr unsigned L_WQ = 65536, L_PHI = 98304, L_VIMG = 114688  , SS_BYTES = 24576;
constexpr unsigned LDS_BYTES = 163840;
constexpr unsigned SPIN_CAP = 40000;
constexpr float LOG2E = 1.4426950408889634f;

DI float asf(unsigned v) { return __builtin_bit_cast(float, v); }
DI unsigned asu(float v) { return __builtin_bit_cast(unsigned, v); }
DI unsigned pack2(float lo, float hi) { bf16x2 v = {(__bf16)lo, (__bf16)hi}; return __builtin_bit_cast(unsigned, v); }
DI float blo(unsigned v) { return __builtin_bit_cast(float, v << 16); }
DI float bhi(unsigned v) { return __builtin_bit_cast(float, v & 0xffff0000u); }
DI f32x16 mfma(u32x4 a, u32x4 b, f32x16 c) { return __builtin_amdgcn_mfma_f32_32x32x16_bf16(__builtin_bit_cast(bf16x8, a), __builtin_bit_cast(bf16x8, b), c, 0, 0, 0); }
DI f32x4 mfma16(u32x4 a, u32x4 b, f32x4 c) { return __builtin_amdgcn_mfma_f32_16x16x32_bf16(__builtin_bit_cast(bf16x8, a), __builtin_bit_cast(bf16x8, b), c, 0, 0, 0); }
DI u32x4 tr2(LAS char* lds, unsigned off, unsigned second) {
    const s16x4 lo = __builtin_amdgcn_ds_read_tr16_b64_v4i16((LAS s16x4*)(lds + off));
    const s16x4 hi = __builtin_amdgcn_ds_read_tr16_b64_v4i16((LAS s16x4*)(lds + off + second));
    return __builtin_bit_cast(u32x4, __builtin_shufflevector(lo, hi, 0, 1, 2, 3, 4, 5, 6, 7));
}
DI void dma16(const rsrc_t rs, unsigned ldsaddr, unsigned voff, unsigned soff) {
    asm volatile("s_mov_b32 m0, %0\n\ts_nop 0\n\tbuffer_load_dwordx4 %1, %2, %3 offen lds" ::"s"(ldsaddr), "v"(voff), "s"(rs), "s"(soff) : "memory");
}
constexpr unsigned BIAS = 4096;
DI rsrc_t biased_rsrc(const void* p, unsigned bytes) { return __builtin_amdgcn_make_buffer_rsrc((void*)((const char*)p - BIAS), 0, bytes + BIAS, 0x00020000); }
DI void dma4(unsigned ldsaddr, const rsrc_t ra, const rsrc_t rb, const rsrc_t rc, const rsrc_t rd, unsigned va, unsigned vb, unsigned s0, unsigned s1, unsigned s2, unsigned s3) {
    asm volatile("s_mov_b32 m0, %0\n\ts_nop 0\n\t"
                 "buffer_load_dwordx4 %5, %1, %7 offen lds\n\tbuffer_load_dwordx4 %6, %2, %8 offen offset:1024 lds\n\t"
                 "buffer_load_dwordx4 %5, %3, %9 offen offset:2048 lds\n\tbuffer_load_dwordx4 %6, %4, %10 offen offset:3072 lds"
                 ::"s"(ldsaddr), "s"(ra), "s"(rb), "s"(rc), "s"(rd), "v"(va), "v"(vb), "s"(s0 + BIAS), "s"(s1 + BIAS - 1024), "s"(s2 + BIAS - 2048), "s"(s3 + BIAS - 3072) : "memory");
}
DI void dma2(unsigned ldsaddr, const rsrc_t r, unsigned va, unsigned vb, unsigned s0, unsigned s1) {
    asm volatile("s_mov_b32 m0, %0\n\ts_nop 0\n\tbuffer_load_dwordx4 %2, %1, %4 offen lds\n\tbuffer_load_dwordx4 %3, %1, %5 offen offset:1024 lds"
                 ::"s"(ldsaddr), "s"(r), "v"(va), "v"(vb), "s"(s0 + BIAS), "s"(s1 + BIAS - 1024) : "memory");
}
#define WAITV(n) asm volatile("s_waitcnt vmcnt(" #n ")" ::: "memory")
#define PIN() do { asm volatile("" ::: "memory"); __builtin_amdgcn_sched_barrier(0); } while (0)
template <int N> DI void waitv() { asm volatile("s_waitcnt vmcnt(%0)" ::"n"(N) : "memory"); }
#define A_EARLY 1
#define B_EARLY 1
#define K0 3
#define R1MODE 2
#define STAMP2_SS(ss) do { if (ss == 0) STAMP2(0); if (ss == 1) STAMP2(1); if (ss == 2) STAMP2(2); if (ss == 3) STAMP2(3); if (ss == 4) STAMP2(4); if (ss == 5) STAMP2(5); if (ss == 6) STAMP2(6); if (ss == 7) STAMP2(7); } while (0)
#define SBAR() asm volatile("s_barrier" ::: "memory")
#define BAR() asm volatile("s_waitcnt lgkmcnt(0)\n\ts_barrier" ::: "memory")
DI u32x2 both_halves(float v) { const unsigned a = asu(v); const auto r = __builtin_amdgcn_permlane32_swap(a, a, false, false); return (u32x2){r[0], r[1]}; }
DI float halves_add(float v) { const u32x2 r = both_halves(v); return asf(r[0]) + asf(r[1]); }
DI float halves_max(float v) { const u32x2 r = both_halves(v); return fmaxf(asf(r[0]), asf(r[1])); }
DI float halves_min(float v) { const u32x2 r = both_halves(v); return fminf(asf(r[0]), asf(r[1])); }
DI float dpp_ror4(float v) { return asf(__builtin_amdgcn_update_dpp(0, (int)asu(v), 0x124, 0xf, 0xf, true)); }
DI float dpp_ror8(float v) { return asf(__builtin_amdgcn_update_dpp(0, (int)asu(v), 0x128, 0xf, 0xf, true)); }
DI float rows16_add(float v) { const unsigned a = asu(v); const auto r = __builtin_amdgcn_permlane16_swap(a, a, false, false); return asf(r[0]) + asf(r[1]); }
DI float stride4_add(float v) { v += dpp_ror4(v); v += dpp_ror8(v); v = rows16_add(v); return halves_add(v); }
DI float quad_add(float v) {
    v += __builtin_bit_cast(float, __builtin_amdgcn_update_dpp(0, __builtin_bit_cast(int, v), 0xB1, 0xf, 0xf, true));
    v += __builtin_bit_cast(float, __builtin_amdgcn_update_dpp(0, __builtin_bit_cast(int, v), 0x4E, 0xf, 0xf, true));
    return v;
}
DI void st16(const rsrc_t rs, u32x4 v, unsigned voff, unsigned soff) { __builtin_amdgcn_raw_buffer_store_b128(v, rs, voff + soff, 0, 16); }
DI u32x4 ld16(const rsrc_t rs, unsigned voff, unsigned soff) { return __builtin_amdgcn_raw_buffer_load_b128(rs, voff, soff, 16); }
DI unsigned ld4(const rsrc_t rs, unsigned voff, unsigned soff) { return __builtin_amdgcn_raw_buffer_load_b32(rs, voff, soff, 16); }

DI void poll_flags3(const char* ws, unsigned off, u64 tag, int lane) {
    const int l = lane < 11 ? lane : 10;
    const GAS u64* f = (const GAS u64*)(ws + off) + (l < 6 ? l : l + 2) * 32;
#pragma unroll 1
    for (unsigned spin = 0; spin < SPIN_CAP; ++spin) {
        const u64 v = __hip_atomic_load(f, __ATOMIC_RELAXED, __HIP_MEMORY_SCOPE_AGENT);
        if (__all(v == tag)) break;
        __builtin_amdgcn_s_sleep(1);
    }
    asm volatile("" ::: "memory");
}
DI void nohook(int) {}
DI void poll_flags(const char* ws, unsigned off, int n, u64 tag, int lane) {
    const GAS u64* f = (const GAS u64*)(ws + off) + (lane < n ? lane : n - 1) * 32;
#pragma unroll 1
    for (unsigned spin = 0; spin < SPIN_CAP; ++spin) {
        const u64 v = __hip_atomic_load(f, __ATOMIC_RELAXED, __HIP_MEMORY_SCOPE_AGENT);
        if (__all(v == tag)) break;
        __builtin_amdgcn_s_sleep(1);
    }
    asm volatile("" ::: "memory");
}
DI void arrive_publish(LAS char* lds, int cnt, unsigned n, char* ws, unsigned flag_off, u64 tag, int lane) {
    if (lane == 0) {
        const unsigned old = __hip_atomic_fetch_add((LAS unsigned*)(lds + L_CNT) + cnt, 1u, __ATOMIC_RELAXED, __HIP_MEMORY_SCOPE_WORKGROUP);
        if (old == n - 1) __hip_atomic_store((GAS u64*)(ws + flag_off), tag, __ATOMIC_RELAXED, __HIP_MEMORY_SCOPE_AGENT);
    }
}
DI void lsig(unsigned lb, int idx) {
    const unsigned a = lb + L_CNT + idx * 4, one = 1;
    asm volatile("s_mov_b64 exec, 1\n\tds_add_u32 %0, %1\n\ts_mov_b64 exec, -1" ::"v"(a), "v"(one) : "memory");
}
DI void lwait(LAS char* lds, int idx, unsigned n) {
    const volatile LAS unsigned* c = (const volatile LAS unsigned*)(lds + L_CNT) + idx;
#pragma unroll 1
    for (unsigned spin = 0; spin < 64 * SPIN_CAP; ++spin) {
        if ((unsigned)__builtin_amdgcn_readfirstlane(*c) >= n) break;
        __builtin_amdgcn_s_sleep(1);
    }
    asm volatile("" ::: "memory");
}
DI void dual_softmax(const f32x16& p, float sl, u32x4& pos, u32x4& neg) {
    float mx = fmaxf(fmaxf(fmaxf(p[0], p[1]), fmaxf(p[2], p[3])), fmaxf(fmaxf(p[4], p[5]), fmaxf(p[6], p[7])));
    float mn = fminf(fminf(fminf(p[0], p[1]), fminf(p[2], p[3])), fminf(fminf(p[4], p[5]), fminf(p[6], p[7])));
    mx = halves_max(mx); mn = halves_min(mn);
    const float cx = -mx * sl, cn = mn * sl;
    f32x2 ep[4], en[4];
#pragma unroll
    for (int i = 0; i < 4; ++i) {
        const f32x2 v = {p[2 * i], p[2 * i + 1]};
        const f32x2 u = v * sl + cx, d = v * (-sl) + cn;
        ep[i] = (f32x2){__builtin_amdgcn_exp2f(u[0]), __builtin_amdgcn_exp2f(u[1])};
        en[i] = (f32x2){__builtin_amdgcn_exp2f(d[0]), __builtin_amdgcn_exp2f(d[1])};
    }
    const f32x2 sp = (ep[0] + ep[1]) + (ep[2] + ep[3]), sn = (en[0] + en[1]) + (en[2] + en[3]);
    const float rp = __builtin_amdgcn_rcpf(halves_add(sp[0] + sp[1])), rn = __builtin_amdgcn_rcpf(halves_add(sn[0] + sn[1]));
#pragma unroll
    for (int i = 0; i < 4; ++i) { const f32x2 a = ep[i] * rp, b = en[i] * rn; pos[i] = pack2(a[0], a[1]); neg[i] = pack2(b[0], b[1]); }
}

#define SB() __builtin_amdgcn_sched_barrier(0)
template <int N, int STRIDE, int SECOND, int OFF, int NA, class Hook>
DI void gemm2(LAS char* lds, unsigned b0, unsigned b1, const u32x4 (&act)[NA], f32x16& acc0, f32x16& acc1, Hook hook) {
    u32x4 fb[2][4];
    asm volatile("" : "+v"(b0), "+v"(b1));
    constexpr int NG = (N + 1) / 2;
#pragma unroll
    for (int e = 0; e < 2; ++e) { fb[0][2 * e] = tr2(lds, b0 + e * STRIDE, SECOND); fb[0][2 * e + 1] = tr2(lds, b1 + e * STRIDE, SECOND); }
    SB();
#pragma unroll
    for (int g = 0; g < NG; ++g) {
#pragma unroll
        for (int e = 0; e < 2; ++e) {
            const int i = 2 * g + e, n = i + 2;
            if (i < N) acc0 = mfma(fb[g & 1][2 * e], act[OFF + i], acc0);
            if (n < N) fb[(g + 1) & 1][2 * e] = tr2(lds, b0 + n * STRIDE, SECOND);
            SB();
            if (i < N) acc1 = mfma(fb[g & 1][2 * e + 1], act[OFF + i], acc1);
            if (n < N) fb[(g + 1) & 1][2 * e + 1] = tr2(lds, b1 + n * STRIDE, SECOND);
            SB();
        }
        hook(g);
    }
    asm volatile("" : "+v"(acc0), "+v"(acc1));
}
template <int OFF, class Hook>
DI void gemm4(LAS char* lds, unsigned b0, unsigned b1, const u32x4 (&act)[32], f32x16& g0, f32x16& g1, f32x16& u0, f32x16& u1, Hook hook) {
    u32x4 fb[2][4];
    asm volatile("" : "+v"(b0), "+v"(b1));
    fb[0][0] = tr2(lds, b0, 1024); fb[0][1] = tr2(lds, b1, 1024); fb[0][2] = tr2(lds, b0 + 2048, 1024); fb[0][3] = tr2(lds, b1 + 2048, 1024);
    SB();
#pragma unroll
    for (int i = 0; i < 16; ++i) {
        const int o = (i + 1) * 4096;
        g0 = mfma(fb[i & 1][0], act[OFF + i], g0);
        if (i < 15) fb[(i + 1) & 1][0] = tr2(lds, b0 + o, 1024);
        SB();
        g1 = mfma(fb[i & 1][1], act[OFF + i], g1);
        if (i < 15) fb[(i + 1) & 1][1] = tr2(lds, b1 + o, 1024);
        SB();
        u0 = mfma(fb[i & 1][2], act[OFF + i], u0);
        if (i < 15) fb[(i + 1) & 1][2] = tr2(lds, b0 + o + 2048, 1024);
        SB();
        u1 = mfma(fb[i & 1][3], act[OFF + i], u1);
        if (i < 15) fb[(i + 1) & 1][3] = tr2(lds, b1 + o + 2048, 1024);
        SB();
        hook(i);
    }
    asm volatile("" : "+v"(g0), "+v"(g1), "+v"(u0), "+v"(u1));
}
DI void touch_lines(const rsrc_t rs, unsigned ldsaddr, unsigned voff, unsigned soff) {
    asm volatile("s_mov_b32 m0, %0\n\ts_nop 0\n\tbuffer_load_dword %1, %2, %3 offen lds" ::"s"(ldsaddr), "v"(voff), "s"(rs), "s"(soff) : "memory");
}

#define STAMP(i) do {} while (0)
#define CSTAMP(i) do {} while (0)
#define STAMP2(i) do {} while (0)

__global__ void __launch_bounds__(512, 2)
hedgehog_block(const bf16_t* __restrict__ x, const bf16_t* __restrict__ wqkv, const bf16_t* __restrict__ wphi, const bf16_t* __restrict__ wo,
               const bf16_t* __restrict__ wg, const bf16_t* __restrict__ wu, const bf16_t* __restrict__ wd, bf16_t* __restrict__ out, char* __restrict__ ws) {
    extern __shared__ __attribute__((aligned(16))) char smem[];
    LAS char* lds = (LAS char*)smem;
    const unsigned lb = (unsigned)(size_t)lds;
    const int tid = threadIdx.x, lane = tid & 63;
    const int w = __builtin_amdgcn_readfirstlane(tid >> 6);
    const int b = blockIdx.x, xcd = b & 7, slot = b >> 3;
    const int p = 4 * (xcd >> 1) + (slot >> 3), j = 8 * (xcd & 1) + (slot & 7);
    const int pP = 2 * xcd + (slot >> 4), jP = slot & 15, c = slot & 7, half = (slot >> 3) & 1;
    const bool has3 = (j & 7) < (j < 8 ? 6 : 5);
    const int u3 = 32 + (j < 8 ? j : j - 2);
    const int t = lane & 31, h2 = lane >> 5, q = (lane & 15) >> 2, pp = lane & 3, blk = (lane >> 4) & 1;
    const int m = w & 3, a = w >> 2, lw = w & 3;
    const int q4 = slot >> 3;
    const int r1 = R1MODE == 0 ? 0 : R1MODE == 1 ? ((c + 2 * q4) & 7) : R1MODE == 2 ? 4 * (q4 & 1) : 2 * q4, r2 = (2 * c + 4 * q4) & 15, r3 = (4 * c + 8 * q4) & 31, r4 = (c + 3 * q4) % 11;
    STAMP(0); CSTAMP(30);
    const rsrc_t rsX = __builtin_amdgcn_make_buffer_rsrc((void*)x, 0, 2048 * 512 * 2, 0x00020000);
    const rsrc_t rsQ = __builtin_amdgcn_make_buffer_rsrc((void*)wqkv, 0, 512 * 1536 * 2, 0x00020000);
    const rsrc_t rsW = __builtin_amdgcn_make_buffer_rsrc((void*)ws, 0, WS_BYTES, 0x00020000);
    u64 tag = 0;
#define UNIFORM64(v) (((u64)(unsigned)__builtin_amdgcn_readfirstlane((unsigned)((v) >> 32)) << 32) | (unsigned)__builtin_amdgcn_readfirstlane((unsigned)(v)))
    const u32x4 ones = {0x3F803F80u, 0x3F803F80u, 0x3F803F80u, 0x3F803F80u};
    const unsigned c128 = ((lane & 7) ^ (4 * ((lane >> 4) & 1))) * 16;
    const unsigned vrow = (lane >> 3) * 1024 + c128;
    const unsigned trn = (8 * h2 + q) * 64 + pp * 8;

    const rsrc_t rsG = biased_rsrc(wg, 512 * 1376 * 2), rsU = biased_rsrc(wu, 512 * 1376 * 2);
    const unsigned vgu = (lane >> 2) * 2752 + (lane & 3) * 16, vgu8 = (lane >> 3) * 2752 + c128;
#define ISSUE_STAGE(st, halfbase, g0, g1) do { _Pragma("unroll") for (int g_ = (g0); g_ < (g1); ++g_) { const int i_ = 4 * g_ + lw; const unsigned so_ = 16 * ((16 * (st) + i_ + r3) & 31) * 2752 + 128 * j; \
            dma4(lb + (halfbase) + i_ * 4096, rsG, rsG, rsU, rsU, vgu8, vgu8, so_, so_ + 8 * 2752, so_, so_ + 8 * 2752); } } while (0)
#define ISSUE_UNIT(u, halfbase) do { _Pragma("unroll") for (int g_ = 0; g_ < 4; ++g_) { const int s_ = 2 * (4 * g_ + lw); \
            const unsigned sa_ = 16 * ((s_ + r3) & 31) * 2752 + 64 * (u), sb_ = 16 * ((s_ + 1 + r3) & 31) * 2752 + 64 * (u); \
            dma4(lb + (halfbase) + s_ * 2048, rsG, rsU, rsG, rsU, vgu, vgu, sa_, sa_, sb_, sb_); } } while (0)
    u32x4 xf[32];
    float yk[16];
    float mean1, rstd1;
    if (w < 4) {
        {
            u64 epoch = __hip_atomic_load((const GAS u64*)ws, __ATOMIC_RELAXED, __HIP_MEMORY_SCOPE_AGENT);
            const unsigned R = 128 * p + 8 * j + 2 * w;
            const u32x4 v0 = __builtin_amdgcn_raw_buffer_load_b128(rsX, lane * 16, R * 1024, 0), v1 = __builtin_amdgcn_raw_buffer_load_b128(rsX, lane * 16, R * 1024 + 1024, 0);
            if (tid < 16) *(LAS unsigned*)(lds + L_CNT + tid * 4) = 0u;
            f32x2 a1 = {0.f, 0.f}, a2 = {0.f, 0.f}, b1 = {0.f, 0.f}, b2 = {0.f, 0.f};
#pragma unroll
            for (int e = 0; e < 4; ++e) {
                const f32x2 z0 = {blo(v0[e]), bhi(v0[e])}, z1 = {blo(v1[e]), bhi(v1[e])};
                a1 += z0; a2 += z0 * z0; b1 += z1; b2 += z1 * z1;
            }
            const float su0 = stride4_add(quad_add(a1[0] + a1[1])), sq0 = stride4_add(quad_add(a2[0] + a2[1]));
            const float su1 = stride4_add(quad_add(b1[0] + b1[1])), sq1 = stride4_add(quad_add(b2[0] + b2[1]));
            const float su = (lane & 1) ? su1 : su0, sq = (lane & 1) ? sq1 : sq0;
            const float mean = su * (1.0f / 512.0f), var = fmaxf(sq * (1.0f / 512.0f) - mean * mean, 0.f);
            const float rstd = __builtin_amdgcn_rsqf(var + 1e-5f);
            asm volatile("s_waitcnt vmcnt(0)" : "+v"(epoch)::"memory");
            tag = UNIFORM64(epoch) + 0x9E3779B97F4A7C15ull;
            if (tid == 0) *(LAS u64*)(lds + L_TAG) = tag;
            if (lane < 2) st16(rsW, (u32x4){asu(mean), asu(rstd), (unsigned)tag, (unsigned)(tag >> 32)}, (R + lane) * 16, WS_XST);
        }
        STAMP(1);
        const unsigned xa0 = (32 * m + t) * 128 + ((h2 ^ ((t >> 1) & 7)) * 16);
        const unsigned twA = 16384 + (blk ? 1024 + 32 : 0) + trn, twB = 16384 + (blk ? 1024 : 32) + trn;
        f32x16 accA = {}, accB = {};
        u32x4 fa[2][2], fb[2][2], xs;
        unsigned bA = twA, bB = twB, bx0 = xa0, bx1 = xa0 ^ 32, bx2 = xa0 ^ 64, bx3 = xa0 ^ 96;
        asm volatile("" : "+v"(bA), "+v"(bB), "+v"(bx0), "+v"(bx1), "+v"(bx2), "+v"(bx3));
#pragma unroll
        for (int ss = 0; ss <= 8; ++ss) {
            if (ss < 8) {
                BAR();
                STAMP2_SS(ss);
                if (ss == 0) STAMP(2);
                if (ss == 6) { xs = ld16(rsW, (128 * p + 32 * m + t) * 16, WS_XST); SB(); }
            }
#pragma unroll
            for (int e = 0; e < 2; ++e) {
                if (ss > 0) accA = mfma(fa[1][e], xf[4 * ss - 2 + e], accA);
                if (ss < 8) { xf[4 * ss + e] = *(LAS u32x4*)(lds + (e ? bx1 : bx0)); fa[0][e] = tr2(lds, bA + e * 2048, 256); }
                SB();
                if (ss > 0) accB = mfma(fb[1][e], xf[4 * ss - 2 + e], accB);
                if (ss < 8) fb[0][e] = tr2(lds, bB + e * 2048, 256);
                SB();
            }
            if (ss < 8) {
#pragma unroll
                for (int e = 0; e < 2; ++e) {
                    accA = mfma(fa[0][e], xf[4 * ss + e], accA);
                    xf[4 * ss + 2 + e] = *(LAS u32x4*)(lds + (e ? bx3 : bx2)); fa[1][e] = tr2(lds, bA + (2 + e) * 2048, 256);
                    SB();
                    accB = mfma(fb[0][e], xf[4 * ss + e], accB);
                    fb[1][e] = tr2(lds, bB + (2 + e) * 2048, 256);
                    SB();
                }
                if (ss < 7) {
                    const unsigned d = ss == 5 ? (unsigned)(-5 * (int)SS_BYTES) : SS_BYTES;
                    bA += d; bB += d; bx0 += d; bx1 += d; bx2 += d; bx3 += d;
                    asm volatile("" : "+v"(bA), "+v"(bB), "+v"(bx0), "+v"(bx1), "+v"(bx2), "+v"(bx3));
                }
            }
        }
        STAMP(3);
#pragma unroll
        for (int g = 0; g < 4; ++g) *(LAS f32x4*)(lds + L_XCH + m * 4096 + g * 1024 + lane * 16) = (f32x4){accB[4 * g], accB[4 * g + 1], accB[4 * g + 2], accB[4 * g + 3]};
#pragma unroll 1
        for (unsigned spin = 0; spin < SPIN_CAP; ++spin) {
            if (__all(xs[2] == (unsigned)tag && xs[3] == (unsigned)(tag >> 32))) break;
            __builtin_amdgcn_s_sleep(1);
            xs = ld16(rsW, (128 * p + 32 * m + t) * 16, WS_XST);
        }
        mean1 = asf(xs[0]); rstd1 = asf(xs[1]);
        if (h2 == 0) *(LAS f32x2*)(lds + L_STAT + (32 * m + t) * 8) = (f32x2){mean1, rstd1};
        BAR();
        STAMP(4);
#pragma unroll
        for (int g = 0; g < 4; ++g) {
            const f32x4 cs = *(LAS f32x4*)(lds + L_CS1 + (8 * g + 4 * h2) * 4);
#pragma unroll
            for (int e = 0; e < 4; ++e) { const float y = __builtin_fmaf(-mean1, cs[e], accA[4 * g + e]); yk[4 * g + e] = g < 2 ? y : y * rstd1; }
        }
    } else {
        const unsigned vx0 = (lane >> 3) * 1024 + (((lane & 7) ^ (lane >> 4)) * 16), vx1 = (lane >> 3) * 1024 + (((lane & 7) ^ (4 + (lane >> 4))) * 16);
        const unsigned vwk = (lane >> 2) * 3072 + (lane & 3) * 16, vwv = (lane >> 2) * 3072 + ((lane & 3) ^ 2) * 16;
        const rsrc_t rbX = biased_rsrc(x, 2048 * 512 * 2), rbQ = biased_rsrc(wqkv, 512 * 1536 * 2);
#define ISSUE_SS(ss, v) do { const unsigned sb_ = lb + ((ss) % 6) * SS_BYTES, ks_ = ((ss) + r1) & 7; \
            const unsigned sx_ = (128 * p + 32 * (v)) * 1024 + ks_ * 128, sw_ = (16 * (v) + 64 * ks_) * 3072 + (512 + 32 * j) * 2; \
            dma2(sb_ + 16384 + (v) * 2048, rbQ, vwk, vwv, sw_, sw_ + 1024); \
            dma4(sb_ + (v) * 4096, rbX, rbX, rbX, rbX, vx0, vx1, sx_, sx_ + 8 * 1024, sx_ + 16 * 1024, sx_ + 24 * 1024); } while (0)
        if (lw == 0) {
            const rsrc_t rsP = __builtin_amdgcn_make_buffer_rsrc((void*)wphi, 0, 32 * 256 * 2, 0x00020000);
            dma16(rsP, lb + L_WPHI, lane * 16, j * 1024);
        }
#pragma unroll
        for (int ss = 0; ss < K0; ++ss) ISSUE_SS(ss, lw);
        STAMP(1);
        f32x4 csacc = {};
        const unsigned tw = 16384 + ((lw & 1) ? 1024 + ((lw >> 1) ^ 1) * 32 : (lw >> 1) * 32) + (lane >> 5) * 2048 + (8 * ((lane >> 4) & 1) + q) * 64 + pp * 8;
        constexpr int NL = 8 - K0;
#define P1WAIT(s) (6 * ((((s) + K0 - 1 < 7 ? (s) + K0 - 1 : 7) - (s))) + ((s) > 4 ? 2 * ((s) - 4) : 0))
#pragma unroll
        for (int ss = 0; ss < 8; ++ss) {
            if (ss == 0) waitv<P1WAIT(0)>();
            if (ss == 1) waitv<P1WAIT(1)>();
            if (ss == 2) waitv<P1WAIT(2)>();
            if (ss == 3) waitv<P1WAIT(3)>();
            if (ss == 4) waitv<P1WAIT(4)>();
            if (ss == 5) waitv<P1WAIT(5)>();
            if (ss == 6) waitv<P1WAIT(6)>();
            if (ss == 7) waitv<P1WAIT(7)>();
            STAMP2_SS(ss);
            BAR();
            if (ss == 0) { const u64 tv = *(LAS u64*)(lds + L_TAG); tag = UNIFORM64(tv); STAMP(2); }
            unsigned bw = (ss % 6) * SS_BYTES + tw;
            asm volatile("" : "+v"(bw));
            const u32x4 f0 = tr2(lds, bw, 256), f1 = tr2(lds, bw + 4096, 256);
            SB();
            if (ss < NL) ISSUE_SS(K0 + ss, lw);
            if (ss == 6) {
#pragma unroll
                for (int i = 0; i < 4; ++i) *(LAS u32x4*)(lds + L_VIMG + (4 * lw + i) * 1024 + lane * 16) = ones;
            }
            if (ss >= 4) {
                const int pc = 8 * lw + 2 * (ss - 4);
                const unsigned so = 16 * ((pc + 4 * r1) & 31) * 3072 + 64 * j;
                dma2(lb + L_WQ + pc * 1024, rbQ, vwk, vwk, so, so + 16 * 3072);
            }
            csacc = mfma16(f0, ones, csacc);
            csacc = mfma16(f1, ones, csacc);
            SB();
        }
        if ((lane & 15) == 0) *(LAS f32x4*)(lds + L_CS1 + (lw >> 1) * 128 + (lw & 1) * 64 + (lane >> 4) * 16) = csacc;
        STAMP(3);
        BAR();
        STAMP(4);
        const f32x2 st = *(LAS f32x2*)(lds + L_STAT + (32 * m + t) * 8);
        mean1 = st[0]; rstd1 = st[1];
#pragma unroll
        for (int g = 0; g < 4; ++g) {
            const f32x4 cs = *(LAS f32x4*)(lds + L_CS1 + 128 + (8 * g + 4 * h2) * 4);
            const f32x4 v = *(LAS f32x4*)(lds + L_XCH + m * 4096 + g * 1024 + lane * 16);
#pragma unroll
            for (int e = 0; e < 4; ++e) { const float y = __builtin_fmaf(-mean1, cs[e], v[e]); yk[4 * g + e] = g < 2 ? y : y * rstd1; }
        }
    }
    const unsigned imgA = L_PHI + a * 8192 + (32 * m + t) * 64 + ((h2 ^ ((t >> 1) & 7)) * 8);
    const u32x4 aphi = tr2(lds, L_WPHI + a * 512 + (4 * h2 + q) * 32 + pp * 8, 256);
    {
        const u32x4 kf = {pack2(yk[0], yk[1]), pack2(yk[2], yk[3]), pack2(yk[4], yk[5]), pack2(yk[6], yk[7])};
        const f32x16 zero = {};
        const f32x16 pk = mfma(aphi, kf, zero);
        *(LAS u32x2*)(lds + imgA + (L_VIMG - L_PHI)) = (u32x2){pack2(yk[8], yk[9]), pack2(yk[10], yk[11])};
        *(LAS u32x2*)(lds + (imgA ^ 16) + (L_VIMG - L_PHI)) = (u32x2){pack2(yk[12], yk[13]), pack2(yk[14], yk[15])};
        u32x4 pos, neg;
        dual_softmax(pk, rstd1 * LOG2E, pos, neg);
        *(LAS u32x2*)(lds + imgA) = (u32x2){pos[0], pos[1]};
        *(LAS u32x2*)(lds + (imgA ^ 16)) = (u32x2){pos[2], pos[3]};
        *(LAS u32x2*)(lds + (imgA ^ 32)) = (u32x2){neg[0], neg[1]};
        *(LAS u32x2*)(lds + (imgA ^ 48)) = (u32x2){neg[2], neg[3]};
    }
    if (w >= 4) waitv<0>();
    BAR();
    STAMP(5);
    float yq[8];
    if (w < 4) {
        unsigned tq = L_WQ + trn + blk * 32;
        asm volatile("" : "+v"(tq));
        f32x16 qa = {};
        u32x4 qb[2][4];
#pragma unroll
        for (int s = 0; s < 4; ++s) qb[0][s] = tr2(lds, tq + s * 1024, 256);
        SB();
#pragma unroll
        for (int s = 0; s < 32; ++s) {
            qa = mfma(qb[(s >> 2) & 1][s & 3], xf[s], qa);
            if (s < 28) qb[((s >> 2) + 1) & 1][s & 3] = tr2(lds, tq + (s + 4) * 1024, 256);
            SB();
        }
#pragma unroll
        for (int g = 0; g < 2; ++g) *(LAS f32x4*)(lds + L_XCH + m * 4096 + g * 1024 + lane * 16) = (f32x4){qa[8 + 4 * g], qa[9 + 4 * g], qa[10 + 4 * g], qa[11 + 4 * g]};
        STAMP(6);
        BAR();
#pragma unroll
        for (int g = 0; g < 2; ++g) {
            const f32x4 c0 = *(LAS f32x4*)(lds + L_CSQ + (8 * g + 4 * h2) * 4);
#pragma unroll
            for (int e = 0; e < 4; ++e) yq[4 * g + e] = __builtin_fmaf(-mean1, c0[e], qa[4 * g + e]);
        }
    } else {
        if (lw >= 2) {
            const int ah = lw - 2;
            f32x16 kv = {};
            unsigned aa[2], ab[2];
#pragma unroll
            for (int u = 0; u < 2; ++u) {
                const unsigned R = 8 * h2 + 4 * u + q, sw = (R >> 1) & 7;
                aa[u] = L_PHI + ah * 8192 + R * 64 + (((4 * blk + pp) ^ sw) * 8);
                ab[u] = aa[u] + (L_VIMG - L_PHI);
                asm volatile("" : "+v"(aa[u]), "+v"(ab[u]));
            }
            u32x4 A[8], B[8];
#pragma unroll
            for (int s = 0; s < 8; ++s) {
                const s16x4 a0 = __builtin_amdgcn_ds_read_tr16_b64_v4i16((LAS s16x4*)(lds + aa[0] + s * 1024));
                const s16x4 a1 = __builtin_amdgcn_ds_read_tr16_b64_v4i16((LAS s16x4*)(lds + aa[1] + s * 1024));
                const s16x4 b0 = __builtin_amdgcn_ds_read_tr16_b64_v4i16((LAS s16x4*)(lds + ab[0] + s * 1024));
                const s16x4 b1 = __builtin_amdgcn_ds_read_tr16_b64_v4i16((LAS s16x4*)(lds + ab[1] + s * 1024));
                A[s] = __builtin_bit_cast(u32x4, __builtin_shufflevector(a0, a1, 0, 1, 2, 3, 4, 5, 6, 7));
                B[s] = __builtin_bit_cast(u32x4, __builtin_shufflevector(b0, b1, 0, 1, 2, 3, 4, 5, 6, 7));
                if (s == 3) PIN();
            }
            PIN();
#pragma unroll
            for (int s = 0; s < 8; ++s) kv = mfma(A[s], B[s], kv);
            if (t <= 16) {
                const unsigned base = WS_KVP + (j * 16 + p) * KVP_BYTES + ((ah * 4 + h2) * 17 + t) * 16;
                st16(rsW, (u32x4){pack2(kv[0], kv[1]), pack2(kv[2], kv[3]), pack2(kv[4], kv[5]), pack2(kv[6], kv[7])}, base, 0);
                st16(rsW, (u32x4){pack2(kv[8], kv[9]), pack2(kv[10], kv[11]), pack2(kv[12], kv[13]), pack2(kv[14], kv[15])}, base + 34 * 16, 0);
            }
            WAITV(0);
            arrive_publish(lds, 0, 2, ws, WS_F1 + (j * 16 + p) * 256, tag, lane);
            STAMP(6);
        } else {
            unsigned tq = L_WQ + lw * 32 + (8 * (lane >> 4) + q) * 64 + pp * 8;
            asm volatile("" : "+v"(tq));
            f32x4 qcs = {};
#pragma unroll
            for (int g = 0; g < 4; ++g) {
                u32x4 f[4];
#pragma unroll
                for (int s = 0; s < 4; ++s) f[s] = tr2(lds, tq + (4 * g + s) * 2048, 256);
                PIN();
#pragma unroll
                for (int s = 0; s < 4; ++s) qcs = mfma16(f[s], ones, qcs);
            }
            if ((lane & 15) == 0) *(LAS f32x4*)(lds + L_CSQ + lw * 64 + (lane >> 4) * 16) = qcs;
            STAMP(6);
        }
        BAR();
        {
            const rsrc_t rsO = biased_rsrc(wo, 512 * 512 * 2);
#pragma unroll
            for (int g = 0; g < 4; ++g) {
                const int i = 4 * g + lw;
                const unsigned so = 16 * ((i + r2) & 15) * 1024 + 128 * c;
                dma4(lb + L_X + i * 4096, rsO, rsO, rsO, rsO, vrow, vrow, so, so + 8 * 1024, so + 256 * 1024, so + 264 * 1024);
            }
            ISSUE_STAGE(0, L_Y, 0, A_EARLY);
        }
#pragma unroll
        for (int g = 0; g < 2; ++g) {
            const f32x4 c0 = *(LAS f32x4*)(lds + L_CSQ + (16 + 8 * g + 4 * h2) * 4);
            const f32x4 v = *(LAS f32x4*)(lds + L_XCH + m * 4096 + g * 1024 + lane * 16);
#pragma unroll
            for (int e = 0; e < 4; ++e) yq[4 * g + e] = __builtin_fmaf(-mean1, c0[e], v[e]);
        }
    }
    STAMP(7);
    u32x4 qpos, qneg;
    {
        const u32x4 qf = {pack2(yq[0], yq[1]), pack2(yq[2], yq[3]), pack2(yq[4], yq[5]), pack2(yq[6], yq[7])};
        const f32x16 zero = {};
        const f32x16 pq = mfma(aphi, qf, zero);
        dual_softmax(pq, rstd1 * LOG2E, qpos, qneg);
    }
    STAMP(8);
    if (w == 0) poll_flags(ws, WS_F1 + j * 16 * 256, 16, tag, lane);
    BAR();
    STAMP(9);
    {
        const unsigned kb = WS_KVP + j * 16 * KVP_BYTES;
        float lo = 0.f, hi = 0.f;
        unsigned v[16], z[8];
#pragma unroll
        for (int i = 0; i < 16; ++i) v[i] = ld4(rsW, tid * 4, kb + ((i + 4 * q4) & 15) * KVP_BYTES);
        if (w == 0) {
#pragma unroll
            for (int i = 0; i < 8; ++i) z[i] = ld4(rsW, (512 + t) * 4 + h2 * 8 * KVP_BYTES, kb + i * KVP_BYTES);
        }
        PIN();
#pragma unroll
        for (int i = 0; i < 16; ++i) { lo += blo(v[i]); hi += bhi(v[i]); }
        if (w == 0) {
            float zl = 0.f, zh = 0.f;
#pragma unroll
            for (int i = 0; i < 8; ++i) { zl += blo(z[i]); zh += bhi(z[i]); }
            zl = halves_add(zl); zh = halves_add(zh);
            if (h2 == 0) *(LAS unsigned*)(lds + L_KVA + (512 + t) * 4) = pack2(zl, zh);
        }
        *(LAS unsigned*)(lds + L_KVA + tid * 4) = pack2(lo, hi);
    }
    BAR();
    STAMP(10);
    const int npass = has3 ? 3 : 2;
    const int m2 = w & 1, kh = (w >> 1) & 1;
    const int T2 = 4 * pP + 2 * half + m2;
    const unsigned C0 = 64 * c + 32 * kh;
    {
        const unsigned col = t < 16 ? t : 16;
        const u32x4 kp = *(LAS u32x4*)(lds + L_KVA + ((a * 4 + h2) * 17 + col) * 16);
        const u32x4 kn = *(LAS u32x4*)(lds + L_KVA + ((a * 4 + 2 + h2) * 17 + col) * 16);
        f32x16 o = {};
        o = mfma(kp, qpos, o);
        o = mfma(kn, qneg, o);
        const float r = __builtin_amdgcn_rcpf(o[8] + 1e-6f);
        st16(rsW, (u32x4){pack2(o[0] * r, o[1] * r), pack2(o[2] * r, o[3] * r), pack2(o[4] * r, o[5] * r), pack2(o[6] * r, o[7] * r)},
             WS_O + ((4 * p + m) * 32 + 2 * j + a) * 1024 + lane * 16, 0);
        WAITV(0);
        BAR();
        if (tid == 0) __hip_atomic_store((GAS u64*)(ws + WS_F2 + (p * 16 + j) * 256), tag, __ATOMIC_RELAXED, __HIP_MEMORY_SCOPE_AGENT);
        SBAR();
    }
    const unsigned trp = (4 * h2 + q) * 64 + blk * 32 + pp * 8;
    const unsigned t128 = (4 * h2 + q) * 128 + (q >> 1) * 64 + blk * 32 + pp * 8;

    if (w >= 4) {
        ISSUE_STAGE(0, L_Y, A_EARLY, 4);
        STAMP(11);
    }
    if (w >= 4) {
        const rsrc_t rsD = biased_rsrc(wd, 1376 * 512 * 2);
#define ISSUE_WD(pi, halfbase) do { _Pragma("unroll") for (int g_ = 0; g_ < ((pi) == 2 ? 3 : 4); ++g_) { const int i_ = 4 * g_ + lw; if ((pi) < 2 || i_ < 11) { \
                const int ii_ = (pi) == 2 ? (i_ + r4 >= 11 ? i_ + r4 - 11 : i_ + r4) : ((i_ + r2) & 15); \
                const unsigned sa_ = 16 * (((pi) == 2 ? 64 : 32 * (pi)) + ii_) * 1024 + 128 * c, sb_ = sa_ + 16 * ((pi) == 2 ? 11 : 16) * 1024; \
                dma4(lb + (halfbase) + i_ * 4096, rsD, rsD, rsD, rsD, vrow, vrow, sa_, sa_ + 8 * 1024, sb_, sb_ + 8 * 1024); } } } while (0)
        f32x4 cs0 = {}, cs1 = {};
        const unsigned gam = lane >> 4;
        const unsigned ca0 = (lw >> 1) * 2048 + (gam >> 1) * 4096 + (8 * (gam & 1) + q) * 128 + (((lw & 1) * 64) ^ (64 * (q >> 1))) + pp * 8;
#define COLSUM_STAGE(halfbase) do { unsigned cb_ = (halfbase) + ca0; asm volatile("" : "+v"(cb_)); \
            _Pragma("unroll") for (int g_ = 0; g_ < 4; ++g_) { const u32x4 f0_ = tr2(lds, cb_ + g_ * 16384, 512), f1_ = tr2(lds, cb_ + g_ * 16384 + 32, 512), \
                                                                         f2_ = tr2(lds, cb_ + g_ * 16384 + 8192, 512), f3_ = tr2(lds, cb_ + g_ * 16384 + 8192 + 32, 512); \
                PIN(); cs0 = mfma16(f0_, ones, cs0); cs1 = mfma16(f1_, ones, cs1); cs0 = mfma16(f2_, ones, cs0); cs1 = mfma16(f3_, ones, cs1); } } while (0)
        WAITV(0);
        BAR();
        STAMP(12);
        COLSUM_STAGE(L_Y);
        BAR();
        SBAR();
        ISSUE_STAGE(1, L_X, 0, B_EARLY);
        BAR();
        SBAR();
        ISSUE_STAGE(1, L_X, B_EARLY, 4);
        STAMP(13);
        BAR();
        WAITV(0);
        lsig(lb, 8); lwait(lds, 8, 4);
        COLSUM_STAGE(L_X);
        if ((lane & 15) == 0) {
            *(LAS f32x4*)(lds + L_CS3 + ((lw & 1) * 64 + (lw >> 1) * 32 + 4 * gam) * 4) = cs0;
            *(LAS f32x4*)(lds + L_CS3 + ((lw & 1) * 64 + (lw >> 1) * 32 + 16 + 4 * gam) * 4) = cs1;
        }
        BAR();
        if (npass == 3) {
            ISSUE_UNIT(u3, L_Y);
            WAITV(0);
            SBAR();
            ISSUE_WD(0, L_X);
            {
                unsigned cb = L_Y + (lw >> 1) * 1024 + (gam >> 1) * 2048 + (8 * (gam & 1) + q) * 64 + (lw & 1) * 32 + pp * 8;
                asm volatile("" : "+v"(cb));
                f32x4 c3 = {};
#pragma unroll
                for (int g = 0; g < 4; ++g) {
                    u32x4 f[4];
#pragma unroll
                    for (int e = 0; e < 4; ++e) f[e] = tr2(lds, cb + (4 * g + e) * 4096, 256);
                    PIN();
#pragma unroll
                    for (int e = 0; e < 4; ++e) c3 = mfma16(f[e], ones, c3);
                }
                if ((lane & 15) == 0) *(LAS f32x4*)(lds + L_CS3 + (128 + (lw >> 1) * 32 + (lw & 1) * 16 + 4 * gam) * 4) = c3;
            }
            BAR();
            WAITV(0);
            BAR();
            SBAR();
            ISSUE_WD(1, L_Y);
            WAITV(0);
            BAR();
            ISSUE_WD(2, L_X);
            WAITV(0);
            BAR();
        } else {
            ISSUE_WD(0, L_Y);
            SBAR();
            ISSUE_WD(1, L_X);
            WAITV(0);
            BAR();
            SBAR();
            BAR();
            ISSUE_WD(2, L_Y);
            WAITV(0);
            BAR();
        }
        BAR();
    } else {
        const unsigned vo = (32 * T2 + t) * 1024 + (C0 + 8 * h2) * 2;
        u32x4 xr0 = __builtin_amdgcn_raw_buffer_load_b128(rsX, vo, 0, 0);
        u32x4 xr1 = __builtin_amdgcn_raw_buffer_load_b128(rsX, vo + 32, 0, 0);
        PIN();
        STAMP(11);
        if (w == 0) poll_flags(ws, WS_F2 + pP * 16 * 256, 16, tag, lane);
        BAR();
        STAMP(12);
        if (b == 0 && w == 1 && lane == 0) __hip_atomic_store((GAS u64*)ws, tag, __ATOMIC_RELAXED, __HIP_MEMORY_SCOPE_AGENT);
        {
            u32x4 hres0, hres1;
            u32x4 of[16];
#pragma unroll
            for (int i = 0; i < 8; ++i) of[i] = ld16(rsW, lane * 16, WS_O + (T2 * 32 + 16 * kh + ((i + r2) & 15)) * 1024);
            PIN();
            f32x16 keep, snd = {};
            { auto r = __builtin_amdgcn_permlane32_swap(xr0[0], xr0[2], false, false); xr0[0] = r[0]; xr0[2] = r[1];
              r = __builtin_amdgcn_permlane32_swap(xr0[1], xr0[3], false, false); xr0[1] = r[0]; xr0[3] = r[1];
              r = __builtin_amdgcn_permlane32_swap(xr1[0], xr1[2], false, false); xr1[0] = r[0]; xr1[2] = r[1];
              r = __builtin_amdgcn_permlane32_swap(xr1[1], xr1[3], false, false); xr1[1] = r[0]; xr1[3] = r[1]; }
#pragma unroll
            for (int e = 0; e < 4; ++e) { keep[2 * e] = blo(xr0[e]); keep[2 * e + 1] = bhi(xr0[e]); keep[8 + 2 * e] = blo(xr1[e]); keep[8 + 2 * e + 1] = bhi(xr1[e]); }
            asm volatile("" : "+v"(keep));
            gemm2<16, 4096, 1024, 0>(lds, L_X + kh * 2048 + (t128 ^ (64 * kh)), L_X + kh * 2048 + (t128 ^ (64 * (kh ^ 1))), of, keep, snd, [&](int g) {
                if (g < 4) {
#pragma unroll
                    for (int e = 0; e < 2; ++e) of[8 + 2 * g + e] = ld16(rsW, lane * 16, WS_O + (T2 * 32 + 16 * kh + ((8 + 2 * g + e + r2) & 15)) * 1024);
                    SB();
                } });
#pragma unroll
            for (int g = 0; g < 4; ++g) *(LAS f32x4*)(lds + L_XCH + w * 4096 + g * 1024 + lane * 16) = (f32x4){snd[4 * g], snd[4 * g + 1], snd[4 * g + 2], snd[4 * g + 3]};
            STAMP(13);
            BAR();
#pragma unroll
            for (int g = 0; g < 4; ++g) {
                const f32x4 v = *(LAS f32x4*)(lds + L_XCH + (w ^ 2) * 4096 + g * 1024 + lane * 16);
#pragma unroll
                for (int e = 0; e < 4; ++e) keep[4 * g + e] += v[e];
            }
#pragma unroll
            for (int e = 0; e < 4; ++e) { hres0[e] = pack2(keep[2 * e], keep[2 * e + 1]); hres1[e] = pack2(keep[8 + 2 * e], keep[8 + 2 * e + 1]); }
            const unsigned hb = WS_H + (T2 * 32 + 4 * c + 2 * kh) * 1024;
            st16(rsW, hres0, lane * 16, hb);
            st16(rsW, hres1, lane * 16, hb + 1024);
            *(LAS u32x4*)(lds + L_HRES + w * 2048 + lane * 16) = hres0;
            *(LAS u32x4*)(lds + L_HRES + w * 2048 + 1024 + lane * 16) = hres1;
            f32x2 s1 = {0.f, 0.f}, s2 = {0.f, 0.f};
#pragma unroll
            for (int e = 0; e < 8; ++e) { const f32x2 v = {keep[2 * e], keep[2 * e + 1]}; s1 += v; s2 += v * v; }
            float su = s1[0] + s1[1], sq = s2[0] + s2[1];
            su = halves_add(su); sq = halves_add(sq);
            __builtin_amdgcn_raw_buffer_store_b32(asu(h2 ? sq : su), rsW, lane * 4 + WS_HST + (T2 * 16 + 2 * c + kh) * 256, 0, 16);
            SBAR();
            WAITV(0);
            BAR();
            if (tid == 0) __hip_atomic_store((GAS u64*)(ws + WS_F3 + (pP * 16 + jP) * 256), tag, __ATOMIC_RELAXED, __HIP_MEMORY_SCOPE_AGENT);
            SBAR();
        }
        STAMP(14);
        if (w == 0) poll_flags(ws, WS_F3 + p * 16 * 256, 16, tag, lane);
        BAR();
        STAMP(15);
        {
            const int T = 4 * p + w;
            u32x4 hf[32];
            float sv[16];
#pragma unroll
            for (int i = 0; i < 16; ++i) sv[i] = asf(ld4(rsW, lane * 4, WS_HST + (T * 16 + i) * 256));
#pragma unroll
            for (int s = 0; s < 8; ++s) hf[s] = ld16(rsW, lane * 16, WS_H + (T * 32 + ((s + r3) & 31)) * 1024);
            PIN();
            float mean, r2;
            auto act_out = [&](const f32x16& ag, const f32x16& au, int cs, unsigned ks) {
                float y[16];
#pragma unroll
                for (int g = 0; g < 4; ++g) {
                    const f32x4 cg = *(LAS f32x4*)(lds + L_CS3 + (cs * 64 + 8 * g + 4 * h2) * 4), cu = *(LAS f32x4*)(lds + L_CS3 + (cs * 64 + 32 + 8 * g + 4 * h2) * 4);
#pragma unroll
                    for (int e = 0; e < 4; ++e) y[4 * g + e] = (__builtin_fmaf(-mean, cg[e], ag[4 * g + e]) * r2) * __builtin_fmaf(-mean, cu[e], au[4 * g + e]);
                }
                const unsigned ab = WS_ACT + (T * 86 + ks) * 1024;
                st16(rsW, (u32x4){pack2(y[0], y[1]), pack2(y[2], y[3]), pack2(y[4], y[5]), pack2(y[6], y[7])}, lane * 16, ab);
                st16(rsW, (u32x4){pack2(y[8], y[9]), pack2(y[10], y[11]), pack2(y[12], y[13]), pack2(y[14], y[15])}, lane * 16, ab + 1024);
            };
            {
                f32x16 g0 = {}, g1 = {}, u0 = {}, u1 = {};
                gemm4<0>(lds, L_Y + t128, L_Y + (t128 ^ 64), hf, g0, g1, u0, u1, [&](int i) {
                    if (i < 8) hf[8 + i] = ld16(rsW, lane * 16, WS_H + (T * 32 + ((8 + i + r3) & 31)) * 1024);
                    else { hf[2 * i] = ld16(rsW, lane * 16, WS_H + (T * 32 + ((2 * i + r3) & 31)) * 1024); hf[2 * i + 1] = ld16(rsW, lane * 16, WS_H + (T * 32 + ((2 * i + 1 + r3) & 31)) * 1024); }
                    if (i == 1) {
                        float tot = 0.f;
#pragma unroll
                        for (int k = 0; k < 16; ++k) tot += sv[k];
                        const u32x2 r = both_halves(tot);
                        mean = asf(r[0]) * (1.0f / 512.0f);
                        const float var = fmaxf(asf(r[1]) * (1.0f / 512.0f) - mean * mean, 0.f);
                        r2 = __builtin_amdgcn_rcpf(var + 1e-5f);
                        asm volatile("" : "+v"(mean), "+v"(r2));
                    }
                    SB(); });
                BAR();
                STAMP(16);
                gemm4<16>(lds, L_X + t128, L_X + (t128 ^ 64), hf, g0, g1, u0, u1, nohook);
                STAMP(17);
                act_out(g0, u0, 0, 4 * j);
                act_out(g1, u1, 1, 4 * j + 2);
                SBAR();
            }
            if (npass == 3) {
                f32x16 ag = {}, au = {};
                gemm2<32, 2048, 512, 0>(lds, L_Y + trp, L_Y + 1024 + trp, hf, ag, au, [&](int g) {
                    if (g == 5) { WAITV(0); arrive_publish(lds, 3, 4, ws, WS_F4 + (p * 16 + j) * 256, tag, lane); PIN(); } });
                BAR();
                STAMP(18);
                act_out(ag, au, 2, 2 * u3);
                WAITV(0); arrive_publish(lds, 4, 4, ws, WS_F4 + 0x10000 + (p * 16 + j) * 256, tag, lane);
            } else {
                WAITV(0); arrive_publish(lds, 3, 4, ws, WS_F4 + (p * 16 + j) * 256, tag, lane);
            }
        }
        STAMP(19);
        {
            f32x16 a0, a1 = {};
            {
                const u32x4 hres0 = *(LAS u32x4*)(lds + L_HRES + w * 2048 + lane * 16), hres1 = *(LAS u32x4*)(lds + L_HRES + w * 2048 + 1024 + lane * 16);
#pragma unroll
                for (int e = 0; e < 4; ++e) { a0[2 * e] = blo(hres0[e]); a0[2 * e + 1] = bhi(hres0[e]); a0[8 + 2 * e] = blo(hres1[e]); a0[8 + 2 * e + 1] = bhi(hres1[e]); }
                asm volatile("" : "+v"(a0));
            }
            const unsigned tk = kh * 2048 + (t128 ^ (64 * kh)), ts = kh * 2048 + (t128 ^ (64 * (kh ^ 1)));
            const unsigned h0 = npass == 3 ? L_X : L_Y, h1 = npass == 3 ? L_Y : L_X;
            if (w == 0) poll_flags(ws, WS_F4 + pP * 16 * 256, 16, tag, lane);
            BAR();
            STAMP(20);
            u32x4 af[43];
            auto ld_act = [&](int i) {
                const int ks = i < 32 ? 32 * (i >> 4) + kh * 16 + ((i + r2) & 15) : 64 + kh * 11 + (i - 32 + r4 >= 11 ? i - 32 + r4 - 11 : i - 32 + r4);
                af[i] = ld16(rsW, lane * 16, WS_ACT + (T2 * 86 + ks) * 1024); };
#pragma unroll
            for (int i = 0; i < 8; ++i) ld_act(i);
            SBAR();
            gemm2<16, 4096, 1024, 0>(lds, h0 + tk, h0 + ts, af, a0, a1, [&](int g) { ld_act(8 + 2 * g); ld_act(9 + 2 * g); SB(); });
            STAMP(21);
            if (w == 0) poll_flags3(ws, WS_F4 + 0x10000 + pP * 16 * 256, tag, lane);
            BAR();
            STAMP(22);
            gemm2<16, 4096, 1024, 16>(lds, h1 + tk, h1 + ts, af, a0, a1, [&](int g) { ld_act(24 + 2 * g); ld_act(25 + 2 * g); SB(); });
            BAR();
            STAMP(23);
            gemm2<11, 4096, 1024, 32>(lds, h0 + tk, h0 + ts, af, a0, a1, [&](int g) { if (g < 2) { ld_act(40 + 2 * g < 43 ? 40 + 2 * g : 42); if (g == 0) ld_act(41); SB(); } });
#pragma unroll
            for (int g = 0; g < 4; ++g) *(LAS f32x4*)(lds + L_XCH + w * 4096 + g * 1024 + lane * 16) = (f32x4){a1[4 * g], a1[4 * g + 1], a1[4 * g + 2], a1[4 * g + 3]};
            BAR();
            STAMP(24);
#pragma unroll
            for (int g = 0; g < 4; ++g) {
                const f32x4 v = *(LAS f32x4*)(lds + L_XCH + (w ^ 2) * 4096 + g * 1024 + lane * 16);
#pragma unroll
                for (int e = 0; e < 4; ++e) a0[4 * g + e] += v[e];
            }
            u32x4 o0, o1;
#pragma unroll
            for (int e = 0; e < 4; ++e) { o0[e] = pack2(a0[2 * e], a0[2 * e + 1]); o1[e] = pack2(a0[8 + 2 * e], a0[8 + 2 * e + 1]); }
            { auto r = __builtin_amdgcn_permlane32_swap(o0[0], o0[2], false, false); o0[0] = r[0]; o0[2] = r[1];
              r = __builtin_amdgcn_permlane32_swap(o0[1], o0[3], false, false); o0[1] = r[0]; o0[3] = r[1];
              r = __builtin_amdgcn_permlane32_swap(o1[0], o1[2], false, false); o1[0] = r[0]; o1[2] = r[1];
              r = __builtin_amdgcn_permlane32_swap(o1[1], o1[3], false, false); o1[1] = r[0]; o1[3] = r[1]; }
            const rsrc_t rsOut = __builtin_amdgcn_make_buffer_rsrc((void*)out, 0, 2048 * 512 * 2, 0x00020000);
            int tz = threadIdx.x; asm volatile("" : "+v"(tz));
            const unsigned vz = (32 * T2 + (tz & 31)) * 1024 + (C0 + 8 * ((tz >> 5) & 1)) * 2;
            __builtin_amdgcn_raw_buffer_store_b128(o0, rsOut, vz, 0, 0);
            __builtin_amdgcn_raw_buffer_store_b128(o1, rsOut, vz + 32, 0, 0);
        }
        STAMP(25); CSTAMP(31);
    }
}

extern "C" void kernel_launch(void* const* d_in, const int* in_sizes, int n_in, void* d_out, int out_size, void* d_ws, size_t ws_size, hipStream_t stream) {
    (void)in_sizes; (void)n_in; (void)out_size; (void)ws_size;
    (void)hipFuncSetAttribute((const void*)hedgehog_block, hipFuncAttributeMaxDynamicSharedMemorySize, LDS_BYTES);
    hedgehog_block<<<256, 512, LDS_BYTES, stream>>>((const bf16_t*)d_in[0], (const bf16_t*)d_in[1], (const bf16_t*)d_in[2], (const bf16_t*)d_in[3],
                                                    (const bf16_t*)d_in[5], (const bf16_t*)d_in[6], (const bf16_t*)d_in[7], (bf16_t*)d_out, (char*)d_ws);
}
```

```cpp
#include <hip/hip_runtime.h>
#include <hip/hip_bf16.h>

#define LAS __attribute__((address_space(3)))
#define GAS __attribute__((address_space(1)))
#define DI __device__ __forceinline__
typedef unsigned short bf16_t;
typedef unsigned u32x4 __attribute__((ext_vector_type(4)));
typedef unsigned u32x2 __attribute__((ext_vector_type(2)));
typedef short s16x4 __attribute__((ext_vector_type(4)));
typedef __bf16 bf16x8 __attribute__((ext_vector_type(8)));
typedef __bf16 bf16x2 __attribute__((ext_vector_type(2)));
typedef float f32x16 __attribute__((ext_vector_type(16)));
typedef float f32x4 __attribute__((ext_vector_type(4)));
typedef float f32x2 __attribute__((ext_vector_type(2)));
typedef unsigned long long u64;
typedef __amdgpu_buffer_rsrc_t rsrc_t;

constexpr unsigned WS_F1 = 0x10000, WS_F2 = 0x20000, WS_F3 = 0x30000, WS_F4 = 0x40000;
constexpr unsigned WS_KVP = 0x80000, KVP_BYTES = 2176;
constexpr unsigned WS_HST = 0x110000;
constexpr unsigned WS_XST = 0x160000;
constexpr unsigned WS_O = 0x200000, WS_H = 0x400000, WS_ACT = 0x600000, WS_PROF = 0xC00000, WS_BYTES = 0x1000000;
constexpr unsigned L_X = 0, L_Y = 65536, L_XCH = 131072, L_MISC = 147456;
constexpr unsigned L_STAT = L_MISC, L_CS1 = L_MISC + 1024, L_CSQ = L_MISC + 1280, L_WPHI = L_MISC + 1536, L_KVA = L_MISC + 2560, L_CS3 = L_MISC + 4736, L_CNT = L_MISC + 5504, L_TAG = L_MISC + 6016, L_DUMMY = L_MISC + 6144, L_HRES = L_MISC + 8192;
constexpr unsigned L_WQ = 65536, L_PHI = 98304, L_VIMG = 114688  , SS_BYTES = 24576;
constexpr unsigned LDS_BYTES = 163840;
constexpr unsigned SPIN_CAP = 40000;
constexpr float LOG2E = 1.4426950408889634f;

DI float asf(unsigned v) { return __builtin_bit_cast(float, v); }
DI unsigned asu(float v) { return __builtin_bit_cast(unsigned, v); }
DI unsigned pack2(float lo, float hi) { bf16x2 v = {(__bf16)lo, (__bf16)hi}; return __builtin_bit_cast(unsigned, v); }
DI float blo(unsigned v) { return __builtin_bit_cast(float, v << 16); }
DI float bhi(unsigned v) { return __builtin_bit_cast(float, v & 0xffff0000u); }
DI f32x16 mfma(u32x4 a, u32x4 b, f32x16 c) { return __builtin_amdgcn_mfma_f32_32x32x16_bf16(__builtin_bit_cast(bf16x8, a), __builtin_bit_cast(bf16x8, b), c, 0, 0, 0); }
DI f32x4 mfma16(u32x4 a, u32x4 b, f32x4 c) { return __builtin_amdgcn_mfma_f32_16x16x32_bf16(__builtin_bit_cast(bf16x8, a), __builtin_bit_cast(bf16x8, b), c, 0, 0, 0); }
DI u32x4 tr2(LAS char* lds, unsigned off, unsigned second) {
    const s16x4 lo = __builtin_amdgcn_ds_read_tr16_b64_v4i16((LAS s16x4*)(lds + off));
    const s16x4 hi = __builtin_amdgcn_ds_read_tr16_b64_v4i16((LAS s16x4*)(lds + off + second));
    return __builtin_bit_cast(u32x4, __builtin_shufflevector(lo, hi, 0, 1, 2, 3, 4, 5, 6, 7));
}
DI void dma16(const rsrc_t rs, unsigned ldsaddr, unsigned voff, unsigned soff) {
    asm volatile("s_mov_b32 m0, %0\n\ts_nop 0\n\tbuffer_load_dwordx4 %1, %2, %3 offen lds" ::"s"(ldsaddr), "v"(voff), "s"(rs), "s"(soff) : "memory");
}
constexpr unsigned BIAS = 4096;
DI rsrc_t biased_rsrc(const void* p, unsigned bytes) { return __builtin_amdgcn_make_buffer_rsrc((void*)((const char*)p - BIAS), 0, bytes + BIAS, 0x00020000); }
DI void dma4(unsigned ldsaddr, const rsrc_t ra, const rsrc_t rb, const rsrc_t rc, const rsrc_t rd, unsigned va, unsigned vb, unsigned s0, unsigned s1, unsigned s2, unsigned s3) {
    asm volatile("s_mov_b32 m0, %0\n\ts_nop 0\n\t"
                 "buffer_load_dwordx4 %5, %1, %7 offen lds\n\tbuffer_load_dwordx4 %6, %2, %8 offen offset:1024 lds\n\t"
                 "buffer_load_dwordx4 %5, %3, %9 offen offset:2048 lds\n\tbuffer_load_dwordx4 %6, %4, %10 offen offset:3072 lds"
                 ::"s"(ldsaddr), "s"(ra), "s"(rb), "s"(rc), "s"(rd), "v"(va), "v"(vb), "s"(s0 + BIAS), "s"(s1 + BIAS - 1024), "s"(s2 + BIAS - 2048), "s"(s3 + BIAS - 3072) : "memory");
}
DI void dma2(unsigned ldsaddr, const rsrc_t r, unsigned va, unsigned vb, unsigned s0, unsigned s1) {
    asm volatile("s_mov_b32 m0, %0\n\ts_nop 0\n\tbuffer_load_dwordx4 %2, %1, %4 offen lds\n\tbuffer_load_dwordx4 %3, %1, %5 offen offset:1024 lds"
                 ::"s"(ldsaddr), "s"(r), "v"(va), "v"(vb), "s"(s0 + BIAS), "s"(s1 + BIAS - 1024) : "memory");
}
#define WAITV(n) asm volatile("s_waitcnt vmcnt(" #n ")" ::: "memory")
#define PIN() do { asm volatile("" ::: "memory"); __builtin_amdgcn_sched_barrier(0); } while (0)
template <int N> DI void waitv() { asm volatile("s_waitcnt vmcnt(%0)" ::"n"(N) : "memory"); }
#define B_EARLY 1
#define PD1 0
#define PD2 4
#define PD3 4
#define PRIO_C 0
#define K0 3
#define R1MODE 2
#define STAMP2_SS(ss) do { if (ss == 0) STAMP2(0); if (ss == 1) STAMP2(1); if (ss == 2) STAMP2(2); if (ss == 3) STAMP2(3); if (ss == 4) STAMP2(4); if (ss == 5) STAMP2(5); if (ss == 6) STAMP2(6); if (ss == 7) STAMP2(7); } while (0)
#define SBAR() asm volatile("s_barrier" ::: "memory")
#define BAR() asm volatile("s_waitcnt lgkmcnt(0)\n\ts_barrier" ::: "memory")
DI u32x2 both_halves(float v) { const unsigned a = asu(v); const auto r = __builtin_amdgcn_permlane32_swap(a, a, false, false); return (u32x2){r[0], r[1]}; }
DI float halves_add(float v) { const u32x2 r = both_halves(v); return asf(r[0]) + asf(r[1]); }
DI float halves_max(float v) { const u32x2 r = both_halves(v); return fmaxf(asf(r[0]), asf(r[1])); }
DI float halves_min(float v) { const u32x2 r = both_halves(v); return fminf(asf(r[0]), asf(r[1])); }
DI float dpp_ror4(float v) { return asf(__builtin_amdgcn_update_dpp(0, (int)asu(v), 0x124, 0xf, 0xf, true)); }
DI float dpp_ror8(float v) { return asf(__builtin_amdgcn_update_dpp(0, (int)asu(v), 0x128, 0xf, 0xf, true)); }
DI float rows16_add(float v) { const unsigned a = asu(v); const auto r = __builtin_amdgcn_permlane16_swap(a, a, false, false); return asf(r[0]) + asf(r[1]); }
DI float stride4_add(float v) { v += dpp_ror4(v); v += dpp_ror8(v); v = rows16_add(v); return halves_add(v); }
DI float quad_add(float v) {
    v += __builtin_bit_cast(float, __builtin_amdgcn_update_dpp(0, __builtin_bit_cast(int, v), 0xB1, 0xf, 0xf, true));
    v += __builtin_bit_cast(float, __builtin_amdgcn_update_dpp(0, __builtin_bit_cast(int, v), 0x4E, 0xf, 0xf, true));
    return v;
}
DI void st16(const rsrc_t rs, u32x4 v, unsigned voff, unsigned soff) { __builtin_amdgcn_raw_buffer_store_b128(v, rs, voff + soff, 0, 16); }
DI u32x4 ld16(const rsrc_t rs, unsigned voff, unsigned soff) { return __builtin_amdgcn_raw_buffer_load_b128(rs, voff, soff, 16); }
DI unsigned ld4(const rsrc_t rs, unsigned voff, unsigned soff) { return __builtin_amdgcn_raw_buffer_load_b32(rs, voff, soff, 16); }

DI void poll_flags3(const char* ws, unsigned off, u64 tag, int lane) {
    const int l = lane < 11 ? lane : 10;
    const GAS u64* f = (const GAS u64*)(ws + off) + (l < 6 ? l : l + 2) * 32;
#pragma unroll 1
    for (unsigned spin = 0; spin < SPIN_CAP; ++spin) {
        const u64 v = __hip_atomic_load(f, __ATOMIC_RELAXED, __HIP_MEMORY_SCOPE_AGENT);
        if (__all(v == tag)) break;
        __builtin_amdgcn_s_sleep(1);
    }
    asm volatile("" ::: "memory");
}
DI void nohook(int) {}
template <int DELAY = 0>
DI void poll_flags(const char* ws, unsigned off, int n, u64 tag, int lane) {
#pragma unroll
    for (int i = 0; i < DELAY; ++i) __builtin_amdgcn_s_sleep(2);
    const GAS u64* f = (const GAS u64*)(ws + off) + (lane < n ? lane : n - 1) * 32;
#pragma unroll 1
    for (unsigned spin = 0; spin < SPIN_CAP; ++spin) {
        const u64 v = __hip_atomic_load(f, __ATOMIC_RELAXED, __HIP_MEMORY_SCOPE_AGENT);
        if (__all(v == tag)) break;
        __builtin_amdgcn_s_sleep(1);
    }
    asm volatile("" ::: "memory");
}
DI void arrive_publish(LAS char* lds, int cnt, unsigned n, char* ws, unsigned flag_off, u64 tag, int lane) {
    if (lane == 0) {
        const unsigned old = __hip_atomic_fetch_add((LAS unsigned*)(lds + L_CNT) + cnt, 1u, __ATOMIC_RELAXED, __HIP_MEMORY_SCOPE_WORKGROUP);
        if (old == n - 1) __hip_atomic_store((GAS u64*)(ws + flag_off), tag, __ATOMIC_RELAXED, __HIP_MEMORY_SCOPE_AGENT);
    }
}
DI void lsig(unsigned lb, int idx) {
    const unsigned a = lb + L_CNT + idx * 4, one = 1;
    asm volatile("s_mov_b64 exec, 1\n\tds_add_u32 %0, %1\n\ts_mov_b64 exec, -1" ::"v"(a), "v"(one) : "memory");
}
DI void lwait(LAS char* lds, int idx, unsigned n) {
    const volatile LAS unsigned* c = (const volatile LAS unsigned*)(lds + L_CNT) + idx;
#pragma unroll 1
    for (unsigned spin = 0; spin < 64 * SPIN_CAP; ++spin) {
        if ((unsigned)__builtin_amdgcn_readfirstlane(*c) >= n) break;
        __builtin_amdgcn_s_sleep(1);
    }
    asm volatile("" ::: "memory");
}
DI void dual_softmax(const f32x16& p, float sl, u32x4& pos, u32x4& neg) {
    float mx = fmaxf(fmaxf(fmaxf(p[0], p[1]), fmaxf(p[2], p[3])), fmaxf(fmaxf(p[4], p[5]), fmaxf(p[6], p[7])));
    float mn = fminf(fminf(fminf(p[0], p[1]), fminf(p[2], p[3])), fminf(fminf(p[4], p[5]), fminf(p[6], p[7])));
    mx = halves_max(mx); mn = halves_min(mn);
    const float cx = -mx * sl, cn = mn * sl;
    f32x2 ep[4], en[4];
#pragma unroll
    for (int i = 0; i < 4; ++i) {
        const f32x2 v = {p[2 * i], p[2 * i + 1]};
        const f32x2 u = v * sl + cx, d = v * (-sl) + cn;
        ep[i] = (f32x2){__builtin_amdgcn_exp2f(u[0]), __builtin_amdgcn_exp2f(u[1])};
        en[i] = (f32x2){__builtin_amdgcn_exp2f(d[0]), __builtin_amdgcn_exp2f(d[1])};
    }
    const f32x2 sp = (ep[0] + ep[1]) + (ep[2] + ep[3]), sn = (en[0] + en[1]) + (en[2] + en[3]);
    const float rp = __builtin_amdgcn_rcpf(halves_add(sp[0] + sp[1])), rn = __builtin_amdgcn_rcpf(halves_add(sn[0] + sn[1]));
#pragma unroll
    for (int i = 0; i < 4; ++i) { const f32x2 a = ep[i] * rp, b = en[i] * rn; pos[i] = pack2(a[0], a[1]); neg[i] = pack2(b[0], b[1]); }
}

#define SB() __builtin_amdgcn_sched_barrier(0)
template <int N, int STRIDE, int SECOND, int OFF, int NA, class Hook>
DI void gemm2(LAS char* lds, unsigned b0, unsigned b1, const u32x4 (&act)[NA], f32x16& acc0, f32x16& acc1, Hook hook) {
    u32x4 fb[2][4];
    asm volatile("" : "+v"(b0), "+v"(b1));
    constexpr int NG = (N + 1) / 2;
#pragma unroll
    for (int e = 0; e < 2; ++e) { fb[0][2 * e] = tr2(lds, b0 + e * STRIDE, SECOND); fb[0][2 * e + 1] = tr2(lds, b1 + e * STRIDE, SECOND); }
    SB();
#pragma unroll
    for (int g = 0; g < NG; ++g) {
#pragma unroll
        for (int e = 0; e < 2; ++e) {
            const int i = 2 * g + e, n = i + 2;
            if (i < N) acc0 = mfma(fb[g & 1][2 * e], act[OFF + i], acc0);
            if (n < N) fb[(g + 1) & 1][2 * e] = tr2(lds, b0 + n * STRIDE, SECOND);
            SB();
            if (i < N) acc1 = mfma(fb[g & 1][2 * e + 1], act[OFF + i], acc1);
            if (n < N) fb[(g + 1) & 1][2 * e + 1] = tr2(lds, b1 + n * STRIDE, SECOND);
            SB();
        }
        hook(g);
    }
    asm volatile("" : "+v"(acc0), "+v"(acc1));
}
template <int OFF, class Hook>
DI void gemm4(LAS char* lds, unsigned b0, unsigned b1, const u32x4 (&act)[32], f32x16& g0, f32x16& g1, f32x16& u0, f32x16& u1, Hook hook) {
    u32x4 fb[2][4];
    asm volatile("" : "+v"(b0), "+v"(b1));
    fb[0][0] = tr2(lds, b0, 1024); fb[0][1] = tr2(lds, b1, 1024); fb[0][2] = tr2(lds, b0 + 2048, 1024); fb[0][3] = tr2(lds, b1 + 2048, 1024);
    SB();
#pragma unroll
    for (int i = 0; i < 16; ++i) {
        const int o = (i + 1) * 4096;
        g0 = mfma(fb[i & 1][0], act[OFF + i], g0);
        if (i < 15) fb[(i + 1) & 1][0] = tr2(lds, b0 + o, 1024);
        SB();
        g1 = mfma(fb[i & 1][1], act[OFF + i], g1);
        if (i < 15) fb[(i + 1) & 1][1] = tr2(lds, b1 + o, 1024);
        SB();
        u0 = mfma(fb[i & 1][2], act[OFF + i], u0);
        if (i < 15) fb[(i + 1) & 1][2] = tr2(lds, b0 + o + 2048, 1024);
        SB();
        u1 = mfma(fb[i & 1][3], act[OFF + i], u1);
        if (i < 15) fb[(i + 1) & 1][3] = tr2(lds, b1 + o + 2048, 1024);
        SB();
        hook(i);
    }
    asm volatile("" : "+v"(g0), "+v"(g1), "+v"(u0), "+v"(u1));
}
DI void touch_lines(const rsrc_t rs, unsigned ldsaddr, unsigned voff, unsigned soff) {
    asm volatile("s_mov_b32 m0, %0\n\ts_nop 0\n\tbuffer_load_dword %1, %2, %3 offen lds" ::"s"(ldsaddr), "v"(voff), "s"(rs), "s"(soff) : "memory");
}

#define STAMP(i) do {} while (0)
#define CSTAMP(i) do {} while (0)
#define STAMP2(i) do {} while (0)

__global__ void __launch_bounds__(512, 2)
hedgehog_block(const bf16_t* __restrict__ x, const bf16_t* __restrict__ wqkv, const bf16_t* __restrict__ wphi, const bf16_t* __restrict__ wo,
               const bf16_t* __restrict__ wg, const bf16_t* __restrict__ wu, const bf16_t* __restrict__ wd, bf16_t* __restrict__ out, char* __restrict__ ws) {
    extern __shared__ __attribute__((aligned(16))) char smem[];
    LAS char* lds = (LAS char*)smem;
    const unsigned lb = (unsigned)(size_t)lds;
    const int tid = threadIdx.x, lane = tid & 63;
    const int w = __builtin_amdgcn_readfirstlane(tid >> 6);
    const int b = blockIdx.x, xcd = b & 7, slot = b >> 3;
    const int p = 4 * (xcd >> 1) + (slot >> 3), j = 8 * (xcd & 1) + (slot & 7);
    const int pP = 2 * xcd + (slot >> 4), jP = slot & 15, c = slot & 7, half = (slot >> 3) & 1;
    const bool has3 = (j & 7) < (j < 8 ? 6 : 5);
    const int u3 = 32 + (j < 8 ? j : j - 2);
    const int t = lane & 31, h2 = lane >> 5, q = (lane & 15) >> 2, pp = lane & 3, blk = (lane >> 4) & 1;
    const int m = w & 3, a = w >> 2, lw = w & 3;
    const int q4 = slot >> 3;
    const int r1 = R1MODE == 0 ? 0 : R1MODE == 1 ? ((c + 2 * q4) & 7) : R1MODE == 2 ? 4 * (q4 & 1) : 2 * q4, r2 = (2 * c + 4 * q4) & 15, r3 = (4 * c + 8 * q4) & 31, r4 = (c + 3 * q4) % 11;
    STAMP(0); CSTAMP(30);
    const rsrc_t rsX = __builtin_amdgcn_make_buffer_rsrc((void*)x, 0, 2048 * 512 * 2, 0x00020000);
    const rsrc_t rsQ = __builtin_amdgcn_make_buffer_rsrc((void*)wqkv, 0, 512 * 1536 * 2, 0x00020000);
    const rsrc_t rsW = __builtin_amdgcn_make_buffer_rsrc((void*)ws, 0, WS_BYTES, 0x00020000);
    u64 tag = 0;
#define UNIFORM64(v) (((u64)(unsigned)__builtin_amdgcn_readfirstlane((unsigned)((v) >> 32)) << 32) | (unsigned)__builtin_amdgcn_readfirstlane((unsigned)(v)))
    const u32x4 ones = {0x3F803F80u, 0x3F803F80u, 0x3F803F80u, 0x3F803F80u};
    const unsigned c128 = ((lane & 7) ^ (4 * ((lane >> 4) & 1))) * 16;
    const unsigned vrow = (lane >> 3) * 1024 + c128;
    const unsigned trn = (8 * h2 + q) * 64 + pp * 8;

    const rsrc_t rsG = biased_rsrc(wg, 512 * 1376 * 2), rsU = biased_rsrc(wu, 512 * 1376 * 2);
    const unsigned vgu = (lane >> 2) * 2752 + (lane & 3) * 16, vgu8 = (lane >> 3) * 2752 + c128;
#define ISSUE_STAGE(st, halfbase, g0, g1) do { _Pragma("unroll") for (int g_ = (g0); g_ < (g1); ++g_) { const int i_ = 4 * g_ + lw; const unsigned so_ = 16 * ((16 * (st) + i_ + r3) & 31) * 2752 + 128 * j; \
            dma4(lb + (halfbase) + i_ * 4096, rsG, rsG, rsU, rsU, vgu8, vgu8, so_, so_ + 8 * 2752, so_, so_ + 8 * 2752); } } while (0)
#define ISSUE_UNIT(u, halfbase) do { _Pragma("unroll") for (int g_ = 0; g_ < 4; ++g_) { const int s_ = 2 * (4 * g_ + lw); \
            const unsigned sa_ = 16 * ((s_ + r3) & 31) * 2752 + 64 * (u), sb_ = 16 * ((s_ + 1 + r3) & 31) * 2752 + 64 * (u); \
            dma4(lb + (halfbase) + s_ * 2048, rsG, rsU, rsG, rsU, vgu, vgu, sa_, sa_, sb_, sb_); } } while (0)
    if (w == 3) {
        u64 pc; asm volatile("s_getpc_b64 %0" : "=s"(pc));
        const rsrc_t rsC = __builtin_amdgcn_make_buffer_rsrc((void*)(pc & ~127ull), 0, 36864, 0x00020000);
        touch_lines(rsC, lb + L_DUMMY, (slot * 9 + (lane < 9 ? lane : 0)) * 128, 0);
    }
    u32x4 xf[32];
    float yk[16];
    float mean1, rstd1;
    if (w < 4) {
        {
            u64 epoch = __hip_atomic_load((const GAS u64*)ws, __ATOMIC_RELAXED, __HIP_MEMORY_SCOPE_AGENT);
            const unsigned R = 128 * p + 8 * j + 2 * w;
            const u32x4 v0 = __builtin_amdgcn_raw_buffer_load_b128(rsX, lane * 16, R * 1024, 0), v1 = __builtin_amdgcn_raw_buffer_load_b128(rsX, lane * 16, R * 1024 + 1024, 0);
            if (tid < 16) *(LAS unsigned*)(lds + L_CNT + tid * 4) = 0u;
            f32x2 a1 = {0.f, 0.f}, a2 = {0.f, 0.f}, b1 = {0.f, 0.f}, b2 = {0.f, 0.f};
#pragma unroll
            for (int e = 0; e < 4; ++e) {
                const f32x2 z0 = {blo(v0[e]), bhi(v0[e])}, z1 = {blo(v1[e]), bhi(v1[e])};
                a1 += z0; a2 += z0 * z0; b1 += z1; b2 += z1 * z1;
            }
            const float su0 = stride4_add(quad_add(a1[0] + a1[1])), sq0 = stride4_add(quad_add(a2[0] + a2[1]));
            const float su1 = stride4_add(quad_add(b1[0] + b1[1])), sq1 = stride4_add(quad_add(b2[0] + b2[1]));
            const float su = (lane & 1) ? su1 : su0, sq = (lane & 1) ? sq1 : sq0;
            const float mean = su * (1.0f / 512.0f), var = fmaxf(sq * (1.0f / 512.0f) - mean * mean, 0.f);
            const float rstd = __builtin_amdgcn_rsqf(var + 1e-5f);
            asm volatile("s_waitcnt vmcnt(0)" : "+v"(epoch)::"memory");
            tag = UNIFORM64(epoch) + 0x9E3779B97F4A7C15ull;
            if (tid == 0) *(LAS u64*)(lds + L_TAG) = tag;
            if (lane < 2) st16(rsW, (u32x4){asu(mean), asu(rstd), (unsigned)tag, (unsigned)(tag >> 32)}, (R + lane) * 16, WS_XST);
        }
        STAMP(1);
        const unsigned xa0 = (32 * m + t) * 128 + ((h2 ^ ((t >> 1) & 7)) * 16);
        const unsigned twA = 16384 + (blk ? 1024 + 32 : 0) + trn, twB = 16384 + (blk ? 1024 : 32) + trn;
        f32x16 accA = {}, accB = {};
        u32x4 fa[2][2], fb[2][2], xs;
        unsigned bA = twA, bB = twB, bx0 = xa0, bx1 = xa0 ^ 32, bx2 = xa0 ^ 64, bx3 = xa0 ^ 96;
        asm volatile("" : "+v"(bA), "+v"(bB), "+v"(bx0), "+v"(bx1), "+v"(bx2), "+v"(bx3));
#pragma unroll
        for (int ss = 0; ss <= 8; ++ss) {
            if (ss < 8) {
                BAR();
                STAMP2_SS(ss);
                if (ss == 0) STAMP(2);
                if (ss == 6) { xs = ld16(rsW, (128 * p + 32 * m + t) * 16, WS_XST); SB(); }
            }
#pragma unroll
            for (int e = 0; e < 2; ++e) {
                if (ss > 0) accA = mfma(fa[1][e], xf[4 * ss - 2 + e], accA);
                if (ss < 8) { xf[4 * ss + e] = *(LAS u32x4*)(lds + (e ? bx1 : bx0)); fa[0][e] = tr2(lds, bA + e * 2048, 256); }
                SB();
                if (ss > 0) accB = mfma(fb[1][e], xf[4 * ss - 2 + e], accB);
                if (ss < 8) fb[0][e] = tr2(lds, bB + e * 2048, 256);
                SB();
            }
            if (ss < 8) {
#pragma unroll
                for (int e = 0; e < 2; ++e) {
                    accA = mfma(fa[0][e], xf[4 * ss + e], accA);
                    xf[4 * ss + 2 + e] = *(LAS u32x4*)(lds + (e ? bx3 : bx2)); fa[1][e] = tr2(lds, bA + (2 + e) * 2048, 256);
                    SB();
                    accB = mfma(fb[0][e], xf[4 * ss + e], accB);
                    fb[1][e] = tr2(lds, bB + (2 + e) * 2048, 256);
                    SB();
                }
                if (ss < 7) {
                    const unsigned d = ss == 5 ? (unsigned)(-5 * (int)SS_BYTES) : SS_BYTES;
                    bA += d; bB += d; bx0 += d; bx1 += d; bx2 += d; bx3 += d;
                    asm volatile("" : "+v"(bA), "+v"(bB), "+v"(bx0), "+v"(bx1), "+v"(bx2), "+v"(bx3));
                }
            }
        }
        STAMP(3);
#pragma unroll
        for (int g = 0; g < 4; ++g) *(LAS f32x4*)(lds + L_XCH + m * 4096 + g * 1024 + lane * 16) = (f32x4){accB[4 * g], accB[4 * g + 1], accB[4 * g + 2], accB[4 * g + 3]};
#pragma unroll 1
        for (unsigned spin = 0; spin < SPIN_CAP; ++spin) {
            if (__all(xs[2] == (unsigned)tag && xs[3] == (unsigned)(tag >> 32))) break;
            __builtin_amdgcn_s_sleep(1);
            xs = ld16(rsW, (128 * p + 32 * m + t) * 16, WS_XST);
        }
        mean1 = asf(xs[0]); rstd1 = asf(xs[1]);
        if (h2 == 0) *(LAS f32x2*)(lds + L_STAT + (32 * m + t) * 8) = (f32x2){mean1, rstd1};
        BAR();
        STAMP(4);
#pragma unroll
        for (int g = 0; g < 4; ++g) {
            const f32x4 cs = *(LAS f32x4*)(lds + L_CS1 + (8 * g + 4 * h2) * 4);
#pragma unroll
            for (int e = 0; e < 4; ++e) { const float y = __builtin_fmaf(-mean1, cs[e], accA[4 * g + e]); yk[4 * g + e] = g < 2 ? y : y * rstd1; }
        }
    } else {
        const unsigned vx0 = (lane >> 3) * 1024 + (((lane & 7) ^ (lane >> 4)) * 16), vx1 = (lane >> 3) * 1024 + (((lane & 7) ^ (4 + (lane >> 4))) * 16);
        const unsigned vwk = (lane >> 2) * 3072 + (lane & 3) * 16, vwv = (lane >> 2) * 3072 + ((lane & 3) ^ 2) * 16;
        const rsrc_t rbX = biased_rsrc(x, 2048 * 512 * 2), rbQ = biased_rsrc(wqkv, 512 * 1536 * 2);
#define ISSUE_SS(ss, v) do { const unsigned sb_ = lb + ((ss) % 6) * SS_BYTES, ks_ = ((ss) + r1) & 7; \
            const unsigned sx_ = (128 * p + 32 * (v)) * 1024 + ks_ * 128, sw_ = (16 * (v) + 64 * ks_) * 3072 + (512 + 32 * j) * 2; \
            dma2(sb_ + 16384 + (v) * 2048, rbQ, vwk, vwv, sw_, sw_ + 1024); \
            dma4(sb_ + (v) * 4096, rbX, rbX, rbX, rbX, vx0, vx1, sx_, sx_ + 8 * 1024, sx_ + 16 * 1024, sx_ + 24 * 1024); } while (0)
        if (lw == 0) {
            const rsrc_t rsP = __builtin_amdgcn_make_buffer_rsrc((void*)wphi, 0, 32 * 256 * 2, 0x00020000);
            dma16(rsP, lb + L_WPHI, lane * 16, j * 1024);
        }
#pragma unroll
        for (int ss = 0; ss < K0; ++ss) ISSUE_SS(ss, lw);
        STAMP(1);
        f32x4 csacc = {};
        const unsigned tw = 16384 + ((lw & 1) ? 1024 + ((lw >> 1) ^ 1) * 32 : (lw >> 1) * 32) + (lane >> 5) * 2048 + (8 * ((lane >> 4) & 1) + q) * 64 + pp * 8;
        constexpr int NL = 8 - K0;
#define P1WAIT(s) (6 * ((((s) + K0 - 1 < 7 ? (s) + K0 - 1 : 7) - (s))) + ((s) > 4 ? 2 * ((s) - 4) : 0))
#pragma unroll
        for (int ss = 0; ss < 8; ++ss) {
            if (ss == 0) waitv<P1WAIT(0)>();
            if (ss == 1) waitv<P1WAIT(1)>();
            if (ss == 2) waitv<P1WAIT(2)>();
            if (ss == 3) waitv<P1WAIT(3)>();
            if (ss == 4) waitv<P1WAIT(4)>();
            if (ss == 5) waitv<P1WAIT(5)>();
            if (ss == 6) waitv<P1WAIT(6)>();
            if (ss == 7) waitv<P1WAIT(7)>();
            STAMP2_SS(ss);
            BAR();
            if (ss == 0) { const u64 tv = *(LAS u64*)(lds + L_TAG); tag = UNIFORM64(tv); STAMP(2); }
            unsigned bw = (ss % 6) * SS_BYTES + tw;
            asm volatile("" : "+v"(bw));
            const u32x4 f0 = tr2(lds, bw, 256), f1 = tr2(lds, bw + 4096, 256);
            SB();
            if (ss < NL) ISSUE_SS(K0 + ss, lw);
            if (ss == 6) {
#pragma unroll
                for (int i = 0; i < 4; ++i) *(LAS u32x4*)(lds + L_VIMG + (4 * lw + i) * 1024 + lane * 16) = ones;
            }
            if (ss >= 4) {
                const int pc = 8 * lw + 2 * (ss - 4);
                const unsigned so = 16 * ((pc + 4 * r1) & 31) * 3072 + 64 * j;
                dma2(lb + L_WQ + pc * 1024, rbQ, vwk, vwk, so, so + 16 * 3072);
            }
            csacc = mfma16(f0, ones, csacc);
            csacc = mfma16(f1, ones, csacc);
            SB();
        }
        if ((lane & 15) == 0) *(LAS f32x4*)(lds + L_CS1 + (lw >> 1) * 128 + (lw & 1) * 64 + (lane >> 4) * 16) = csacc;
        STAMP(3);
        BAR();
        STAMP(4);
        const f32x2 st = *(LAS f32x2*)(lds + L_STAT + (32 * m + t) * 8);
        mean1 = st[0]; rstd1 = st[1];
#pragma unroll
        for (int g = 0; g < 4; ++g) {
            const f32x4 cs = *(LAS f32x4*)(lds + L_CS1 + 128 + (8 * g + 4 * h2) * 4);
            const f32x4 v = *(LAS f32x4*)(lds + L_XCH + m * 4096 + g * 1024 + lane * 16);
#pragma unroll
            for (int e = 0; e < 4; ++e) { const float y = __builtin_fmaf(-mean1, cs[e], v[e]); yk[4 * g + e] = g < 2 ? y : y * rstd1; }
        }
    }
    const unsigned imgA = L_PHI + a * 8192 + (32 * m + t) * 64 + ((h2 ^ ((t >> 1) & 7)) * 8);
    const u32x4 aphi = tr2(lds, L_WPHI + a * 512 + (4 * h2 + q) * 32 + pp * 8, 256);
    {
        const u32x4 kf = {pack2(yk[0], yk[1]), pack2(yk[2], yk[3]), pack2(yk[4], yk[5]), pack2(yk[6], yk[7])};
        const f32x16 zero = {};
        const f32x16 pk = mfma(aphi, kf, zero);
        *(LAS u32x2*)(lds + imgA + (L_VIMG - L_PHI)) = (u32x2){pack2(yk[8], yk[9]), pack2(yk[10], yk[11])};
        *(LAS u32x2*)(lds + (imgA ^ 16) + (L_VIMG - L_PHI)) = (u32x2){pack2(yk[12], yk[13]), pack2(yk[14], yk[15])};
        u32x4 pos, neg;
        dual_softmax(pk, rstd1 * LOG2E, pos, neg);
        *(LAS u32x2*)(lds + imgA) = (u32x2){pos[0], pos[1]};
        *(LAS u32x2*)(lds + (imgA ^ 16)) = (u32x2){pos[2], pos[3]};
        *(LAS u32x2*)(lds + (imgA ^ 32)) = (u32x2){neg[0], neg[1]};
        *(LAS u32x2*)(lds + (imgA ^ 48)) = (u32x2){neg[2], neg[3]};
    }
    if (w >= 4) waitv<0>();
    BAR();
    STAMP(5);
    float yq[8];
    if (w < 4) {
        unsigned tq = L_WQ + trn + blk * 32;
        asm volatile("" : "+v"(tq));
        f32x16 qa = {};
        u32x4 qb[2][4];
#pragma unroll
        for (int s = 0; s < 4; ++s) qb[0][s] = tr2(lds, tq + s * 1024, 256);
        SB();
#pragma unroll
        for (int s = 0; s < 32; ++s) {
            qa = mfma(qb[(s >> 2) & 1][s & 3], xf[s], qa);
            if (s < 28) qb[((s >> 2) + 1) & 1][s & 3] = tr2(lds, tq + (s + 4) * 1024, 256);
            SB();
        }
#pragma unroll
        for (int g = 0; g < 2; ++g) *(LAS f32x4*)(lds + L_XCH + m * 4096 + g * 1024 + lane * 16) = (f32x4){qa[8 + 4 * g], qa[9 + 4 * g], qa[10 + 4 * g], qa[11 + 4 * g]};
        STAMP(6);
        BAR();
#pragma unroll
        for (int g = 0; g < 2; ++g) {
            const f32x4 c0 = *(LAS f32x4*)(lds + L_CSQ + (8 * g + 4 * h2) * 4);
#pragma unroll
            for (int e = 0; e < 4; ++e) yq[4 * g + e] = __builtin_fmaf(-mean1, c0[e], qa[4 * g + e]);
        }
    } else {
        if (lw >= 2) {
            __builtin_amdgcn_s_setprio(3);
            const int ah = lw - 2;
            f32x16 kv = {};
            unsigned aa[2], ab[2];
#pragma unroll
            for (int u = 0; u < 2; ++u) {
                const unsigned R = 8 * h2 + 4 * u + q, sw = (R >> 1) & 7;
                aa[u] = L_PHI + ah * 8192 + R * 64 + (((4 * blk + pp) ^ sw) * 8);
                ab[u] = aa[u] + (L_VIMG - L_PHI);
                asm volatile("" : "+v"(aa[u]), "+v"(ab[u]));
            }
            u32x4 A[8], B[8];
#pragma unroll
            for (int s = 0; s < 8; ++s) {
                const s16x4 a0 = __builtin_amdgcn_ds_read_tr16_b64_v4i16((LAS s16x4*)(lds + aa[0] + s * 1024));
                const s16x4 a1 = __builtin_amdgcn_ds_read_tr16_b64_v4i16((LAS s16x4*)(lds + aa[1] + s * 1024));
                const s16x4 b0 = __builtin_amdgcn_ds_read_tr16_b64_v4i16((LAS s16x4*)(lds + ab[0] + s * 1024));
                const s16x4 b1 = __builtin_amdgcn_ds_read_tr16_b64_v4i16((LAS s16x4*)(lds + ab[1] + s * 1024));
                A[s] = __builtin_bit_cast(u32x4, __builtin_shufflevector(a0, a1, 0, 1, 2, 3, 4, 5, 6, 7));
                B[s] = __builtin_bit_cast(u32x4, __builtin_shufflevector(b0, b1, 0, 1, 2, 3, 4, 5, 6, 7));
                if (s == 3) PIN();
            }
            PIN();
#pragma unroll
            for (int s = 0; s < 8; ++s) kv = mfma(A[s], B[s], kv);
            if (t <= 16) {
                const unsigned base = WS_KVP + (j * 16 + p) * KVP_BYTES + ((ah * 4 + h2) * 17 + t) * 16;
                st16(rsW, (u32x4){pack2(kv[0], kv[1]), pack2(kv[2], kv[3]), pack2(kv[4], kv[5]), pack2(kv[6], kv[7])}, base, 0);
                st16(rsW, (u32x4){pack2(kv[8], kv[9]), pack2(kv[10], kv[11]), pack2(kv[12], kv[13]), pack2(kv[14], kv[15])}, base + 34 * 16, 0);
            }
            WAITV(0);
            arrive_publish(lds, 0, 2, ws, WS_F1 + (j * 16 + p) * 256, tag, lane);
            __builtin_amdgcn_s_setprio(0);
            STAMP(6);
        } else {
            unsigned tq = L_WQ + lw * 32 + (8 * (lane >> 4) + q) * 64 + pp * 8;
            asm volatile("" : "+v"(tq));
            f32x4 qcs = {};
#pragma unroll
            for (int g = 0; g < 4; ++g) {
                u32x4 f[4];
#pragma unroll
                for (int s = 0; s < 4; ++s) f[s] = tr2(lds, tq + (4 * g + s) * 2048, 256);
                PIN();
#pragma unroll
                for (int s = 0; s < 4; ++s) qcs = mfma16(f[s], ones, qcs);
            }
            if ((lane & 15) == 0) *(LAS f32x4*)(lds + L_CSQ + lw * 64 + (lane >> 4) * 16) = qcs;
            STAMP(6);
        }
        BAR();
        {
            const rsrc_t rsO = biased_rsrc(wo, 512 * 512 * 2);
#pragma unroll
            for (int g = 0; g < 4; ++g) {
                const int i = 4 * g + lw;
                const unsigned so = 16 * ((i + r2) & 15) * 1024 + 128 * c;
                dma4(lb + L_X + i * 4096, rsO, rsO, rsO, rsO, vrow, vrow, so, so + 8 * 1024, so + 256 * 1024, so + 264 * 1024);
            }
        }
#pragma unroll
        for (int g = 0; g < 2; ++g) {
            const f32x4 c0 = *(LAS f32x4*)(lds + L_CSQ + (16 + 8 * g + 4 * h2) * 4);
            const f32x4 v = *(LAS f32x4*)(lds + L_XCH + m * 4096 + g * 1024 + lane * 16);
#pragma unroll
            for (int e = 0; e < 4; ++e) yq[4 * g + e] = __builtin_fmaf(-mean1, c0[e], v[e]);
        }
    }
    STAMP(7);
    u32x4 qpos, qneg;
    {
        const u32x4 qf = {pack2(yq[0], yq[1]), pack2(yq[2], yq[3]), pack2(yq[4], yq[5]), pack2(yq[6], yq[7])};
        const f32x16 zero = {};
        const f32x16 pq = mfma(aphi, qf, zero);
        dual_softmax(pq, rstd1 * LOG2E, qpos, qneg);
    }
    STAMP(8);
    unsigned kso[16];
#pragma unroll
    for (int i = 0; i < 16; ++i) { kso[i] = WS_KVP + (j * 16 + ((i + 4 * q4) & 15)) * KVP_BYTES; asm volatile("" : "+s"(kso[i])); }
    if (w == 0) poll_flags<PD1>(ws, WS_F1 + j * 16 * 256, 16, tag, lane);
    BAR();
    STAMP(9);
    {
        const unsigned kb = WS_KVP + j * 16 * KVP_BYTES;
        float lo = 0.f, hi = 0.f;
        unsigned v[16], z[8];
#pragma unroll
        for (int i = 0; i < 16; ++i) v[i] = ld4(rsW, tid * 4, kso[i]);
        if (w == 0) {
#pragma unroll
            for (int i = 0; i < 8; ++i) z[i] = ld4(rsW, (512 + t) * 4 + h2 * 8 * KVP_BYTES, kb + i * KVP_BYTES);
        }
        PIN();
#pragma unroll
        for (int i = 0; i < 16; ++i) { lo += blo(v[i]); hi += bhi(v[i]); }
        if (w == 0) {
            float zl = 0.f, zh = 0.f;
#pragma unroll
            for (int i = 0; i < 8; ++i) { zl += blo(z[i]); zh += bhi(z[i]); }
            zl = halves_add(zl); zh = halves_add(zh);
            if (h2 == 0) *(LAS unsigned*)(lds + L_KVA + (512 + t) * 4) = pack2(zl, zh);
        }
        *(LAS unsigned*)(lds + L_KVA + tid * 4) = pack2(lo, hi);
    }
    BAR();
    STAMP(10);
    const int npass = has3 ? 3 : 2;
    const int m2 = w & 1, kh = (w >> 1) & 1;
    const int T2 = 4 * pP + 2 * half + m2;
    const unsigned C0 = 64 * c + 32 * kh;
    {
        const unsigned col = t < 16 ? t : 16;
        const u32x4 kp = *(LAS u32x4*)(lds + L_KVA + ((a * 4 + h2) * 17 + col) * 16);
        const u32x4 kn = *(LAS u32x4*)(lds + L_KVA + ((a * 4 + 2 + h2) * 17 + col) * 16);
        f32x16 o = {};
        o = mfma(kp, qpos, o);
        o = mfma(kn, qneg, o);
        const float r = __builtin_amdgcn_rcpf(o[8] + 1e-6f);
        st16(rsW, (u32x4){pack2(o[0] * r, o[1] * r), pack2(o[2] * r, o[3] * r), pack2(o[4] * r, o[5] * r), pack2(o[6] * r, o[7] * r)},
             WS_O + ((4 * p + m) * 32 + 2 * j + a) * 1024 + lane * 16, 0);
        WAITV(0);
        BAR();
        if (tid == 0) __hip_atomic_store((GAS u64*)(ws + WS_F2 + (p * 16 + j) * 256), tag, __ATOMIC_RELAXED, __HIP_MEMORY_SCOPE_AGENT);
        SBAR();
    }
    const unsigned trp = (4 * h2 + q) * 64 + blk * 32 + pp * 8;
    const unsigned t128 = (4 * h2 + q) * 128 + (q >> 1) * 64 + blk * 32 + pp * 8;

    if (w >= 4) {
        ISSUE_STAGE(0, L_Y, 0, 4);
        STAMP(11);
    }
    if (w >= 4) {
        const rsrc_t rsD = biased_rsrc(wd, 1376 * 512 * 2);
#define ISSUE_WD(pi, halfbase) do { _Pragma("unroll") for (int g_ = 0; g_ < ((pi) == 2 ? 3 : 4); ++g_) { const int i_ = 4 * g_ + lw; if ((pi) < 2 || i_ < 11) { \
                const int ii_ = (pi) == 2 ? (i_ + r4 >= 11 ? i_ + r4 - 11 : i_ + r4) : ((i_ + r2) & 15); \
                const unsigned sa_ = 16 * (((pi) == 2 ? 64 : 32 * (pi)) + ii_) * 1024 + 128 * c, sb_ = sa_ + 16 * ((pi) == 2 ? 11 : 16) * 1024; \
                dma4(lb + (halfbase) + i_ * 4096, rsD, rsD, rsD, rsD, vrow, vrow, sa_, sa_ + 8 * 1024, sb_, sb_ + 8 * 1024); } } } while (0)
        f32x4 cs0 = {}, cs1 = {};
        const unsigned gam = lane >> 4;
        const unsigned ca0 = (lw >> 1) * 2048 + (gam >> 1) * 4096 + (8 * (gam & 1) + q) * 128 + (((lw & 1) * 64) ^ (64 * (q >> 1))) + pp * 8;
#define COLSUM_STAGE(halfbase) do { unsigned cb_ = (halfbase) + ca0; asm volatile("" : "+v"(cb_)); \
            _Pragma("unroll") for (int g_ = 0; g_ < 4; ++g_) { const u32x4 f0_ = tr2(lds, cb_ + g_ * 16384, 512), f1_ = tr2(lds, cb_ + g_ * 16384 + 32, 512), \
                                                                         f2_ = tr2(lds, cb_ + g_ * 16384 + 8192, 512), f3_ = tr2(lds, cb_ + g_ * 16384 + 8192 + 32, 512); \
                PIN(); cs0 = mfma16(f0_, ones, cs0); cs1 = mfma16(f1_, ones, cs1); cs0 = mfma16(f2_, ones, cs0); cs1 = mfma16(f3_, ones, cs1); } } while (0)
        BAR();
        STAMP(12);
        BAR();
        SBAR();
        ISSUE_STAGE(1, L_X, 0, B_EARLY);
        BAR();
        SBAR();
        ISSUE_STAGE(1, L_X, B_EARLY, 4);
        STAMP(13);
        waitv<16>();
        BAR();
        COLSUM_STAGE(L_Y);
        WAITV(0);
        lsig(lb, 8); lwait(lds, 8, 4);
        COLSUM_STAGE(L_X);
        if ((lane & 15) == 0) {
            *(LAS f32x4*)(lds + L_CS3 + ((lw & 1) * 64 + (lw >> 1) * 32 + 4 * gam) * 4) = cs0;
            *(LAS f32x4*)(lds + L_CS3 + ((lw & 1) * 64 + (lw >> 1) * 32 + 16 + 4 * gam) * 4) = cs1;
        }
        BAR();
        if (npass == 3) {
            ISSUE_UNIT(u3, L_Y);
            WAITV(0);
            SBAR();
            ISSUE_WD(0, L_X);
            {
                unsigned cb = L_Y + (lw >> 1) * 1024 + (gam >> 1) * 2048 + (8 * (gam & 1) + q) * 64 + (lw & 1) * 32 + pp * 8;
                asm volatile("" : "+v"(cb));
                f32x4 c3 = {};
#pragma unroll
                for (int g = 0; g < 4; ++g) {
                    u32x4 f[4];
#pragma unroll
                    for (int e = 0; e < 4; ++e) f[e] = tr2(lds, cb + (4 * g + e) * 4096, 256);
                    PIN();
#pragma unroll
                    for (int e = 0; e < 4; ++e) c3 = mfma16(f[e], ones, c3);
                }
                if ((lane & 15) == 0) *(LAS f32x4*)(lds + L_CS3 + (128 + (lw >> 1) * 32 + (lw & 1) * 16 + 4 * gam) * 4) = c3;
            }
            BAR();
            WAITV(0);
            BAR();
            if (lw == 0) {
                lwait(lds, 11, 4);
                if (lane == 0) __hip_atomic_store((GAS u64*)(ws + WS_F4 + 0x10000 + (p * 16 + j) * 256), tag, __ATOMIC_RELAXED, __HIP_MEMORY_SCOPE_AGENT);
            }
            SBAR();
            ISSUE_WD(1, L_Y);
            WAITV(0);
            BAR();
            if (lw == 0) { poll_flags3(ws, WS_F4 + 0x10000 + pP * 16 * 256, tag, lane); lsig(lb, 14); }
            ISSUE_WD(2, L_X);
            WAITV(0);
            BAR();
        } else {
            ISSUE_WD(0, L_Y);
            SBAR();
            ISSUE_WD(1, L_X);
            WAITV(0);
            BAR();
            SBAR();
            BAR();
            if (lw == 0) { poll_flags3(ws, WS_F4 + 0x10000 + pP * 16 * 256, tag, lane); lsig(lb, 14); }
            ISSUE_WD(2, L_Y);
            WAITV(0);
            BAR();
        }
        BAR();
    } else {
        if (PRIO_C) __builtin_amdgcn_s_setprio(PRIO_C);
        const unsigned vo = (32 * T2 + t) * 1024 + (C0 + 8 * h2) * 2;
        u32x4 xr0 = __builtin_amdgcn_raw_buffer_load_b128(rsX, vo, 0, 0);
        u32x4 xr1 = __builtin_amdgcn_raw_buffer_load_b128(rsX, vo + 32, 0, 0);
        PIN();
        STAMP(11);
        if (w == 0) poll_flags<PD2>(ws, WS_F2 + pP * 16 * 256, 16, tag, lane);
        BAR();
        STAMP(12);
        if (b == 0 && w == 1 && lane == 0) __hip_atomic_store((GAS u64*)ws, tag, __ATOMIC_RELAXED, __HIP_MEMORY_SCOPE_AGENT);
        {
            u32x4 hres0, hres1;
            u32x4 of[16];
#pragma unroll
            for (int i = 0; i < 8; ++i) of[i] = ld16(rsW, lane * 16, WS_O + (T2 * 32 + 16 * kh + ((i + r2) & 15)) * 1024);
            PIN();
            f32x16 keep, snd = {};
            { auto r = __builtin_amdgcn_permlane32_swap(xr0[0], xr0[2], false, false); xr0[0] = r[0]; xr0[2] = r[1];
              r = __builtin_amdgcn_permlane32_swap(xr0[1], xr0[3], false, false); xr0[1] = r[0]; xr0[3] = r[1];
              r = __builtin_amdgcn_permlane32_swap(xr1[0], xr1[2], false, false); xr1[0] = r[0]; xr1[2] = r[1];
              r = __builtin_amdgcn_permlane32_swap(xr1[1], xr1[3], false, false); xr1[1] = r[0]; xr1[3] = r[1]; }
#pragma unroll
            for (int e = 0; e < 4; ++e) { keep[2 * e] = blo(xr0[e]); keep[2 * e + 1] = bhi(xr0[e]); keep[8 + 2 * e] = blo(xr1[e]); keep[8 + 2 * e + 1] = bhi(xr1[e]); }
            asm volatile("" : "+v"(keep));
            gemm2<16, 4096, 1024, 0>(lds, L_X + kh * 2048 + (t128 ^ (64 * kh)), L_X + kh * 2048 + (t128 ^ (64 * (kh ^ 1))), of, keep, snd, [&](int g) {
                if (g < 4) {
#pragma unroll
                    for (int e = 0; e < 2; ++e) of[8 + 2 * g + e] = ld16(rsW, lane * 16, WS_O + (T2 * 32 + 16 * kh + ((8 + 2 * g + e + r2) & 15)) * 1024);
                    SB();
                } });
#pragma unroll
            for (int g = 0; g < 4; ++g) *(LAS f32x4*)(lds + L_XCH + w * 4096 + g * 1024 + lane * 16) = (f32x4){snd[4 * g], snd[4 * g + 1], snd[4 * g + 2], snd[4 * g + 3]};
            STAMP(13);
            BAR();
#pragma unroll
            for (int g = 0; g < 4; ++g) {
                const f32x4 v = *(LAS f32x4*)(lds + L_XCH + (w ^ 2) * 4096 + g * 1024 + lane * 16);
#pragma unroll
                for (int e = 0; e < 4; ++e) keep[4 * g + e] += v[e];
            }
#pragma unroll
            for (int e = 0; e < 4; ++e) { hres0[e] = pack2(keep[2 * e], keep[2 * e + 1]); hres1[e] = pack2(keep[8 + 2 * e], keep[8 + 2 * e + 1]); }
            const unsigned hb = WS_H + (T2 * 32 + 4 * c + 2 * kh) * 1024;
            st16(rsW, hres0, lane * 16, hb);
            st16(rsW, hres1, lane * 16, hb + 1024);
            *(LAS u32x4*)(lds + L_HRES + w * 2048 + lane * 16) = hres0;
            *(LAS u32x4*)(lds + L_HRES + w * 2048 + 1024 + lane * 16) = hres1;
            f32x2 s1 = {0.f, 0.f}, s2 = {0.f, 0.f};
#pragma unroll
            for (int e = 0; e < 8; ++e) { const f32x2 v = {keep[2 * e], keep[2 * e + 1]}; s1 += v; s2 += v * v; }
            float su = s1[0] + s1[1], sq = s2[0] + s2[1];
            su = halves_add(su); sq = halves_add(sq);
            __builtin_amdgcn_raw_buffer_store_b32(asu(h2 ? sq : su), rsW, lane * 4 + WS_HST + (T2 * 16 + 2 * c + kh) * 256, 0, 16);
            SBAR();
            WAITV(0);
            BAR();
            if (tid == 0) __hip_atomic_store((GAS u64*)(ws + WS_F3 + (pP * 16 + jP) * 256), tag, __ATOMIC_RELAXED, __HIP_MEMORY_SCOPE_AGENT);
            SBAR();
        }
        STAMP(14);
        if (w == 0) poll_flags<PD3>(ws, WS_F3 + p * 16 * 256, 16, tag, lane);
        BAR();
        STAMP(15);
        {
            const int T = 4 * p + w;
            u32x4 hf[32];
            float sv[16];
#pragma unroll
            for (int s = 0; s < 8; ++s) hf[s] = ld16(rsW, lane * 16, WS_H + (T * 32 + ((s + r3) & 31)) * 1024);
            PIN();
            float mean, r2;
            auto act_out = [&](const f32x16& ag, const f32x16& au, int cs, unsigned ks) {
                float y[16];
#pragma unroll
                for (int g = 0; g < 4; ++g) {
                    const f32x4 cg = *(LAS f32x4*)(lds + L_CS3 + (cs * 64 + 8 * g + 4 * h2) * 4), cu = *(LAS f32x4*)(lds + L_CS3 + (cs * 64 + 32 + 8 * g + 4 * h2) * 4);
#pragma unroll
                    for (int e = 0; e < 4; ++e) y[4 * g + e] = (__builtin_fmaf(-mean, cg[e], ag[4 * g + e]) * r2) * __builtin_fmaf(-mean, cu[e], au[4 * g + e]);
                }
                const unsigned ab = WS_ACT + (T * 86 + ks) * 1024;
                st16(rsW, (u32x4){pack2(y[0], y[1]), pack2(y[2], y[3]), pack2(y[4], y[5]), pack2(y[6], y[7])}, lane * 16, ab);
                st16(rsW, (u32x4){pack2(y[8], y[9]), pack2(y[10], y[11]), pack2(y[12], y[13]), pack2(y[14], y[15])}, lane * 16, ab + 1024);
            };
            {
                f32x16 g0 = {}, g1 = {}, u0 = {}, u1 = {};
                gemm4<0>(lds, L_Y + t128, L_Y + (t128 ^ 64), hf, g0, g1, u0, u1, [&](int i) {
                    if (i < 8) hf[8 + i] = ld16(rsW, lane * 16, WS_H + (T * 32 + ((8 + i + r3) & 31)) * 1024);
                    else { hf[2 * i] = ld16(rsW, lane * 16, WS_H + (T * 32 + ((2 * i + r3) & 31)) * 1024); hf[2 * i + 1] = ld16(rsW, lane * 16, WS_H + (T * 32 + ((2 * i + 1 + r3) & 31)) * 1024); }
                    if (i < 4) {
#pragma unroll
                        for (int k = 0; k < 4; ++k) sv[4 * i + k] = asf(ld4(rsW, lane * 4, WS_HST + (T * 16 + 4 * i + k) * 256));
                    }
                    if (i == 9) {
                        float tot = 0.f;
#pragma unroll
                        for (int k = 0; k < 16; ++k) tot += sv[k];
                        const u32x2 r = both_halves(tot);
                        mean = asf(r[0]) * (1.0f / 512.0f);
                        const float var = fmaxf(asf(r[1]) * (1.0f / 512.0f) - mean * mean, 0.f);
                        r2 = __builtin_amdgcn_rcpf(var + 1e-5f);
                        asm volatile("" : "+v"(mean), "+v"(r2));
                    }
                    SB(); });
                BAR();
                STAMP(16);
                gemm4<16>(lds, L_X + t128, L_X + (t128 ^ 64), hf, g0, g1, u0, u1, nohook);
                STAMP(17);
                act_out(g0, u0, 0, 4 * j);
                act_out(g1, u1, 1, 4 * j + 2);
                SBAR();
            }
            if (npass == 3) {
                f32x16 ag = {}, au = {};
                gemm2<32, 2048, 512, 0>(lds, L_Y + trp, L_Y + 1024 + trp, hf, ag, au, [&](int g) {
                    if (g == 3) { WAITV(0); arrive_publish(lds, 3, 4, ws, WS_F4 + (p * 16 + j) * 256, tag, lane); PIN(); } });
                BAR();
                STAMP(18);
                act_out(ag, au, 2, 2 * u3);
            } else {
                WAITV(0); arrive_publish(lds, 3, 4, ws, WS_F4 + (p * 16 + j) * 256, tag, lane);
            }
        }
        STAMP(19);
        {
            f32x16 a0, a1 = {};
            {
                const u32x4 hres0 = *(LAS u32x4*)(lds + L_HRES + w * 2048 + lane * 16), hres1 = *(LAS u32x4*)(lds + L_HRES + w * 2048 + 1024 + lane * 16);
#pragma unroll
                for (int e = 0; e < 4; ++e) { a0[2 * e] = blo(hres0[e]); a0[2 * e + 1] = bhi(hres0[e]); a0[8 + 2 * e] = blo(hres1[e]); a0[8 + 2 * e + 1] = bhi(hres1[e]); }
                asm volatile("" : "+v"(a0));
            }
            const unsigned tk = kh * 2048 + (t128 ^ (64 * kh)), ts = kh * 2048 + (t128 ^ (64 * (kh ^ 1)));
            const unsigned h0 = npass == 3 ? L_X : L_Y, h1 = npass == 3 ? L_Y : L_X;
            if (w == 0) poll_flags(ws, WS_F4 + pP * 16 * 256, 16, tag, lane);
            BAR();
            STAMP(20);
            u32x4 af[43];
            auto ld_act = [&](int i) {
                const int ks = i < 32 ? 32 * (i >> 4) + kh * 16 + ((i + r2) & 15) : 64 + kh * 11 + (i - 32 + r4 >= 11 ? i - 32 + r4 - 11 : i - 32 + r4);
                af[i] = ld16(rsW, lane * 16, WS_ACT + (T2 * 86 + ks) * 1024); };
            if (npass == 3) { WAITV(0); lsig(lb, 11); }
#pragma unroll
            for (int i = 0; i < 8; ++i) ld_act(i);
            SBAR();
            gemm2<16, 4096, 1024, 0>(lds, h0 + tk, h0 + ts, af, a0, a1, [&](int g) { ld_act(8 + 2 * g); ld_act(9 + 2 * g); SB(); });
            STAMP(21);
            BAR();
            STAMP(22);
            gemm2<16, 4096, 1024, 16>(lds, h1 + tk, h1 + ts, af, a0, a1, [&](int g) {
                if (g == 4) lwait(lds, 14, 1);
                ld_act(24 + 2 * g); ld_act(25 + 2 * g); SB(); });
            BAR();
            STAMP(23);
            gemm2<11, 4096, 1024, 32>(lds, h0 + tk, h0 + ts, af, a0, a1, [&](int g) { if (g < 2) { ld_act(40 + 2 * g < 43 ? 40 + 2 * g : 42); if (g == 0) ld_act(41); SB(); } });
#pragma unroll
            for (int g = 0; g < 4; ++g) *(LAS f32x4*)(lds + L_XCH + w * 4096 + g * 1024 + lane * 16) = (f32x4){a1[4 * g], a1[4 * g + 1], a1[4 * g + 2], a1[4 * g + 3]};
            BAR();
            STAMP(24);
#pragma unroll
            for (int g = 0; g < 4; ++g) {
                const f32x4 v = *(LAS f32x4*)(lds + L_XCH + (w ^ 2) * 4096 + g * 1024 + lane * 16);
#pragma unroll
                for (int e = 0; e < 4; ++e) a0[4 * g + e] += v[e];
            }
            u32x4 o0, o1;
#pragma unroll
            for (int e = 0; e < 4; ++e) { o0[e] = pack2(a0[2 * e], a0[2 * e + 1]); o1[e] = pack2(a0[8 + 2 * e], a0[8 + 2 * e + 1]); }
            { auto r = __builtin_amdgcn_permlane32_swap(o0[0], o0[2], false, false); o0[0] = r[0]; o0[2] = r[1];
              r = __builtin_amdgcn_permlane32_swap(o0[1], o0[3], false, false); o0[1] = r[0]; o0[3] = r[1];
              r = __builtin_amdgcn_permlane32_swap(o1[0], o1[2], false, false); o1[0] = r[0]; o1[2] = r[1];
              r = __builtin_amdgcn_permlane32_swap(o1[1], o1[3], false, false); o1[1] = r[0]; o1[3] = r[1]; }
            int tz = threadIdx.x; asm volatile("" : "+v"(tz));
            const unsigned tr_ = m2 * 32 + (tz & 31), ch = kh * 4 + ((tz >> 5) & 1);
            *(LAS u32x4*)(lds + L_HRES + tr_ * 128 + ((ch ^ (tr_ & 7)) * 16)) = o0;
            *(LAS u32x4*)(lds + L_HRES + tr_ * 128 + (((ch + 2) ^ (tr_ & 7)) * 16)) = o1;
            BAR();
            const rsrc_t rsOut = __builtin_amdgcn_make_buffer_rsrc((void*)out, 0, 2048 * 512 * 2, 0x00020000);
#pragma unroll
            for (int i = 0; i < 2; ++i) {
                const unsigned row = w * 16 + i * 8 + ((tz & 63) >> 3), c16 = tz & 7;
                const u32x4 v = *(LAS u32x4*)(lds + L_HRES + row * 128 + ((c16 ^ (row & 7)) * 16));
                __builtin_amdgcn_raw_buffer_store_b128(v, rsOut, (64 * (2 * pP + half) + row) * 1024 + 128 * c + c16 * 16, 0, 0);
            }
        }
        STAMP(25); CSTAMP(31);
    }
}

extern "C" void kernel_launch(void* const* d_in, const int* in_sizes, int n_in, void* d_out, int out_size, void* d_ws, size_t ws_size, hipStream_t stream) {
    (void)in_sizes; (void)n_in; (void)out_size; (void)ws_size;
    (void)hipFuncSetAttribute((const void*)hedgehog_block, hipFuncAttributeMaxDynamicSharedMemorySize, LDS_BYTES);
    hedgehog_block<<<256, 512, LDS_BYTES, stream>>>((const bf16_t*)d_in[0], (const bf16_t*)d_in[1], (const bf16_t*)d_in[2], (const bf16_t*)d_in[3],
                                                    (const bf16_t*)d_in[5], (const bf16_t*)d_in[6], (const bf16_t*)d_in[7], (bf16_t*)d_out, (char*)d_ws);
}
```

```cpp
#include <hip/hip_runtime.h>
#include <hip/hip_bf16.h>

#define LAS __attribute__((address_space(3)))
#define GAS __attribute__((address_space(1)))
#define DI __device__ __forceinline__
typedef unsigned short bf16_t;
typedef unsigned u32x4 __attribute__((ext_vector_type(4)));
typedef unsigned u32x2 __attribute__((ext_vector_type(2)));
typedef short s16x4 __attribute__((ext_vector_type(4)));
typedef __bf16 bf16x8 __attribute__((ext_vector_type(8)));
typedef __bf16 bf16x2 __attribute__((ext_vector_type(2)));
typedef float f32x16 __attribute__((ext_vector_type(16)));
typedef float f32x4 __attribute__((ext_vector_type(4)));
typedef float f32x2 __attribute__((ext_vector_type(2)));
typedef unsigned long long u64;
typedef __amdgpu_buffer_rsrc_t rsrc_t;

constexpr unsigned WS_F1 = 0x10000, WS_F2 = 0x20000, WS_F3 = 0x30000, WS_F4 = 0x40000;
constexpr unsigned WS_KVP = 0x80000, KVP_BYTES = 2176;
constexpr unsigned WS_HST = 0x110000;
constexpr unsigned WS_XST = 0x160000;
constexpr unsigned WS_O = 0x200000, WS_H = 0x400000, WS_ACT = 0x600000, WS_PROF = 0xC00000, WS_BYTES = 0x1000000;
constexpr unsigned L_X = 0, L_Y = 65536, L_XCH = 131072, L_MISC = 147456;
constexpr unsigned L_STAT = L_MISC, L_CS1 = L_MISC + 1024, L_CSQ = L_MISC + 1280, L_WPHI = L_MISC + 1536, L_KVA = L_MISC + 2560, L_CS3 = L_MISC + 4736, L_CNT = L_MISC + 5504, L_TAG = L_MISC + 6016, L_DUMMY = L_MISC + 6144, L_HRES = L_MISC + 8192;
constexpr unsigned L_WQ = 65536, L_PHI = 98304, L_VIMG = 114688  , SS_BYTES = 24576;
constexpr unsigned LDS_BYTES = 163840;
constexpr unsigned SPIN_CAP = 40000;
constexpr float LOG2E = 1.4426950408889634f;

DI float asf(unsigned v) { return __builtin_bit_cast(float, v); }
DI unsigned asu(float v) { return __builtin_bit_cast(unsigned, v); }
DI unsigned pack2(float lo, float hi) { bf16x2 v = {(__bf16)lo, (__bf16)hi}; return __builtin_bit_cast(unsigned, v); }
DI float blo(unsigned v) { return __builtin_bit_cast(float, v << 16); }
DI float bhi(unsigned v) { return __builtin_bit_cast(float, v & 0xffff0000u); }
DI f32x16 mfma(u32x4 a, u32x4 b, f32x16 c) { return __builtin_amdgcn_mfma_f32_32x32x16_bf16(__builtin_bit_cast(bf16x8, a), __builtin_bit_cast(bf16x8, b), c, 0, 0, 0); }
DI f32x4 mfma16(u32x4 a, u32x4 b, f32x4 c) { return __builtin_amdgcn_mfma_f32_16x16x32_bf16(__builtin_bit_cast(bf16x8, a), __builtin_bit_cast(bf16x8, b), c, 0, 0, 0); }
DI u32x4 tr2(LAS char* lds, unsigned off, unsigned second) {
    const s16x4 lo = __builtin_amdgcn_ds_read_tr16_b64_v4i16((LAS s16x4*)(lds + off));
    const s16x4 hi = __builtin_amdgcn_ds_read_tr16_b64_v4i16((LAS s16x4*)(lds + off + second));
    return __builtin_bit_cast(u32x4, __builtin_shufflevector(lo, hi, 0, 1, 2, 3, 4, 5, 6, 7));
}
DI void dma16(const rsrc_t rs, unsigned ldsaddr, unsigned voff, unsigned soff) {
    asm volatile("s_mov_b32 m0, %0\n\ts_nop 0\n\tbuffer_load_dwordx4 %1, %2, %3 offen lds" ::"s"(ldsaddr), "v"(voff), "s"(rs), "s"(soff) : "memory");
}
constexpr unsigned BIAS = 4096;
DI rsrc_t biased_rsrc(const void* p, unsigned bytes) { return __builtin_amdgcn_make_buffer_rsrc((void*)((const char*)p - BIAS), 0, bytes + BIAS, 0x00020000); }
DI void dma4(unsigned ldsaddr, const rsrc_t ra, const rsrc_t rb, const rsrc_t rc, const rsrc_t rd, unsigned va, unsigned vb, unsigned s0, unsigned s1, unsigned s2, unsigned s3) {
    asm volatile("s_mov_b32 m0, %0\n\ts_nop 0\n\t"
                 "buffer_load_dwordx4 %5, %1, %7 offen lds\n\tbuffer_load_dwordx4 %6, %2, %8 offen offset:1024 lds\n\t"
                 "buffer_load_dwordx4 %5, %3, %9 offen offset:2048 lds\n\tbuffer_load_dwordx4 %6, %4, %10 offen offset:3072 lds"
                 ::"s"(ldsaddr), "s"(ra), "s"(rb), "s"(rc), "s"(rd), "v"(va), "v"(vb), "s"(s0 + BIAS), "s"(s1 + BIAS - 1024), "s"(s2 + BIAS - 2048), "s"(s3 + BIAS - 3072) : "memory");
}
DI void dma2(unsigned ldsaddr, const rsrc_t r, unsigned va, unsigned vb, unsigned s0, unsigned s1) {
    asm volatile("s_mov_b32 m0, %0\n\ts_nop 0\n\tbuffer_load_dwordx4 %2, %1, %4 offen lds\n\tbuffer_load_dwordx4 %3, %1, %5 offen offset:1024 lds"
                 ::"s"(ldsaddr), "s"(r), "v"(va), "v"(vb), "s"(s0 + BIAS), "s"(s1 + BIAS - 1024) : "memory");
}
#define WAITV(n) asm volatile("s_waitcnt vmcnt(" #n ")" ::: "memory")
#define PIN() do { asm volatile("" ::: "memory"); __builtin_amdgcn_sched_barrier(0); } while (0)
template <int N> DI void waitv() { asm volatile("s_waitcnt vmcnt(%0)" ::"n"(N) : "memory"); }
#define B_EARLY 1
#define PD1 0
#define PD2 4
#define PD3 4
#define PRIO_C 0
#define K0 3
#define R1MODE 2
#define STAMP2_SS(ss) do { if (ss == 0) STAMP2(0); if (ss == 1) STAMP2(1); if (ss == 2) STAMP2(2); if (ss == 3) STAMP2(3); if (ss == 4) STAMP2(4); if (ss == 5) STAMP2(5); if (ss == 6) STAMP2(6); if (ss == 7) STAMP2(7); } while (0)
#define SBAR() asm volatile("s_barrier" ::: "memory")
#define BAR() asm volatile("s_waitcnt lgkmcnt(0)\n\ts_barrier" ::: "memory")
DI u32x2 both_halves(float v) { const unsigned a = asu(v); const auto r = __builtin_amdgcn_permlane32_swap(a, a, false, false); return (u32x2){r[0], r[1]}; }
DI float halves_add(float v) { const u32x2 r = both_halves(v); return asf(r[0]) + asf(r[1]); }
DI float halves_max(float v) { const u32x2 r = both_halves(v); return fmaxf(asf(r[0]), asf(r[1])); }
DI float halves_min(float v) { const u32x2 r = both_halves(v); return fminf(asf(r[0]), asf(r[1])); }
DI float dpp_ror4(float v) { return asf(__builtin_amdgcn_update_dpp(0, (int)asu(v), 0x124, 0xf, 0xf, true)); }
DI float dpp_ror8(float v) { return asf(__builtin_amdgcn_update_dpp(0, (int)asu(v), 0x128, 0xf, 0xf, true)); }
DI float rows16_add(float v) { const unsigned a = asu(v); const auto r = __builtin_amdgcn_permlane16_swap(a, a, false, false); return asf(r[0]) + asf(r[1]); }
DI float stride4_add(float v) { v += dpp_ror4(v); v += dpp_ror8(v); v = rows16_add(v); return halves_add(v); }
DI float quad_add(float v) {
    v += __builtin_bit_cast(float, __builtin_amdgcn_update_dpp(0, __builtin_bit_cast(int, v), 0xB1, 0xf, 0xf, true));
    v += __builtin_bit_cast(float, __builtin_amdgcn_update_dpp(0, __builtin_bit_cast(int, v), 0x4E, 0xf, 0xf, true));
    return v;
}
DI void st16(const rsrc_t rs, u32x4 v, unsigned voff, unsigned soff) { __builtin_amdgcn_raw_buffer_store_b128(v, rs, voff + soff, 0, 16); }
DI u32x4 ld16(const rsrc_t rs, unsigned voff, unsigned soff) { return __builtin_amdgcn_raw_buffer_load_b128(rs, voff, soff, 16); }
DI unsigned ld4(const rsrc_t rs, unsigned voff, unsigned soff) { return __builtin_amdgcn_raw_buffer_load_b32(rs, voff, soff, 16); }

DI void poll_flags3(const char* ws, unsigned off, u64 tag, int lane) {
    const int l = lane < 11 ? lane : 10;
    const GAS u64* f = (const GAS u64*)(ws + off) + (l < 6 ? l : l + 2) * 32;
#pragma unroll 1
    for (unsigned spin = 0; spin < SPIN_CAP; ++spin) {
        const u64 v = __hip_atomic_load(f, __ATOMIC_RELAXED, __HIP_MEMORY_SCOPE_AGENT);
        if (__all(v == tag)) break;
        __builtin_amdgcn_s_sleep(1);
    }
    asm volatile("" ::: "memory");
}
DI void nohook(int) {}
template <int DELAY = 0>
DI void poll_flags(const char* ws, unsigned off, int n, u64 tag, int lane) {
#pragma unroll
    for (int i = 0; i < DELAY; ++i) __builtin_amdgcn_s_sleep(2);
    const GAS u64* f = (const GAS u64*)(ws + off) + (lane < n ? lane : n - 1) * 32;
#pragma unroll 1
    for (unsigned spin = 0; spin < SPIN_CAP; ++spin) {
        const u64 v = __hip_atomic_load(f, __ATOMIC_RELAXED, __HIP_MEMORY_SCOPE_AGENT);
        if (__all(v == tag)) break;
        __builtin_amdgcn_s_sleep(1);
    }
    asm volatile("" ::: "memory");
}
DI void arrive_publish(LAS char* lds, int cnt, unsigned n, char* ws, unsigned flag_off, u64 tag, int lane) {
    if (lane == 0) {
        const unsigned old = __hip_atomic_fetch_add((LAS unsigned*)(lds + L_CNT) + cnt, 1u, __ATOMIC_RELAXED, __HIP_MEMORY_SCOPE_WORKGROUP);
        if (old == n - 1) __hip_atomic_store((GAS u64*)(ws + flag_off), tag, __ATOMIC_RELAXED, __HIP_MEMORY_SCOPE_AGENT);
    }
}
DI void lsig(unsigned lb, int idx) {
    const unsigned a = lb + L_CNT + idx * 4, one = 1;
    asm volatile("s_mov_b64 exec, 1\n\tds_add_u32 %0, %1\n\ts_mov_b64 exec, -1" ::"v"(a), "v"(one) : "memory");
}
DI void lwait(LAS char* lds, int idx, unsigned n) {
    const volatile LAS unsigned* c = (const volatile LAS unsigned*)(lds + L_CNT) + idx;
#pragma unroll 1
    for (unsigned spin = 0; spin < 64 * SPIN_CAP; ++spin) {
        if ((unsigned)__builtin_amdgcn_readfirstlane(*c) >= n) break;
        __builtin_amdgcn_s_sleep(1);
    }
    asm volatile("" ::: "memory");
}
DI void dual_softmax(const f32x16& p, float sl, u32x4& pos, u32x4& neg) {
    float mx = fmaxf(fmaxf(fmaxf(p[0], p[1]), fmaxf(p[2], p[3])), fmaxf(fmaxf(p[4], p[5]), fmaxf(p[6], p[7])));
    float mn = fminf(fminf(fminf(p[0], p[1]), fminf(p[2], p[3])), fminf(fminf(p[4], p[5]), fminf(p[6], p[7])));
    mx = halves_max(mx); mn = halves_min(mn);
    const float cx = -mx * sl, cn = mn * sl;
    f32x2 ep[4], en[4];
#pragma unroll
    for (int i = 0; i < 4; ++i) {
        const f32x2 v = {p[2 * i], p[2 * i + 1]};
        const f32x2 u = v * sl + cx, d = v * (-sl) + cn;
        ep[i] = (f32x2){__builtin_amdgcn_exp2f(u[0]), __builtin_amdgcn_exp2f(u[1])};
        en[i] = (f32x2){__builtin_amdgcn_exp2f(d[0]), __builtin_amdgcn_exp2f(d[1])};
    }
    const f32x2 sp = (ep[0] + ep[1]) + (ep[2] + ep[3]), sn = (en[0] + en[1]) + (en[2] + en[3]);
    const float rp = __builtin_amdgcn_rcpf(halves_add(sp[0] + sp[1])), rn = __builtin_amdgcn_rcpf(halves_add(sn[0] + sn[1]));
#pragma unroll
    for (int i = 0; i < 4; ++i) { const f32x2 a = ep[i] * rp, b = en[i] * rn; pos[i] = pack2(a[0], a[1]); neg[i] = pack2(b[0], b[1]); }
}

#define SB() __builtin_amdgcn_sched_barrier(0)
template <int N, int STRIDE, int SECOND, int OFF, int NA, class Hook>
DI void gemm2(LAS char* lds, unsigned b0, unsigned b1, const u32x4 (&act)[NA], f32x16& acc0, f32x16& acc1, Hook hook) {
    u32x4 fb[2][4];
    asm volatile("" : "+v"(b0), "+v"(b1));
    constexpr int NG = (N + 1) / 2;
#pragma unroll
    for (int e = 0; e < 2; ++e) { fb[0][2 * e] = tr2(lds, b0 + e * STRIDE, SECOND); fb[0][2 * e + 1] = tr2(lds, b1 + e * STRIDE, SECOND); }
    SB();
#pragma unroll
    for (int g = 0; g < NG; ++g) {
#pragma unroll
        for (int e = 0; e < 2; ++e) {
            const int i = 2 * g + e, n = i + 2;
            if (i < N) acc0 = mfma(fb[g & 1][2 * e], act[OFF + i], acc0);
            if (n < N) fb[(g + 1) & 1][2 * e] = tr2(lds, b0 + n * STRIDE, SECOND);
            SB();
            if (i < N) acc1 = mfma(fb[g & 1][2 * e + 1], act[OFF + i], acc1);
            if (n < N) fb[(g + 1) & 1][2 * e + 1] = tr2(lds, b1 + n * STRIDE, SECOND);
            SB();
        }
        hook(g);
    }
    asm volatile("" : "+v"(acc0), "+v"(acc1));
}
template <int OFF, class Hook>
DI void gemm4(LAS char* lds, unsigned b0, unsigned b1, const u32x4 (&act)[32], f32x16& g0, f32x16& g1, f32x16& u0, f32x16& u1, Hook hook) {
    u32x4 fb[2][4];
    asm volatile("" : "+v"(b0), "+v"(b1));
    fb[0][0] = tr2(lds, b0, 1024); fb[0][1] = tr2(lds, b1, 1024); fb[0][2] = tr2(lds, b0 + 2048, 1024); fb[0][3] = tr2(lds, b1 + 2048, 1024);
    SB();
#pragma unroll
    for (int i = 0; i < 16; ++i) {
        const int o = (i + 1) * 4096;
        g0 = mfma(fb[i & 1][0], act[OFF + i], g0);
        if (i < 15) fb[(i + 1) & 1][0] = tr2(lds, b0 + o, 1024);
        SB();
        g1 = mfma(fb[i & 1][1], act[OFF + i], g1);
        if (i < 15) fb[(i + 1) & 1][1] = tr2(lds, b1 + o, 1024);
        SB();
        u0 = mfma(fb[i & 1][2], act[OFF + i], u0);
        if (i < 15) fb[(i + 1) & 1][2] = tr2(lds, b0 + o + 2048, 1024);
        SB();
        u1 = mfma(fb[i & 1][3], act[OFF + i], u1);
        if (i < 15) fb[(i + 1) & 1][3] = tr2(lds, b1 + o + 2048, 1024);
        SB();
        hook(i);
    }
    asm volatile("" : "+v"(g0), "+v"(g1), "+v"(u0), "+v"(u1));
}
DI void touch_lines(const rsrc_t rs, unsigned ldsaddr, unsigned voff, unsigned soff) {
    asm volatile("s_mov_b32 m0, %0\n\ts_nop 0\n\tbuffer_load_dword %1, %2, %3 offen lds" ::"s"(ldsaddr), "v"(voff), "s"(rs), "s"(soff) : "memory");
}

#define STAMP(i) do {} while (0)
#define CSTAMP(i) do {} while (0)
#define STAMP2(i) do {} while (0)

__global__ void __launch_bounds__(512, 2)
hedgehog_block(const bf16_t* __restrict__ x, const bf16_t* __restrict__ wqkv, const bf16_t* __restrict__ wphi, const bf16_t* __restrict__ wo,
               const bf16_t* __restrict__ wg, const bf16_t* __restrict__ wu, const bf16_t* __restrict__ wd, bf16_t* __restrict__ out, char* __restrict__ ws) {
    extern __shared__ __attribute__((aligned(16))) char smem[];
    LAS char* lds = (LAS char*)smem;
    const unsigned lb = (unsigned)(size_t)lds;
    const int tid = threadIdx.x, lane = tid & 63;
    const int w = __builtin_amdgcn_readfirstlane(tid >> 6);
    const int b = blockIdx.x, xcd = b & 7, slot = b >> 3;
    const int p = 4 * (xcd >> 1) + (slot >> 3), j = 8 * (xcd & 1) + (slot & 7);
    const int pP = 2 * xcd + (slot >> 4), jP = slot & 15, c = slot & 7, half = (slot >> 3) & 1;
    const bool has3 = (j & 7) < (j < 8 ? 6 : 5);
    const int u3 = 32 + (j < 8 ? j : j - 2);
    const int t = lane & 31, h2 = lane >> 5, q = (lane & 15) >> 2, pp = lane & 3, blk = (lane >> 4) & 1;
    const int m = w & 3, a = w >> 2, lw = w & 3;
    const int q4 = slot >> 3;
    const int r1 = R1MODE == 0 ? 0 : R1MODE == 1 ? ((c + 2 * q4) & 7) : R1MODE == 2 ? 4 * (q4 & 1) : 2 * q4, r2 = (2 * c + 4 * q4) & 15, r3 = (4 * c + 8 * q4) & 31, r4 = (c + 3 * q4) % 11;
    STAMP(0); CSTAMP(30);
    const rsrc_t rsX = __builtin_amdgcn_make_buffer_rsrc((void*)x, 0, 2048 * 512 * 2, 0x00020000);
    const rsrc_t rsQ = __builtin_amdgcn_make_buffer_rsrc((void*)wqkv, 0, 512 * 1536 * 2, 0x00020000);
    const rsrc_t rsW = __builtin_amdgcn_make_buffer_rsrc((void*)ws, 0, WS_BYTES, 0x00020000);
    u64 tag = 0;
#define UNIFORM64(v) (((u64)(unsigned)__builtin_amdgcn_readfirstlane((unsigned)((v) >> 32)) << 32) | (unsigned)__builtin_amdgcn_readfirstlane((unsigned)(v)))
    const u32x4 ones = {0x3F803F80u, 0x3F803F80u, 0x3F803F80u, 0x3F803F80u};
    const unsigned c128 = ((lane & 7) ^ (4 * ((lane >> 4) & 1))) * 16;
    const unsigned vrow = (lane >> 3) * 1024 + c128;
    const unsigned trn = (8 * h2 + q) * 64 + pp * 8;

    const rsrc_t rsG = biased_rsrc(wg, 512 * 1376 * 2), rsU = biased_rsrc(wu, 512 * 1376 * 2);
    const unsigned vgu = (lane >> 2) * 2752 + (lane & 3) * 16, vgu8 = (lane >> 3) * 2752 + c128;
#define ISSUE_STAGE(st, halfbase, g0, g1) do { _Pragma("unroll") for (int g_ = (g0); g_ < (g1); ++g_) { const int i_ = 4 * g_ + lw; const unsigned so_ = 16 * ((16 * (st) + i_ + r3) & 31) * 2752 + 128 * j; \
            dma4(lb + (halfbase) + i_ * 4096, rsG, rsG, rsU, rsU, vgu8, vgu8, so_, so_ + 8 * 2752, so_, so_ + 8 * 2752); } } while (0)
#define ISSUE_UNIT(u, halfbase) do { _Pragma("unroll") for (int g_ = 0; g_ < 4; ++g_) { const int s_ = 2 * (4 * g_ + lw); \
            const unsigned sa_ = 16 * ((s_ + r3) & 31) * 2752 + 64 * (u), sb_ = 16 * ((s_ + 1 + r3) & 31) * 2752 + 64 * (u); \
            dma4(lb + (halfbase) + s_ * 2048, rsG, rsU, rsG, rsU, vgu, vgu, sa_, sa_, sb_, sb_); } } while (0)
    if (w == 3) {
        u64 pc; asm volatile("s_getpc_b64 %0" : "=s"(pc));
        const rsrc_t rsC = __builtin_amdgcn_make_buffer_rsrc((void*)(pc & ~127ull), 0, 36864, 0x00020000);
        touch_lines(rsC, lb + L_DUMMY, (slot * 9 + (lane < 9 ? lane : 0)) * 128, 0);
    }
    if (w < 3) {
        const rsrc_t rsA = __builtin_amdgcn_make_buffer_rsrc((void*)wg, 0, 512 * 1376 * 2, 0x00020000), rsB = __builtin_amdgcn_make_buffer_rsrc((void*)wu, 0, 512 * 1376 * 2, 0x00020000);
        const rsrc_t rsCc = __builtin_amdgcn_make_buffer_rsrc((void*)wd, 0, 512 * 1376 * 2, 0x00020000), rsE = __builtin_amdgcn_make_buffer_rsrc((void*)wo, 0, 512 * 512 * 2, 0x00020000);
        const unsigned blk64 = b * 3 + w;
        if (blk64 < 172) touch_lines(rsA, lb + L_DUMMY, (blk64 * 64 + lane) * 128, 0);
        else if (blk64 < 344) touch_lines(rsB, lb + L_DUMMY, ((blk64 - 172) * 64 + lane) * 128, 0);
        else if (blk64 < 516) touch_lines(rsCc, lb + L_DUMMY, ((blk64 - 344) * 64 + lane) * 128, 0);
        else if (blk64 < 580) touch_lines(rsE, lb + L_DUMMY, ((blk64 - 516) * 64 + lane) * 128, 0);
    }
    u32x4 xf[32];
    float yk[16];
    float mean1, rstd1;
    if (w < 4) {
        {
            u64 epoch = __hip_atomic_load((const GAS u64*)ws, __ATOMIC_RELAXED, __HIP_MEMORY_SCOPE_AGENT);
            const unsigned R = 128 * p + 8 * j + 2 * w;
            const u32x4 v0 = __builtin_amdgcn_raw_buffer_load_b128(rsX, lane * 16, R * 1024, 0), v1 = __builtin_amdgcn_raw_buffer_load_b128(rsX, lane * 16, R * 1024 + 1024, 0);
            if (tid < 16) *(LAS unsigned*)(lds + L_CNT + tid * 4) = 0u;
            f32x2 a1 = {0.f, 0.f}, a2 = {0.f, 0.f}, b1 = {0.f, 0.f}, b2 = {0.f, 0.f};
#pragma unroll
            for (int e = 0; e < 4; ++e) {
                const f32x2 z0 = {blo(v0[e]), bhi(v0[e])}, z1 = {blo(v1[e]), bhi(v1[e])};
                a1 += z0; a2 += z0 * z0; b1 += z1; b2 += z1 * z1;
            }
            const float su0 = stride4_add(quad_add(a1[0] + a1[1])), sq0 = stride4_add(quad_add(a2[0] + a2[1]));
            const float su1 = stride4_add(quad_add(b1[0] + b1[1])), sq1 = stride4_add(quad_add(b2[0] + b2[1]));
            const float su = (lane & 1) ? su1 : su0, sq = (lane & 1) ? sq1 : sq0;
            const float mean = su * (1.0f / 512.0f), var = fmaxf(sq * (1.0f / 512.0f) - mean * mean, 0.f);
            const float rstd = __builtin_amdgcn_rsqf(var + 1e-5f);
            asm volatile("s_waitcnt vmcnt(0)" : "+v"(epoch)::"memory");
            tag = UNIFORM64(epoch) + 0x9E3779B97F4A7C15ull;
            if (tid == 0) *(LAS u64*)(lds + L_TAG) = tag;
            if (lane < 2) st16(rsW, (u32x4){asu(mean), asu(rstd), (unsigned)tag, (unsigned)(tag >> 32)}, (R + lane) * 16, WS_XST);
        }
        STAMP(1);
        const unsigned xa0 = (32 * m + t) * 128 + ((h2 ^ ((t >> 1) & 7)) * 16);
        const unsigned twA = 16384 + (blk ? 1024 + 32 : 0) + trn, twB = 16384 + (blk ? 1024 : 32) + trn;
        f32x16 accA = {}, accB = {};
        u32x4 fa[2][2], fb[2][2], xs;
        unsigned bA = twA, bB = twB, bx0 = xa0, bx1 = xa0 ^ 32, bx2 = xa0 ^ 64, bx3 = xa0 ^ 96;
        asm volatile("" : "+v"(bA), "+v"(bB), "+v"(bx0), "+v"(bx1), "+v"(bx2), "+v"(bx3));
#pragma unroll
        for (int ss = 0; ss <= 8; ++ss) {
            if (ss < 8) {
                BAR();
                STAMP2_SS(ss);
                if (ss == 0) STAMP(2);
                if (ss == 6) { xs = ld16(rsW, (128 * p + 32 * m + t) * 16, WS_XST); SB(); }
            }
#pragma unroll
            for (int e = 0; e < 2; ++e) {
                if (ss > 0) accA = mfma(fa[1][e], xf[4 * ss - 2 + e], accA);
                if (ss < 8) { xf[4 * ss + e] = *(LAS u32x4*)(lds + (e ? bx1 : bx0)); fa[0][e] = tr2(lds, bA + e * 2048, 256); }
                SB();
                if (ss > 0) accB = mfma(fb[1][e], xf[4 * ss - 2 + e], accB);
                if (ss < 8) fb[0][e] = tr2(lds, bB + e * 2048, 256);
                SB();
            }
            if (ss < 8) {
#pragma unroll
                for (int e = 0; e < 2; ++e) {
                    accA = mfma(fa[0][e], xf[4 * ss + e], accA);
                    xf[4 * ss + 2 + e] = *(LAS u32x4*)(lds + (e ? bx3 : bx2)); fa[1][e] = tr2(lds, bA + (2 + e) * 2048, 256);
                    SB();
                    accB = mfma(fb[0][e], xf[4 * ss + e], accB);
                    fb[1][e] = tr2(lds, bB + (2 + e) * 2048, 256);
                    SB();
                }
                if (ss < 7) {
                    const unsigned d = ss == 5 ? (unsigned)(-5 * (int)SS_BYTES) : SS_BYTES;
                    bA += d; bB += d; bx0 += d; bx1 += d; bx2 += d; bx3 += d;
                    asm volatile("" : "+v"(bA), "+v"(bB), "+v"(bx0), "+v"(bx1), "+v"(bx2), "+v"(bx3));
                }
            }
        }
        STAMP(3);
#pragma unroll
        for (int g = 0; g < 4; ++g) *(LAS f32x4*)(lds + L_XCH + m * 4096 + g * 1024 + lane * 16) = (f32x4){accB[4 * g], accB[4 * g + 1], accB[4 * g + 2], accB[4 * g + 3]};
#pragma unroll 1
        for (unsigned spin = 0; spin < SPIN_CAP; ++spin) {
            if (__all(xs[2] == (unsigned)tag && xs[3] == (unsigned)(tag >> 32))) break;
            __builtin_amdgcn_s_sleep(1);
            xs = ld16(rsW, (128 * p + 32 * m + t) * 16, WS_XST);
        }
        mean1 = asf(xs[0]); rstd1 = asf(xs[1]);
        if (h2 == 0) *(LAS f32x2*)(lds + L_STAT + (32 * m + t) * 8) = (f32x2){mean1, rstd1};
        BAR();
        STAMP(4);
#pragma unroll
        for (int g = 0; g < 4; ++g) {
            const f32x4 cs = *(LAS f32x4*)(lds + L_CS1 + (8 * g + 4 * h2) * 4);
#pragma unroll
            for (int e = 0; e < 4; ++e) { const float y = __builtin_fmaf(-mean1, cs[e], accA[4 * g + e]); yk[4 * g + e] = g < 2 ? y : y * rstd1; }
        }
    } else {
        const unsigned vx0 = (lane >> 3) * 1024 + (((lane & 7) ^ (lane >> 4)) * 16), vx1 = (lane >> 3) * 1024 + (((lane & 7) ^ (4 + (lane >> 4))) * 16);
        const unsigned vwk = (lane >> 2) * 3072 + (lane & 3) * 16, vwv = (lane >> 2) * 3072 + ((lane & 3) ^ 2) * 16;
        const rsrc_t rbX = biased_rsrc(x, 2048 * 512 * 2), rbQ = biased_rsrc(wqkv, 512 * 1536 * 2);
#define ISSUE_SS(ss, v) do { const unsigned sb_ = lb + ((ss) % 6) * SS_BYTES, ks_ = ((ss) + r1) & 7; \
            const unsigned sx_ = (128 * p + 32 * (v)) * 1024 + ks_ * 128, sw_ = (16 * (v) + 64 * ks_) * 3072 + (512 + 32 * j) * 2; \
            dma2(sb_ + 16384 + (v) * 2048, rbQ, vwk, vwv, sw_, sw_ + 1024); \
            dma4(sb_ + (v) * 4096, rbX, rbX, rbX, rbX, vx0, vx1, sx_, sx_ + 8 * 1024, sx_ + 16 * 1024, sx_ + 24 * 1024); } while (0)
        if (lw == 0) {
            const rsrc_t rsP = __builtin_amdgcn_make_buffer_rsrc((void*)wphi, 0, 32 * 256 * 2, 0x00020000);
            dma16(rsP, lb + L_WPHI, lane * 16, j * 1024);
        }
#pragma unroll
        for (int ss = 0; ss < K0; ++ss) ISSUE_SS(ss, lw);
        STAMP(1);
        f32x4 csacc = {};
        const unsigned tw = 16384 + ((lw & 1) ? 1024 + ((lw >> 1) ^ 1) * 32 : (lw >> 1) * 32) + (lane >> 5) * 2048 + (8 * ((lane >> 4) & 1) + q) * 64 + pp * 8;
        constexpr int NL = 8 - K0;
#define P1WAIT(s) (6 * ((((s) + K0 - 1 < 7 ? (s) + K0 - 1 : 7) - (s))) + ((s) > 4 ? 2 * ((s) - 4) : 0))
#pragma unroll
        for (int ss = 0; ss < 8; ++ss) {
            if (ss == 0) waitv<P1WAIT(0)>();
            if (ss == 1) waitv<P1WAIT(1)>();
            if (ss == 2) waitv<P1WAIT(2)>();
            if (ss == 3) waitv<P1WAIT(3)>();
            if (ss == 4) waitv<P1WAIT(4)>();
            if (ss == 5) waitv<P1WAIT(5)>();
            if (ss == 6) waitv<P1WAIT(6)>();
            if (ss == 7) waitv<P1WAIT(7)>();
            STAMP2_SS(ss);
            BAR();
            if (ss == 0) { const u64 tv = *(LAS u64*)(lds + L_TAG); tag = UNIFORM64(tv); STAMP(2); }
            unsigned bw = (ss % 6) * SS_BYTES + tw;
            asm volatile("" : "+v"(bw));
            const u32x4 f0 = tr2(lds, bw, 256), f1 = tr2(lds, bw + 4096, 256);
            SB();
            if (ss < NL) ISSUE_SS(K0 + ss, lw);
            if (ss == 6) {
#pragma unroll
                for (int i = 0; i < 4; ++i) *(LAS u32x4*)(lds + L_VIMG + (4 * lw + i) * 1024 + lane * 16) = ones;
            }
            if (ss >= 4) {
                const int pc = 8 * lw + 2 * (ss - 4);
                const unsigned so = 16 * ((pc + 4 * r1) & 31) * 3072 + 64 * j;
                dma2(lb + L_WQ + pc * 1024, rbQ, vwk, vwk, so, so + 16 * 3072);
            }
            csacc = mfma16(f0, ones, csacc);
            csacc = mfma16(f1, ones, csacc);
            SB();
        }
        if ((lane & 15) == 0) *(LAS f32x4*)(lds + L_CS1 + (lw >> 1) * 128 + (lw & 1) * 64 + (lane >> 4) * 16) = csacc;
        STAMP(3);
        BAR();
        STAMP(4);
        const f32x2 st = *(LAS f32x2*)(lds + L_STAT + (32 * m + t) * 8);
        mean1 = st[0]; rstd1 = st[1];
#pragma unroll
        for (int g = 0; g < 4; ++g) {
            const f32x4 cs = *(LAS f32x4*)(lds + L_CS1 + 128 + (8 * g + 4 * h2) * 4);
            const f32x4 v = *(LAS f32x4*)(lds + L_XCH + m * 4096 + g * 1024 + lane * 16);
#pragma unroll
            for (int e = 0; e < 4; ++e) { const float y = __builtin_fmaf(-mean1, cs[e], v[e]); yk[4 * g + e] = g < 2 ? y : y * rstd1; }
        }
    }
    const unsigned imgA = L_PHI + a * 8192 + (32 * m + t) * 64 + ((h2 ^ ((t >> 1) & 7)) * 8);
    const u32x4 aphi = tr2(lds, L_WPHI + a * 512 + (4 * h2 + q) * 32 + pp * 8, 256);
    {
        const u32x4 kf = {pack2(yk[0], yk[1]), pack2(yk[2], yk[3]), pack2(yk[4], yk[5]), pack2(yk[6], yk[7])};
        const f32x16 zero = {};
        const f32x16 pk = mfma(aphi, kf, zero);
        *(LAS u32x2*)(lds + imgA + (L_VIMG - L_PHI)) = (u32x2){pack2(yk[8], yk[9]), pack2(yk[10], yk[11])};
        *(LAS u32x2*)(lds + (imgA ^ 16) + (L_VIMG - L_PHI)) = (u32x2){pack2(yk[12], yk[13]), pack2(yk[14], yk[15])};
        u32x4 pos, neg;
        dual_softmax(pk, rstd1 * LOG2E, pos, neg);
        *(LAS u32x2*)(lds + imgA) = (u32x2){pos[0], pos[1]};
        *(LAS u32x2*)(lds + (imgA ^ 16)) = (u32x2){pos[2], pos[3]};
        *(LAS u32x2*)(lds + (imgA ^ 32)) = (u32x2){neg[0], neg[1]};
        *(LAS u32x2*)(lds + (imgA ^ 48)) = (u32x2){neg[2], neg[3]};
    }
    if (w >= 4) waitv<0>();
    BAR();
    STAMP(5);
    float yq[8];
    if (w < 4) {
        unsigned tq = L_WQ + trn + blk * 32;
        asm volatile("" : "+v"(tq));
        f32x16 qa = {};
        u32x4 qb[2][4];
#pragma unroll
        for (int s = 0; s < 4; ++s) qb[0][s] = tr2(lds, tq + s * 1024, 256);
        SB();
#pragma unroll
        for (int s = 0; s < 32; ++s) {
            qa = mfma(qb[(s >> 2) & 1][s & 3], xf[s], qa);
            if (s < 28) qb[((s >> 2) + 1) & 1][s & 3] = tr2(lds, tq + (s + 4) * 1024, 256);
            SB();
        }
#pragma unroll
        for (int g = 0; g < 2; ++g) *(LAS f32x4*)(lds + L_XCH + m * 4096 + g * 1024 + lane * 16) = (f32x4){qa[8 + 4 * g], qa[9 + 4 * g], qa[10 + 4 * g], qa[11 + 4 * g]};
        STAMP(6);
        BAR();
#pragma unroll
        for (int g = 0; g < 2; ++g) {
            const f32x4 c0 = *(LAS f32x4*)(lds + L_CSQ + (8 * g + 4 * h2) * 4);
#pragma unroll
            for (int e = 0; e < 4; ++e) yq[4 * g + e] = __builtin_fmaf(-mean1, c0[e], qa[4 * g + e]);
        }
    } else {
        if (lw >= 2) {
            __builtin_amdgcn_s_setprio(3);
            const int ah = lw - 2;
            f32x16 kv = {};
            unsigned aa[2], ab[2];
#pragma unroll
            for (int u = 0; u < 2; ++u) {
                const unsigned R = 8 * h2 + 4 * u + q, sw = (R >> 1) & 7;
                aa[u] = L_PHI + ah * 8192 + R * 64 + (((4 * blk + pp) ^ sw) * 8);
                ab[u] = aa[u] + (L_VIMG - L_PHI);
                asm volatile("" : "+v"(aa[u]), "+v"(ab[u]));
            }
            u32x4 A[8], B[8];
#pragma unroll
            for (int s = 0; s < 8; ++s) {
                const s16x4 a0 = __builtin_amdgcn_ds_read_tr16_b64_v4i16((LAS s16x4*)(lds + aa[0] + s * 1024));
                const s16x4 a1 = __builtin_amdgcn_ds_read_tr16_b64_v4i16((LAS s16x4*)(lds + aa[1] + s * 1024));
                const s16x4 b0 = __builtin_amdgcn_ds_read_tr16_b64_v4i16((LAS s16x4*)(lds + ab[0] + s * 1024));
                const s16x4 b1 = __builtin_amdgcn_ds_read_tr16_b64_v4i16((LAS s16x4*)(lds + ab[1] + s * 1024));
                A[s] = __builtin_bit_cast(u32x4, __builtin_shufflevector(a0, a1, 0, 1, 2, 3, 4, 5, 6, 7));
                B[s] = __builtin_bit_cast(u32x4, __builtin_shufflevector(b0, b1, 0, 1, 2, 3, 4, 5, 6, 7));
                if (s == 3) PIN();
            }
            PIN();
#pragma unroll
            for (int s = 0; s < 8; ++s) kv = mfma(A[s], B[s], kv);
            if (t <= 16) {
                const unsigned base = WS_KVP + (j * 16 + p) * KVP_BYTES + ((ah * 4 + h2) * 17 + t) * 16;
                st16(rsW, (u32x4){pack2(kv[0], kv[1]), pack2(kv[2], kv[3]), pack2(kv[4], kv[5]), pack2(kv[6], kv[7])}, base, 0);
                st16(rsW, (u32x4){pack2(kv[8], kv[9]), pack2(kv[10], kv[11]), pack2(kv[12], kv[13]), pack2(kv[14], kv[15])}, base + 34 * 16, 0);
            }
            WAITV(0);
            arrive_publish(lds, 0, 2, ws, WS_F1 + (j * 16 + p) * 256, tag, lane);
            __builtin_amdgcn_s_setprio(0);
            STAMP(6);
        } else {
            unsigned tq = L_WQ + lw * 32 + (8 * (lane >> 4) + q) * 64 + pp * 8;
            asm volatile("" : "+v"(tq));
            f32x4 qcs = {};
#pragma unroll
            for (int g = 0; g < 4; ++g) {
                u32x4 f[4];
#pragma unroll
                for (int s = 0; s < 4; ++s) f[s] = tr2(lds, tq + (4 * g + s) * 2048, 256);
                PIN();
#pragma unroll
                for (int s = 0; s < 4; ++s) qcs = mfma16(f[s], ones, qcs);
            }
            if ((lane & 15) == 0) *(LAS f32x4*)(lds + L_CSQ + lw * 64 + (lane >> 4) * 16) = qcs;
            STAMP(6);
        }
        BAR();
        {
            const rsrc_t rsO = biased_rsrc(wo, 512 * 512 * 2);
#pragma unroll
            for (int g = 0; g < 4; ++g) {
                const int i = 4 * g + lw;
                const unsigned so = 16 * ((i + r2) & 15) * 1024 + 128 * c;
                dma4(lb + L_X + i * 4096, rsO, rsO, rsO, rsO, vrow, vrow, so, so + 8 * 1024, so + 256 * 1024, so + 264 * 1024);
            }
        }
#pragma unroll
        for (int g = 0; g < 2; ++g) {
            const f32x4 c0 = *(LAS f32x4*)(lds + L_CSQ + (16 + 8 * g + 4 * h2) * 4);
            const f32x4 v = *(LAS f32x4*)(lds + L_XCH + m * 4096 + g * 1024 + lane * 16);
#pragma unroll
            for (int e = 0; e < 4; ++e) yq[4 * g + e] = __builtin_fmaf(-mean1, c0[e], v[e]);
        }
    }
    STAMP(7);
    u32x4 qpos, qneg;
    {
        const u32x4 qf = {pack2(yq[0], yq[1]), pack2(yq[2], yq[3]), pack2(yq[4], yq[5]), pack2(yq[6], yq[7])};
        const f32x16 zero = {};
        const f32x16 pq = mfma(aphi, qf, zero);
        dual_softmax(pq, rstd1 * LOG2E, qpos, qneg);
    }
    STAMP(8);
    unsigned kso[16];
#pragma unroll
    for (int i = 0; i < 16; ++i) { kso[i] = WS_KVP + (j * 16 + ((i + 4 * q4) & 15)) * KVP_BYTES; asm volatile("" : "+s"(kso[i])); }
    if (w == 0) poll_flags<PD1>(ws, WS_F1 + j * 16 * 256, 16, tag, lane);
    BAR();
    STAMP(9);
    {
        const unsigned kb = WS_KVP + j * 16 * KVP_BYTES;
        float lo = 0.f, hi = 0.f;
        unsigned v[16], z[8];
#pragma unroll
        for (int i = 0; i < 16; ++i) v[i] = ld4(rsW, tid * 4, kso[i]);
        if (w == 0) {
#pragma unroll
            for (int i = 0; i < 8; ++i) z[i] = ld4(rsW, (512 + t) * 4 + h2 * 8 * KVP_BYTES, kb + i * KVP_BYTES);
        }
        PIN();
#pragma unroll
        for (int i = 0; i < 16; ++i) { lo += blo(v[i]); hi += bhi(v[i]); }
        if (w == 0) {
            float zl = 0.f, zh = 0.f;
#pragma unroll
            for (int i = 0; i < 8; ++i) { zl += blo(z[i]); zh += bhi(z[i]); }
            zl = halves_add(zl); zh = halves_add(zh);
            if (h2 == 0) *(LAS unsigned*)(lds + L_KVA + (512 + t) * 4) = pack2(zl, zh);
        }
        *(LAS unsigned*)(lds + L_KVA + tid * 4) = pack2(lo, hi);
    }
    BAR();
    STAMP(10);
    const int npass = has3 ? 3 : 2;
    const int m2 = w & 1, kh = (w >> 1) & 1;
    const int T2 = 4 * pP + 2 * half + m2;
    const unsigned C0 = 64 * c + 32 * kh;
    {
        const unsigned col = t < 16 ? t : 16;
        const u32x4 kp = *(LAS u32x4*)(lds + L_KVA + ((a * 4 + h2) * 17 + col) * 16);
        const u32x4 kn = *(LAS u32x4*)(lds + L_KVA + ((a * 4 + 2 + h2) * 17 + col) * 16);
        f32x16 o = {};
        o = mfma(kp, qpos, o);
        o = mfma(kn, qneg, o);
        const float r = __builtin_amdgcn_rcpf(o[8] + 1e-6f);
        st16(rsW, (u32x4){pack2(o[0] * r, o[1] * r), pack2(o[2] * r, o[3] * r), pack2(o[4] * r, o[5] * r), pack2(o[6] * r, o[7] * r)},
             WS_O + ((4 * p + m) * 32 + 2 * j + a) * 1024 + lane * 16, 0);
        WAITV(0);
        BAR();
        if (tid == 0) __hip_atomic_store((GAS u64*)(ws + WS_F2 + (p * 16 + j) * 256), tag, __ATOMIC_RELAXED, __HIP_MEMORY_SCOPE_AGENT);
        SBAR();
    }
    const unsigned trp = (4 * h2 + q) * 64 + blk * 32 + pp * 8;
    const unsigned t128 = (4 * h2 + q) * 128 + (q >> 1) * 64 + blk * 32 + pp * 8;

    if (w >= 4) {
        ISSUE_STAGE(0, L_Y, 0, 4);
        STAMP(11);
    }
    if (w >= 4) {
        const rsrc_t rsD = biased_rsrc(wd, 1376 * 512 * 2);
#define ISSUE_WD(pi, halfbase) do { _Pragma("unroll") for (int g_ = 0; g_ < ((pi) == 2 ? 3 : 4); ++g_) { const int i_ = 4 * g_ + lw; if ((pi) < 2 || i_ < 11) { \
                const int ii_ = (pi) == 2 ? (i_ + r4 >= 11 ? i_ + r4 - 11 : i_ + r4) : ((i_ + r2) & 15); \
                const unsigned sa_ = 16 * (((pi) == 2 ? 64 : 32 * (pi)) + ii_) * 1024 + 128 * c, sb_ = sa_ + 16 * ((pi) == 2 ? 11 : 16) * 1024; \
                dma4(lb + (halfbase) + i_ * 4096, rsD, rsD, rsD, rsD, vrow, vrow, sa_, sa_ + 8 * 1024, sb_, sb_ + 8 * 1024); } } } while (0)
        f32x4 cs0 = {}, cs1 = {};
        const unsigned gam = lane >> 4;
        const unsigned ca0 = (lw >> 1) * 2048 + (gam >> 1) * 4096 + (8 * (gam & 1) + q) * 128 + (((lw & 1) * 64) ^ (64 * (q >> 1))) + pp * 8;
#define COLSUM_STAGE(halfbase) do { unsigned cb_ = (halfbase) + ca0; asm volatile("" : "+v"(cb_)); \
            _Pragma("unroll") for (int g_ = 0; g_ < 4; ++g_) { const u32x4 f0_ = tr2(lds, cb_ + g_ * 16384, 512), f1_ = tr2(lds, cb_ + g_ * 16384 + 32, 512), \
                                                                         f2_ = tr2(lds, cb_ + g_ * 16384 + 8192, 512), f3_ = tr2(lds, cb_ + g_ * 16384 + 8192 + 32, 512); \
                PIN(); cs0 = mfma16(f0_, ones, cs0); cs1 = mfma16(f1_, ones, cs1); cs0 = mfma16(f2_, ones, cs0); cs1 = mfma16(f3_, ones, cs1); } } while (0)
        BAR();
        STAMP(12);
        BAR();
        SBAR();
        ISSUE_STAGE(1, L_X, 0, B_EARLY);
        BAR();
        SBAR();
        ISSUE_STAGE(1, L_X, B_EARLY, 4);
        STAMP(13);
        waitv<16>();
        BAR();
        COLSUM_STAGE(L_Y);
        WAITV(0);
        lsig(lb, 8); lwait(lds, 8, 4);
        COLSUM_STAGE(L_X);
        if ((lane & 15) == 0) {
            *(LAS f32x4*)(lds + L_CS3 + ((lw & 1) * 64 + (lw >> 1) * 32 + 4 * gam) * 4) = cs0;
            *(LAS f32x4*)(lds + L_CS3 + ((lw & 1) * 64 + (lw >> 1) * 32 + 16 + 4 * gam) * 4) = cs1;
        }
        BAR();
        if (npass == 3) {
            ISSUE_UNIT(u3, L_Y);
            WAITV(0);
            SBAR();
            ISSUE_WD(0, L_X);
            {
                unsigned cb = L_Y + (lw >> 1) * 1024 + (gam >> 1) * 2048 + (8 * (gam & 1) + q) * 64 + (lw & 1) * 32 + pp * 8;
                asm volatile("" : "+v"(cb));
                f32x4 c3 = {};
#pragma unroll
                for (int g = 0; g < 4; ++g) {
                    u32x4 f[4];
#pragma unroll
                    for (int e = 0; e < 4; ++e) f[e] = tr2(lds, cb + (4 * g + e) * 4096, 256);
                    PIN();
#pragma unroll
                    for (int e = 0; e < 4; ++e) c3 = mfma16(f[e], ones, c3);
                }
                if ((lane & 15) == 0) *(LAS f32x4*)(lds + L_CS3 + (128 + (lw >> 1) * 32 + (lw & 1) * 16 + 4 * gam) * 4) = c3;
            }
            BAR();
            WAITV(0);
            BAR();
            if (lw == 0) {
                lwait(lds, 11, 4);
                if (lane == 0) __hip_atomic_store((GAS u64*)(ws + WS_F4 + 0x10000 + (p * 16 + j) * 256), tag, __ATOMIC_RELAXED, __HIP_MEMORY_SCOPE_AGENT);
            }
            SBAR();
            ISSUE_WD(1, L_Y);
            WAITV(0);
            BAR();
            if (lw == 0) { poll_flags3(ws, WS_F4 + 0x10000 + pP * 16 * 256, tag, lane); lsig(lb, 14); }
            ISSUE_WD(2, L_X);
            WAITV(0);
            BAR();
        } else {
            ISSUE_WD(0, L_Y);
            SBAR();
            ISSUE_WD(1, L_X);
            WAITV(0);
            BAR();
            SBAR();
            BAR();
            if (lw == 0) { poll_flags3(ws, WS_F4 + 0x10000 + pP * 16 * 256, tag, lane); lsig(lb, 14); }
            ISSUE_WD(2, L_Y);
            WAITV(0);
            BAR();
        }
        BAR();
    } else {
        if (PRIO_C) __builtin_amdgcn_s_setprio(PRIO_C);
        const unsigned vo = (32 * T2 + t) * 1024 + (C0 + 8 * h2) * 2;
        u32x4 xr0 = __builtin_amdgcn_raw_buffer_load_b128(rsX, vo, 0, 0);
        u32x4 xr1 = __builtin_amdgcn_raw_buffer_load_b128(rsX, vo + 32, 0, 0);
        PIN();
        STAMP(11);
        if (w == 0) poll_flags<PD2>(ws, WS_F2 + pP * 16 * 256, 16, tag, lane);
        BAR();
        STAMP(12);
        if (b == 0 && w == 1 && lane == 0) __hip_atomic_store((GAS u64*)ws, tag, __ATOMIC_RELAXED, __HIP_MEMORY_SCOPE_AGENT);
        {
            u32x4 hres0, hres1;
            u32x4 of[16];
#pragma unroll
            for (int i = 0; i < 8; ++i) of[i] = ld16(rsW, lane * 16, WS_O + (T2 * 32 + 16 * kh + ((i + r2) & 15)) * 1024);
            PIN();
            f32x16 keep, snd = {};
            { auto r = __builtin_amdgcn_permlane32_swap(xr0[0], xr0[2], false, false); xr0[0] = r[0]; xr0[2] = r[1];
              r = __builtin_amdgcn_permlane32_swap(xr0[1], xr0[3], false, false); xr0[1] = r[0]; xr0[3] = r[1];
              r = __builtin_amdgcn_permlane32_swap(xr1[0], xr1[2], false, false); xr1[0] = r[0]; xr1[2] = r[1];
              r = __builtin_amdgcn_permlane32_swap(xr1[1], xr1[3], false, false); xr1[1] = r[0]; xr1[3] = r[1]; }
#pragma unroll
            for (int e = 0; e < 4; ++e) { keep[2 * e] = blo(xr0[e]); keep[2 * e + 1] = bhi(xr0[e]); keep[8 + 2 * e] = blo(xr1[e]); keep[8 + 2 * e + 1] = bhi(xr1[e]); }
            asm volatile("" : "+v"(keep));
            gemm2<16, 4096, 1024, 0>(lds, L_X + kh * 2048 + (t128 ^ (64 * kh)), L_X + kh * 2048 + (t128 ^ (64 * (kh ^ 1))), of, keep, snd, [&](int g) {
                if (g < 4) {
#pragma unroll
                    for (int e = 0; e < 2; ++e) of[8 + 2 * g + e] = ld16(rsW, lane * 16, WS_O + (T2 * 32 + 16 * kh + ((8 + 2 * g + e + r2) & 15)) * 1024);
                    SB();
                } });
#pragma unroll
            for (int g = 0; g < 4; ++g) *(LAS f32x4*)(lds + L_XCH + w * 4096 + g * 1024 + lane * 16) = (f32x4){snd[4 * g], snd[4 * g + 1], snd[4 * g + 2], snd[4 * g + 3]};
            STAMP(13);
            BAR();
#pragma unroll
            for (int g = 0; g < 4; ++g) {
                const f32x4 v = *(LAS f32x4*)(lds + L_XCH + (w ^ 2) * 4096 + g * 1024 + lane * 16);
#pragma unroll
                for (int e = 0; e < 4; ++e) keep[4 * g + e] += v[e];
            }
#pragma unroll
            for (int e = 0; e < 4; ++e) { hres0[e] = pack2(keep[2 * e], keep[2 * e + 1]); hres1[e] = pack2(keep[8 + 2 * e], keep[8 + 2 * e + 1]); }
            const unsigned hb = WS_H + (T2 * 32 + 4 * c + 2 * kh) * 1024;
            st16(rsW, hres0, lane * 16, hb);
            st16(rsW, hres1, lane * 16, hb + 1024);
            *(LAS u32x4*)(lds + L_HRES + w * 2048 + lane * 16) = hres0;
            *(LAS u32x4*)(lds + L_HRES + w * 2048 + 1024 + lane * 16) = hres1;
            f32x2 s1 = {0.f, 0.f}, s2 = {0.f, 0.f};
#pragma unroll
            for (int e = 0; e < 8; ++e) { const f32x2 v = {keep[2 * e], keep[2 * e + 1]}; s1 += v; s2 += v * v; }
            float su = s1[0] + s1[1], sq = s2[0] + s2[1];
            su = halves_add(su); sq = halves_add(sq);
            __builtin_amdgcn_raw_buffer_store_b32(asu(h2 ? sq : su), rsW, lane * 4 + WS_HST + (T2 * 16 + 2 * c + kh) * 256, 0, 16);
            SBAR();
            WAITV(0);
            BAR();
            if (tid == 0) __hip_atomic_store((GAS u64*)(ws + WS_F3 + (pP * 16 + jP) * 256), tag, __ATOMIC_RELAXED, __HIP_MEMORY_SCOPE_AGENT);
            SBAR();
        }
        STAMP(14);
        if (w == 0) poll_flags<PD3>(ws, WS_F3 + p * 16 * 256, 16, tag, lane);
        BAR();
        STAMP(15);
        {
            const int T = 4 * p + w;
            u32x4 hf[32];
            float sv[16];
#pragma unroll
            for (int s = 0; s < 8; ++s) hf[s] = ld16(rsW, lane * 16, WS_H + (T * 32 + ((s + r3) & 31)) * 1024);
            PIN();
            float mean, r2;
            auto act_out = [&](const f32x16& ag, const f32x16& au, int cs, unsigned ks) {
                float y[16];
#pragma unroll
                for (int g = 0; g < 4; ++g) {
                    const f32x4 cg = *(LAS f32x4*)(lds + L_CS3 + (cs * 64 + 8 * g + 4 * h2) * 4), cu = *(LAS f32x4*)(lds + L_CS3 + (cs * 64 + 32 + 8 * g + 4 * h2) * 4);
#pragma unroll
                    for (int e = 0; e < 4; ++e) y[4 * g + e] = (__builtin_fmaf(-mean, cg[e], ag[4 * g + e]) * r2) * __builtin_fmaf(-mean, cu[e], au[4 * g + e]);
                }
                const unsigned ab = WS_ACT + (T * 86 + ks) * 1024;
                st16(rsW, (u32x4){pack2(y[0], y[1]), pack2(y[2], y[3]), pack2(y[4], y[5]), pack2(y[6], y[7])}, lane * 16, ab);
                st16(rsW, (u32x4){pack2(y[8], y[9]), pack2(y[10], y[11]), pack2(y[12], y[13]), pack2(y[14], y[15])}, lane * 16, ab + 1024);
            };
            {
                f32x16 g0 = {}, g1 = {}, u0 = {}, u1 = {};
                gemm4<0>(lds, L_Y + t128, L_Y + (t128 ^ 64), hf, g0, g1, u0, u1, [&](int i) {
                    if (i < 8) hf[8 + i] = ld16(rsW, lane * 16, WS_H + (T * 32 + ((8 + i + r3) & 31)) * 1024);
                    else { hf[2 * i] = ld16(rsW, lane * 16, WS_H + (T * 32 + ((2 * i + r3) & 31)) * 1024); hf[2 * i + 1] = ld16(rsW, lane * 16, WS_H + (T * 32 + ((2 * i + 1 + r3) & 31)) * 1024); }
                    if (i < 4) {
#pragma unroll
                        for (int k = 0; k < 4; ++k) sv[4 * i + k] = asf(ld4(rsW, lane * 4, WS_HST + (T * 16 + 4 * i + k) * 256));
                    }
                    if (i == 9) {
                        float tot = 0.f;
#pragma unroll
                        for (int k = 0; k < 16; ++k) tot += sv[k];
                        const u32x2 r = both_halves(tot);
                        mean = asf(r[0]) * (1.0f / 512.0f);
                        const float var = fmaxf(asf(r[1]) * (1.0f / 512.0f) - mean * mean, 0.f);
                        r2 = __builtin_amdgcn_rcpf(var + 1e-5f);
                        asm volatile("" : "+v"(mean), "+v"(r2));
                    }
                    SB(); });
                BAR();
                STAMP(16);
                gemm4<16>(lds, L_X + t128, L_X + (t128 ^ 64), hf, g0, g1, u0, u1, nohook);
                STAMP(17);
                act_out(g0, u0, 0, 4 * j);
                act_out(g1, u1, 1, 4 * j + 2);
                SBAR();
            }
            if (npass == 3) {
                f32x16 ag = {}, au = {};
                gemm2<32, 2048, 512, 0>(lds, L_Y + trp, L_Y + 1024 + trp, hf, ag, au, [&](int g) {
                    if (g == 3) { WAITV(0); arrive_publish(lds, 3, 4, ws, WS_F4 + (p * 16 + j) * 256, tag, lane); PIN(); } });
                BAR();
                STAMP(18);
                act_out(ag, au, 2, 2 * u3);
            } else {
                WAITV(0); arrive_publish(lds, 3, 4, ws, WS_F4 + (p * 16 + j) * 256, tag, lane);
            }
        }
        STAMP(19);
        {
            f32x16 a0, a1 = {};
            {
                const u32x4 hres0 = *(LAS u32x4*)(lds + L_HRES + w * 2048 + lane * 16), hres1 = *(LAS u32x4*)(lds + L_HRES + w * 2048 + 1024 + lane * 16);
#pragma unroll
                for (int e = 0; e < 4; ++e) { a0[2 * e] = blo(hres0[e]); a0[2 * e + 1] = bhi(hres0[e]); a0[8 + 2 * e] = blo(hres1[e]); a0[8 + 2 * e + 1] = bhi(hres1[e]); }
                asm volatile("" : "+v"(a0));
            }
            const unsigned tk = kh * 2048 + (t128 ^ (64 * kh)), ts = kh * 2048 + (t128 ^ (64 * (kh ^ 1)));
            const unsigned h0 = npass == 3 ? L_X : L_Y, h1 = npass == 3 ? L_Y : L_X;
            if (w == 0) poll_flags(ws, WS_F4 + pP * 16 * 256, 16, tag, lane);
            BAR();
            STAMP(20);
            u32x4 af[43];
            auto ld_act = [&](int i) {
                const int ks = i < 32 ? 32 * (i >> 4) + kh * 16 + ((i + r2) & 15) : 64 + kh * 11 + (i - 32 + r4 >= 11 ? i - 32 + r4 - 11 : i - 32 + r4);
                af[i] = ld16(rsW, lane * 16, WS_ACT + (T2 * 86 + ks) * 1024); };
            if (npass == 3) { WAITV(0); lsig(lb, 11); }
#pragma unroll
            for (int i = 0; i < 8; ++i) ld_act(i);
            SBAR();
            gemm2<16, 4096, 1024, 0>(lds, h0 + tk, h0 + ts, af, a0, a1, [&](int g) { ld_act(8 + 2 * g); ld_act(9 + 2 * g); SB(); });
            STAMP(21);
            BAR();
            STAMP(22);
            gemm2<16, 4096, 1024, 16>(lds, h1 + tk, h1 + ts, af, a0, a1, [&](int g) {
                if (g == 4) lwait(lds, 14, 1);
                ld_act(24 + 2 * g); ld_act(25 + 2 * g); SB(); });
            BAR();
            STAMP(23);
            gemm2<11, 4096, 1024, 32>(lds, h0 + tk, h0 + ts, af, a0, a1, [&](int g) { if (g < 2) { ld_act(40 + 2 * g < 43 ? 40 + 2 * g : 42); if (g == 0) ld_act(41); SB(); } });
#pragma unroll
            for (int g = 0; g < 4; ++g) *(LAS f32x4*)(lds + L_XCH + w * 4096 + g * 1024 + lane * 16) = (f32x4){a1[4 * g], a1[4 * g + 1], a1[4 * g + 2], a1[4 * g + 3]};
            BAR();
            STAMP(24);
#pragma unroll
            for (int g = 0; g < 4; ++g) {
                const f32x4 v = *(LAS f32x4*)(lds + L_XCH + (w ^ 2) * 4096 + g * 1024 + lane * 16);
#pragma unroll
                for (int e = 0; e < 4; ++e) a0[4 * g + e] += v[e];
            }
            u32x4 o0, o1;
#pragma unroll
            for (int e = 0; e < 4; ++e) { o0[e] = pack2(a0[2 * e], a0[2 * e + 1]); o1[e] = pack2(a0[8 + 2 * e], a0[8 + 2 * e + 1]); }
            { auto r = __builtin_amdgcn_permlane32_swap(o0[0], o0[2], false, false); o0[0] = r[0]; o0[2] = r[1];
              r = __builtin_amdgcn_permlane32_swap(o0[1], o0[3], false, false); o0[1] = r[0]; o0[3] = r[1];
              r = __builtin_amdgcn_permlane32_swap(o1[0], o1[2], false, false); o1[0] = r[0]; o1[2] = r[1];
              r = __builtin_amdgcn_permlane32_swap(o1[1], o1[3], false, false); o1[1] = r[0]; o1[3] = r[1]; }
            const rsrc_t rsOut = __builtin_amdgcn_make_buffer_rsrc((void*)out, 0, 2048 * 512 * 2, 0x00020000);
            int tz = threadIdx.x; asm volatile("" : "+v"(tz));
            const unsigned vz = (32 * T2 + (tz & 31)) * 1024 + (C0 + 8 * ((tz >> 5) & 1)) * 2;
            __builtin_amdgcn_raw_buffer_store_b128(o0, rsOut, vz, 0, 0);
            __builtin_amdgcn_raw_buffer_store_b128(o1, rsOut, vz + 32, 0, 0);
        }
        STAMP(25); CSTAMP(31);
    }
}

extern "C" void kernel_launch(void* const* d_in, const int* in_sizes, int n_in, void* d_out, int out_size, void* d_ws, size_t ws_size, hipStream_t stream) {
    (void)in_sizes; (void)n_in; (void)out_size; (void)ws_size;
    (void)hipFuncSetAttribute((const void*)hedgehog_block, hipFuncAttributeMaxDynamicSharedMemorySize, LDS_BYTES);
    hedgehog_block<<<256, 512, LDS_BYTES, stream>>>((const bf16_t*)d_in[0], (const bf16_t*)d_in[1], (const bf16_t*)d_in[2], (const bf16_t*)d_in[3],
                                                    (const bf16_t*)d_in[5], (const bf16_t*)d_in[6], (const bf16_t*)d_in[7], (bf16_t*)d_out, (char*)d_ws);
}
```

```cpp
#include <hip/hip_runtime.h>
#include <hip/hip_bf16.h>

#define LAS __attribute__((address_space(3)))
#define GAS __attribute__((address_space(1)))
#define DI __device__ __forceinline__
typedef unsigned short bf16_t;
typedef unsigned u32x4 __attribute__((ext_vector_type(4)));
typedef unsigned u32x2 __attribute__((ext_vector_type(2)));
typedef short s16x4 __attribute__((ext_vector_type(4)));
typedef __bf16 bf16x8 __attribute__((ext_vector_type(8)));
typedef __bf16 bf16x2 __attribute__((ext_vector_type(2)));
typedef float f32x16 __attribute__((ext_vector_type(16)));
typedef float f32x4 __attribute__((ext_vector_type(4)));
typedef float f32x2 __attribute__((ext_vector_type(2)));
typedef unsigned long long u64;
typedef __amdgpu_buffer_rsrc_t rsrc_t;

constexpr unsigned WS_F1 = 0x10000, WS_F2 = 0x20000, WS_F3 = 0x30000, WS_F4 = 0x40000;
constexpr unsigned WS_KVP = 0x80000, KVP_BYTES = 2176;
constexpr unsigned WS_HST = 0x110000;
constexpr unsigned WS_XST = 0x160000;
constexpr unsigned WS_O = 0x200000, WS_H = 0x400000, WS_ACT = 0x600000, WS_PROF = 0xC00000, WS_BYTES = 0x1000000;
constexpr unsigned L_X = 0, L_Y = 65536, L_XCH = 131072, L_MISC = 147456;
constexpr unsigned L_STAT = L_MISC, L_CS1 = L_MISC + 1024, L_CSQ = L_MISC + 1280, L_WPHI = L_MISC + 1536, L_KVA = L_MISC + 2560, L_CS3 = L_MISC + 4736, L_CNT = L_MISC + 5504, L_TAG = L_MISC + 6016, L_DUMMY = L_MISC + 6144, L_HRES = L_MISC + 8192;
constexpr unsigned L_WQ = 65536, L_PHI = 98304, L_VIMG = 114688  , SS_BYTES = 24576;
constexpr unsigned LDS_BYTES = 163840;
constexpr unsigned SPIN_CAP = 40000;
constexpr float LOG2E = 1.4426950408889634f;

DI float asf(unsigned v) { return __builtin_bit_cast(float, v); }
DI unsigned asu(float v) { return __builtin_bit_cast(unsigned, v); }
DI unsigned pack2(float lo, float hi) { bf16x2 v = {(__bf16)lo, (__bf16)hi}; return __builtin_bit_cast(unsigned, v); }
DI float blo(unsigned v) { return __builtin_bit_cast(float, v << 16); }
DI float bhi(unsigned v) { return __builtin_bit_cast(float, v & 0xffff0000u); }
DI f32x16 mfma(u32x4 a, u32x4 b, f32x16 c) { return __builtin_amdgcn_mfma_f32_32x32x16_bf16(__builtin_bit_cast(bf16x8, a), __builtin_bit_cast(bf16x8, b), c, 0, 0, 0); }
DI f32x4 mfma16(u32x4 a, u32x4 b, f32x4 c) { return __builtin_amdgcn_mfma_f32_16x16x32_bf16(__builtin_bit_cast(bf16x8, a), __builtin_bit_cast(bf16x8, b), c, 0, 0, 0); }
DI u32x4 tr2(LAS char* lds, unsigned off, unsigned second) {
    const s16x4 lo = __builtin_amdgcn_ds_read_tr16_b64_v4i16((LAS s16x4*)(lds + off));
    const s16x4 hi = __builtin_amdgcn_ds_read_tr16_b64_v4i16((LAS s16x4*)(lds + off + second));
    return __builtin_bit_cast(u32x4, __builtin_shufflevector(lo, hi, 0, 1, 2, 3, 4, 5, 6, 7));
}
DI void dma16(const rsrc_t rs, unsigned ldsaddr, unsigned voff, unsigned soff) {
    asm volatile("s_mov_b32 m0, %0\n\ts_nop 0\n\tbuffer_load_dwordx4 %1, %2, %3 offen lds" ::"s"(ldsaddr), "v"(voff), "s"(rs), "s"(soff) : "memory");
}
constexpr unsigned BIAS = 4096;
DI rsrc_t biased_rsrc(const void* p, unsigned bytes) { return __builtin_amdgcn_make_buffer_rsrc((void*)((const char*)p - BIAS), 0, bytes + BIAS, 0x00020000); }
DI void dma4(unsigned ldsaddr, const rsrc_t ra, const rsrc_t rb, const rsrc_t rc, const rsrc_t rd, unsigned va, unsigned vb, unsigned s0, unsigned s1, unsigned s2, unsigned s3) {
    asm volatile("s_mov_b32 m0, %0\n\ts_nop 0\n\t"
                 "buffer_load_dwordx4 %5, %1, %7 offen lds\n\tbuffer_load_dwordx4 %6, %2, %8 offen offset:1024 lds\n\t"
                 "buffer_load_dwordx4 %5, %3, %9 offen offset:2048 lds\n\tbuffer_load_dwordx4 %6, %4, %10 offen offset:3072 lds"
                 ::"s"(ldsaddr), "s"(ra), "s"(rb), "s"(rc), "s"(rd), "v"(va), "v"(vb), "s"(s0 + BIAS), "s"(s1 + BIAS - 1024), "s"(s2 + BIAS - 2048), "s"(s3 + BIAS - 3072) : "memory");
}
DI void dma2(unsigned ldsaddr, const rsrc_t r, unsigned va, unsigned vb, unsigned s0, unsigned s1) {
    asm volatile("s_mov_b32 m0, %0\n\ts_nop 0\n\tbuffer_load_dwordx4 %2, %1, %4 offen lds\n\tbuffer_load_dwordx4 %3, %1, %5 offen offset:1024 lds"
                 ::"s"(ldsaddr), "s"(r), "v"(va), "v"(vb), "s"(s0 + BIAS), "s"(s1 + BIAS - 1024) : "memory");
}
#define WAITV(n) asm volatile("s_waitcnt vmcnt(" #n ")" ::: "memory")
#define PIN() do { asm volatile("" ::: "memory"); __builtin_amdgcn_sched_barrier(0); } while (0)
template <int N> DI void waitv() { asm volatile("s_waitcnt vmcnt(%0)" ::"n"(N) : "memory"); }
#define B_EARLY 1
#define PD1 0
#define PD2 4
#define PD3 4
#define PRIO_C 0
#define K0 3
#define R1MODE 2
#define STAMP2_SS(ss) do { if (ss == 0) STAMP2(0); if (ss == 1) STAMP2(1); if (ss == 2) STAMP2(2); if (ss == 3) STAMP2(3); if (ss == 4) STAMP2(4); if (ss == 5) STAMP2(5); if (ss == 6) STAMP2(6); if (ss == 7) STAMP2(7); } while (0)
#define SBAR() asm volatile("s_barrier" ::: "memory")
#define BAR() asm volatile("s_waitcnt lgkmcnt(0)\n\ts_barrier" ::: "memory")
DI u32x2 both_halves(float v) { const unsigned a = asu(v); const auto r = __builtin_amdgcn_permlane32_swap(a, a, false, false); return (u32x2){r[0], r[1]}; }
DI float halves_add(float v) { const u32x2 r = both_halves(v); return asf(r[0]) + asf(r[1]); }
DI float halves_max(float v) { const u32x2 r = both_halves(v); return fmaxf(asf(r[0]), asf(r[1])); }
DI float halves_min(float v) { const u32x2 r = both_halves(v); return fminf(asf(r[0]), asf(r[1])); }
DI float dpp_ror4(float v) { return asf(__builtin_amdgcn_update_dpp(0, (int)asu(v), 0x124, 0xf, 0xf, true)); }
DI float dpp_ror8(float v) { return asf(__builtin_amdgcn_update_dpp(0, (int)asu(v), 0x128, 0xf, 0xf, true)); }
DI float rows16_add(float v) { const unsigned a = asu(v); const auto r = __builtin_amdgcn_permlane16_swap(a, a, false, false); return asf(r[0]) + asf(r[1]); }
DI float stride4_add(float v) { v += dpp_ror4(v); v += dpp_ror8(v); v = rows16_add(v); return halves_add(v); }
DI float quad_add(float v) {
    v += __builtin_bit_cast(float, __builtin_amdgcn_update_dpp(0, __builtin_bit_cast(int, v), 0xB1, 0xf, 0xf, true));
    v += __builtin_bit_cast(float, __builtin_amdgcn_update_dpp(0, __builtin_bit_cast(int, v), 0x4E, 0xf, 0xf, true));
    return v;
}
DI void st16(const rsrc_t rs, u32x4 v, unsigned voff, unsigned soff) { __builtin_amdgcn_raw_buffer_store_b128(v, rs, voff + soff, 0, 16); }
DI u32x4 ld16(const rsrc_t rs, unsigned voff, unsigned soff) { return __builtin_amdgcn_raw_buffer_load_b128(rs, voff, soff, 16); }
DI unsigned ld4(const rsrc_t rs, unsigned voff, unsigned soff) { return __builtin_amdgcn_raw_buffer_load_b32(rs, voff, soff, 16); }

DI void poll_flags3(const char* ws, unsigned off, u64 tag, int lane) {
    const int l = lane < 11 ? lane : 10;
    const GAS u64* f = (const GAS u64*)(ws + off) + (l < 6 ? l : l + 2) * 32;
#pragma unroll 1
    for (unsigned spin = 0; spin < SPIN_CAP; ++spin) {
        const u64 v = __hip_atomic_load(f, __ATOMIC_RELAXED, __HIP_MEMORY_SCOPE_AGENT);
        if (__all(v == tag)) break;
        __builtin_amdgcn_s_sleep(1);
    }
    asm volatile("" ::: "memory");
}
DI void nohook(int) {}
template <int DELAY = 0>
DI void poll_flags(const char* ws, unsigned off, int n, u64 tag, int lane) {
#pragma unroll
    for (int i = 0; i < DELAY; ++i) __builtin_amdgcn_s_sleep(2);
    const GAS u64* f = (const GAS u64*)(ws + off) + (lane < n ? lane : n - 1) * 32;
#pragma unroll 1
    for (unsigned spin = 0; spin < SPIN_CAP; ++spin) {
        const u64 v = __hip_atomic_load(f, __ATOMIC_RELAXED, __HIP_MEMORY_SCOPE_AGENT);
        if (__all(v == tag)) break;
        __builtin_amdgcn_s_sleep(1);
    }
    asm volatile("" ::: "memory");
}
DI void arrive_publish(LAS char* lds, int cnt, unsigned n, char* ws, unsigned flag_off, u64 tag, int lane) {
    if (lane == 0) {
        const unsigned old = __hip_atomic_fetch_add((LAS unsigned*)(lds + L_CNT) + cnt, 1u, __ATOMIC_RELAXED, __HIP_MEMORY_SCOPE_WORKGROUP);
        if (old == n - 1) __hip_atomic_store((GAS u64*)(ws + flag_off), tag, __ATOMIC_RELAXED, __HIP_MEMORY_SCOPE_AGENT);
    }
}
DI void lsig(unsigned lb, int idx) {
    const unsigned a = lb + L_CNT + idx * 4, one = 1;
    asm volatile("s_mov_b64 exec, 1\n\tds_add_u32 %0, %1\n\ts_mov_b64 exec, -1" ::"v"(a), "v"(one) : "memory");
}
DI void lwait(LAS char* lds, int idx, unsigned n) {
    const volatile LAS unsigned* c = (const volatile LAS unsigned*)(lds + L_CNT) + idx;
#pragma unroll 1
    for (unsigned spin = 0; spin < 64 * SPIN_CAP; ++spin) {
        if ((unsigned)__builtin_amdgcn_readfirstlane(*c) >= n) break;
        __builtin_amdgcn_s_sleep(1);
    }
    asm volatile("" ::: "memory");
}
DI void dual_softmax(const f32x16& p, float sl, u32x4& pos, u32x4& neg) {
    float mx = fmaxf(fmaxf(fmaxf(p[0], p[1]), fmaxf(p[2], p[3])), fmaxf(fmaxf(p[4], p[5]), fmaxf(p[6], p[7])));
    float mn = fminf(fminf(fminf(p[0], p[1]), fminf(p[2], p[3])), fminf(fminf(p[4], p[5]), fminf(p[6], p[7])));
    mx = halves_max(mx); mn = halves_min(mn);
    const float cx = -mx * sl, cn = mn * sl;
    f32x2 ep[4], en[4];
#pragma unroll
    for (int i = 0; i < 4; ++i) {
        const f32x2 v = {p[2 * i], p[2 * i + 1]};
        const f32x2 u = v * sl + cx, d = v * (-sl) + cn;
        ep[i] = (f32x2){__builtin_amdgcn_exp2f(u[0]), __builtin_amdgcn_exp2f(u[1])};
        en[i] = (f32x2){__builtin_amdgcn_exp2f(d[0]), __builtin_amdgcn_exp2f(d[1])};
    }
    const f32x2 sp = (ep[0] + ep[1]) + (ep[2] + ep[3]), sn = (en[0] + en[1]) + (en[2] + en[3]);
    const float rp = __builtin_amdgcn_rcpf(halves_add(sp[0] + sp[1])), rn = __builtin_amdgcn_rcpf(halves_add(sn[0] + sn[1]));
#pragma unroll
    for (int i = 0; i < 4; ++i) { const f32x2 a = ep[i] * rp, b = en[i] * rn; pos[i] = pack2(a[0], a[1]); neg[i] = pack2(b[0], b[1]); }
}

#define SB() __builtin_amdgcn_sched_barrier(0)
template <int N, int STRIDE, int SECOND, int OFF, int NA, class Hook>
DI void gemm2(LAS char* lds, unsigned b0, unsigned b1, const u32x4 (&act)[NA], f32x16& acc0, f32x16& acc1, Hook hook) {
    u32x4 fb[2][4];
    asm volatile("" : "+v"(b0), "+v"(b1));
    constexpr int NG = (N + 1) / 2;
#pragma unroll
    for (int e = 0; e < 2; ++e) { fb[0][2 * e] = tr2(lds, b0 + e * STRIDE, SECOND); fb[0][2 * e + 1] = tr2(lds, b1 + e * STRIDE, SECOND); }
    SB();
#pragma unroll
    for (int g = 0; g < NG; ++g) {
#pragma unroll
        for (int e = 0; e < 2; ++e) {
            const int i = 2 * g + e, n = i + 2;
            if (i < N) acc0 = mfma(fb[g & 1][2 * e], act[OFF + i], acc0);
            if (n < N) fb[(g + 1) & 1][2 * e] = tr2(lds, b0 + n * STRIDE, SECOND);
            SB();
            if (i < N) acc1 = mfma(fb[g & 1][2 * e + 1], act[OFF + i], acc1);
            if (n < N) fb[(g + 1) & 1][2 * e + 1] = tr2(lds, b1 + n * STRIDE, SECOND);
            SB();
        }
        hook(g);
    }
    asm volatile("" : "+v"(acc0), "+v"(acc1));
}
template <int OFF, class Hook>
DI void gemm4(LAS char* lds, unsigned b0, unsigned b1, const u32x4 (&act)[32], f32x16& g0, f32x16& g1, f32x16& u0, f32x16& u1, Hook hook) {
    u32x4 fb[2][4];
    asm volatile("" : "+v"(b0), "+v"(b1));
    fb[0][0] = tr2(lds, b0, 1024); fb[0][1] = tr2(lds, b1, 1024); fb[0][2] = tr2(lds, b0 + 2048, 1024); fb[0][3] = tr2(lds, b1 + 2048, 1024);
    SB();
#pragma unroll
    for (int i = 0; i < 16; ++i) {
        const int o = (i + 1) * 4096;
        g0 = mfma(fb[i & 1][0], act[OFF + i], g0);
        if (i < 15) fb[(i + 1) & 1][0] = tr2(lds, b0 + o, 1024);
        SB();
        g1 = mfma(fb[i & 1][1], act[OFF + i], g1);
        if (i < 15) fb[(i + 1) & 1][1] = tr2(lds, b1 + o, 1024);
        SB();
        u0 = mfma(fb[i & 1][2], act[OFF + i], u0);
        if (i < 15) fb[(i + 1) & 1][2] = tr2(lds, b0 + o + 2048, 1024);
        SB();
        u1 = mfma(fb[i & 1][3], act[OFF + i], u1);
        if (i < 15) fb[(i + 1) & 1][3] = tr2(lds, b1 + o + 2048, 1024);
        SB();
        hook(i);
    }
    asm volatile("" : "+v"(g0), "+v"(g1), "+v"(u0), "+v"(u1));
}
DI void touch_lines(const rsrc_t rs, unsigned ldsaddr, unsigned voff, unsigned soff) {
    asm volatile("s_mov_b32 m0, %0\n\ts_nop 0\n\tbuffer_load_dword %1, %2, %3 offen lds" ::"s"(ldsaddr), "v"(voff), "s"(rs), "s"(soff) : "memory");
}

#define STAMP(i) do {} while (0)
#define CSTAMP(i) do {} while (0)
#define STAMP2(i) do {} while (0)

__global__ void __launch_bounds__(512, 2)
hedgehog_block(const bf16_t* __restrict__ x, const bf16_t* __restrict__ wqkv, const bf16_t* __restrict__ wphi, const bf16_t* __restrict__ wo,
               const bf16_t* __restrict__ wg, const bf16_t* __restrict__ wu, const bf16_t* __restrict__ wd, bf16_t* __restrict__ out, char* __restrict__ ws) {
    extern __shared__ __attribute__((aligned(16))) char smem[];
    LAS char* lds = (LAS char*)smem;
    const unsigned lb = (unsigned)(size_t)lds;
    const int tid = threadIdx.x, lane = tid & 63;
    const int w = __builtin_amdgcn_readfirstlane(tid >> 6);
    const int b = blockIdx.x, xcd = b & 7, slot = b >> 3;
    const int p = 4 * (xcd >> 1) + (slot >> 3), j = 8 * (xcd & 1) + (slot & 7);
    const int pP = 2 * xcd + (slot >> 4), jP = slot & 15, c = slot & 7, half = (slot >> 3) & 1;
    const bool has3 = (j & 7) < (j < 8 ? 6 : 5);
    const int u3 = 32 + (j < 8 ? j : j - 2);
    const int t = lane & 31, h2 = lane >> 5, q = (lane & 15) >> 2, pp = lane & 3, blk = (lane >> 4) & 1;
    const int m = w & 3, a = w >> 2, lw = w & 3;
    const int q4 = slot >> 3;
    const int r1 = R1MODE == 0 ? 0 : R1MODE == 1 ? ((c + 2 * q4) & 7) : R1MODE == 2 ? 4 * (q4 & 1) : 2 * q4, r2 = (2 * c + 4 * q4) & 15, r3 = (4 * c + 8 * q4) & 31, r4 = (c + 3 * q4) % 11;
    STAMP(0); CSTAMP(30);
    const rsrc_t rsX = __builtin_amdgcn_make_buffer_rsrc((void*)x, 0, 2048 * 512 * 2, 0x00020000);
    const rsrc_t rsQ = __builtin_amdgcn_make_buffer_rsrc((void*)wqkv, 0, 512 * 1536 * 2, 0x00020000);
    const rsrc_t rsW = __builtin_amdgcn_make_buffer_rsrc((void*)ws, 0, WS_BYTES, 0x00020000);
    u64 tag = 0;
#define UNIFORM64(v) (((u64)(unsigned)__builtin_amdgcn_readfirstlane((unsigned)((v) >> 32)) << 32) | (unsigned)__builtin_amdgcn_readfirstlane((unsigned)(v)))
    const u32x4 ones = {0x3F803F80u, 0x3F803F80u, 0x3F803F80u, 0x3F803F80u};
    const unsigned c128 = ((lane & 7) ^ (4 * ((lane >> 4) & 1))) * 16;
    const unsigned vrow = (lane >> 3) * 1024 + c128;
    const unsigned trn = (8 * h2 + q) * 64 + pp * 8;

    const rsrc_t rsG = biased_rsrc(wg, 512 * 1376 * 2), rsU = biased_rsrc(wu, 512 * 1376 * 2);
    const unsigned vgu = (lane >> 2) * 2752 + (lane & 3) * 16, vgu8 = (lane >> 3) * 2752 + c128;
#define ISSUE_STAGE(st, halfbase, g0, g1) do { _Pragma("unroll") for (int g_ = (g0); g_ < (g1); ++g_) { const int i_ = 4 * g_ + lw; const unsigned so_ = 16 * ((16 * (st) + i_ + r3) & 31) * 2752 + 128 * j; \
            dma4(lb + (halfbase) + i_ * 4096, rsG, rsG, rsU, rsU, vgu8, vgu8, so_, so_ + 8 * 2752, so_, so_ + 8 * 2752); } } while (0)
#define ISSUE_UNIT(u, halfbase) do { _Pragma("unroll") for (int g_ = 0; g_ < 4; ++g_) { const int s_ = 2 * (4 * g_ + lw); \
            const unsigned sa_ = 16 * ((s_ + r3) & 31) * 2752 + 64 * (u), sb_ = 16 * ((s_ + 1 + r3) & 31) * 2752 + 64 * (u); \
            dma4(lb + (halfbase) + s_ * 2048, rsG, rsU, rsG, rsU, vgu, vgu, sa_, sa_, sb_, sb_); } } while (0)
    if (w == 3) {
        u64 pc; asm volatile("s_getpc_b64 %0" : "=s"(pc));
        const rsrc_t rsC = __builtin_amdgcn_make_buffer_rsrc((void*)(pc & ~127ull), 0, 36864, 0x00020000);
        touch_lines(rsC, lb + L_DUMMY, (slot * 9 + (lane < 9 ? lane : 0)) * 128, 0);
    }
    if (w < 3) {
        const rsrc_t rsA = __builtin_amdgcn_make_buffer_rsrc((void*)wg, 0, 512 * 1376 * 2, 0x00020000), rsB = __builtin_amdgcn_make_buffer_rsrc((void*)wu, 0, 512 * 1376 * 2, 0x00020000);
        const rsrc_t rsCc = __builtin_amdgcn_make_buffer_rsrc((void*)wd, 0, 512 * 1376 * 2, 0x00020000), rsE = __builtin_amdgcn_make_buffer_rsrc((void*)wo, 0, 512 * 512 * 2, 0x00020000);
        const unsigned blk64 = b * 3 + w;
        if (blk64 < 172) touch_lines(rsA, lb + L_DUMMY, (blk64 * 64 + lane) * 128, 0);
        else if (blk64 < 344) touch_lines(rsB, lb + L_DUMMY, ((blk64 - 172) * 64 + lane) * 128, 0);
        else if (blk64 < 516) touch_lines(rsCc, lb + L_DUMMY, ((blk64 - 344) * 64 + lane) * 128, 0);
        else if (blk64 < 580) touch_lines(rsE, lb + L_DUMMY, ((blk64 - 516) * 64 + lane) * 128, 0);
    }
    if (w == 3) {
        unsigned o = WS_KVP + (j * 16 + p) * KVP_BYTES + (lane < 17 ? lane : 0) * 128;
        o = lane == 17 ? WS_F1 + (j * 16 + p) * 256 : o;
        o = lane == 18 ? WS_F2 + (p * 16 + j) * 256 : o;
        o = lane == 19 ? WS_F3 + (pP * 16 + jP) * 256 : o;
        o = lane == 20 ? WS_F4 + (p * 16 + j) * 256 : o;
        o = lane == 21 ? WS_F4 + 0x10000 + (p * 16 + j) * 256 : o;
        o = lane == 22 ? WS_XST + (128 * p + 8 * j) * 16 : o;
        touch_lines(rsW, lb + L_DUMMY, o, 0);
    }
    u32x4 xf[32];
    float yk[16];
    float mean1, rstd1;
    if (w < 4) {
        {
            u64 epoch = __hip_atomic_load((const GAS u64*)ws, __ATOMIC_RELAXED, __HIP_MEMORY_SCOPE_AGENT);
            const unsigned R = 128 * p + 8 * j + 2 * w;
            const u32x4 v0 = __builtin_amdgcn_raw_buffer_load_b128(rsX, lane * 16, R * 1024, 0), v1 = __builtin_amdgcn_raw_buffer_load_b128(rsX, lane * 16, R * 1024 + 1024, 0);
            if (tid < 16) *(LAS unsigned*)(lds + L_CNT + tid * 4) = 0u;
            f32x2 a1 = {0.f, 0.f}, a2 = {0.f, 0.f}, b1 = {0.f, 0.f}, b2 = {0.f, 0.f};
#pragma unroll
            for (int e = 0; e < 4; ++e) {
                const f32x2 z0 = {blo(v0[e]), bhi(v0[e])}, z1 = {blo(v1[e]), bhi(v1[e])};
                a1 += z0; a2 += z0 * z0; b1 += z1; b2 += z1 * z1;
            }
            const float su0 = stride4_add(quad_add(a1[0] + a1[1])), sq0 = stride4_add(quad_add(a2[0] + a2[1]));
            const float su1 = stride4_add(quad_add(b1[0] + b1[1])), sq1 = stride4_add(quad_add(b2[0] + b2[1]));
            const float su = (lane & 1) ? su1 : su0, sq = (lane & 1) ? sq1 : sq0;
            const float mean = su * (1.0f / 512.0f), var = fmaxf(sq * (1.0f / 512.0f) - mean * mean, 0.f);
            const float rstd = __builtin_amdgcn_rsqf(var + 1e-5f);
            asm volatile("s_waitcnt vmcnt(0)" : "+v"(epoch)::"memory");
            tag = UNIFORM64(epoch) + 0x9E3779B97F4A7C15ull;
            if (tid == 0) *(LAS u64*)(lds + L_TAG) = tag;
            if (lane < 2) st16(rsW, (u32x4){asu(mean), asu(rstd), (unsigned)tag, (unsigned)(tag >> 32)}, (R + lane) * 16, WS_XST);
        }
        STAMP(1);
        const unsigned xa0 = (32 * m + t) * 128 + ((h2 ^ ((t >> 1) & 7)) * 16);
        const unsigned twA = 16384 + (blk ? 1024 + 32 : 0) + trn, twB = 16384 + (blk ? 1024 : 32) + trn;
        f32x16 accA = {}, accB = {};
        u32x4 fa[2][2], fb[2][2], xs;
        unsigned bA = twA, bB = twB, bx0 = xa0, bx1 = xa0 ^ 32, bx2 = xa0 ^ 64, bx3 = xa0 ^ 96;
        asm volatile("" : "+v"(bA), "+v"(bB), "+v"(bx0), "+v"(bx1), "+v"(bx2), "+v"(bx3));
#pragma unroll
        for (int ss = 0; ss <= 8; ++ss) {
            if (ss < 8) {
                BAR();
                STAMP2_SS(ss);
                if (ss == 0) STAMP(2);
                if (ss == 6) { xs = ld16(rsW, (128 * p + 32 * m + t) * 16, WS_XST); SB(); }
            }
#pragma unroll
            for (int e = 0; e < 2; ++e) {
                if (ss > 0) accA = mfma(fa[1][e], xf[4 * ss - 2 + e], accA);
                if (ss < 8) { xf[4 * ss + e] = *(LAS u32x4*)(lds + (e ? bx1 : bx0)); fa[0][e] = tr2(lds, bA + e * 2048, 256); }
                SB();
                if (ss > 0) accB = mfma(fb[1][e], xf[4 * ss - 2 + e], accB);
                if (ss < 8) fb[0][e] = tr2(lds, bB + e * 2048, 256);
                SB();
            }
            if (ss < 8) {
#pragma unroll
                for (int e = 0; e < 2; ++e) {
                    accA = mfma(fa[0][e], xf[4 * ss + e], accA);
                    xf[4 * ss + 2 + e] = *(LAS u32x4*)(lds + (e ? bx3 : bx2)); fa[1][e] = tr2(lds, bA + (2 + e) * 2048, 256);
                    SB();
                    accB = mfma(fb[0][e], xf[4 * ss + e], accB);
                    fb[1][e] = tr2(lds, bB + (2 + e) * 2048, 256);
                    SB();
                }
                if (ss < 7) {
                    const unsigned d = ss == 5 ? (unsigned)(-5 * (int)SS_BYTES) : SS_BYTES;
                    bA += d; bB += d; bx0 += d; bx1 += d; bx2 += d; bx3 += d;
                    asm volatile("" : "+v"(bA), "+v"(bB), "+v"(bx0), "+v"(bx1), "+v"(bx2), "+v"(bx3));
                }
            }
        }
        STAMP(3);
#pragma unroll
        for (int g = 0; g < 4; ++g) *(LAS f32x4*)(lds + L_XCH + m * 4096 + g * 1024 + lane * 16) = (f32x4){accB[4 * g], accB[4 * g + 1], accB[4 * g + 2], accB[4 * g + 3]};
#pragma unroll 1
        for (unsigned spin = 0; spin < SPIN_CAP; ++spin) {
            if (__all(xs[2] == (unsigned)tag && xs[3] == (unsigned)(tag >> 32))) break;
            __builtin_amdgcn_s_sleep(1);
            xs = ld16(rsW, (128 * p + 32 * m + t) * 16, WS_XST);
        }
        mean1 = asf(xs[0]); rstd1 = asf(xs[1]);
        if (h2 == 0) *(LAS f32x2*)(lds + L_STAT + (32 * m + t) * 8) = (f32x2){mean1, rstd1};
        BAR();
        STAMP(4);
#pragma unroll
        for (int g = 0; g < 4; ++g) {
            const f32x4 cs = *(LAS f32x4*)(lds + L_CS1 + (8 * g + 4 * h2) * 4);
#pragma unroll
            for (int e = 0; e < 4; ++e) { const float y = __builtin_fmaf(-mean1, cs[e], accA[4 * g + e]); yk[4 * g + e] = g < 2 ? y : y * rstd1; }
        }
    } else {
        const unsigned vx0 = (lane >> 3) * 1024 + (((lane & 7) ^ (lane >> 4)) * 16), vx1 = (lane >> 3) * 1024 + (((lane & 7) ^ (4 + (lane >> 4))) * 16);
        const unsigned vwk = (lane >> 2) * 3072 + (lane & 3) * 16, vwv = (lane >> 2) * 3072 + ((lane & 3) ^ 2) * 16;
        const rsrc_t rbX = biased_rsrc(x, 2048 * 512 * 2), rbQ = biased_rsrc(wqkv, 512 * 1536 * 2);
#define ISSUE_SS(ss, v) do { const unsigned sb_ = lb + ((ss) % 6) * SS_BYTES, ks_ = ((ss) + r1) & 7; \
            const unsigned sx_ = (128 * p + 32 * (v)) * 1024 + ks_ * 128, sw_ = (16 * (v) + 64 * ks_) * 3072 + (512 + 32 * j) * 2; \
            dma2(sb_ + 16384 + (v) * 2048, rbQ, vwk, vwv, sw_, sw_ + 1024); \
            dma4(sb_ + (v) * 4096, rbX, rbX, rbX, rbX, vx0, vx1, sx_, sx_ + 8 * 1024, sx_ + 16 * 1024, sx_ + 24 * 1024); } while (0)
        if (lw == 0) {
            const rsrc_t rsP = __builtin_amdgcn_make_buffer_rsrc((void*)wphi, 0, 32 * 256 * 2, 0x00020000);
            dma16(rsP, lb + L_WPHI, lane * 16, j * 1024);
        }
#pragma unroll
        for (int ss = 0; ss < K0; ++ss) ISSUE_SS(ss, lw);
        STAMP(1);
        f32x4 csacc = {};
        const unsigned tw = 16384 + ((lw & 1) ? 1024 + ((lw >> 1) ^ 1) * 32 : (lw >> 1) * 32) + (lane >> 5) * 2048 + (8 * ((lane >> 4) & 1) + q) * 64 + pp * 8;
        constexpr int NL = 8 - K0;
#define P1WAIT(s) (6 * ((((s) + K0 - 1 < 7 ? (s) + K0 - 1 : 7) - (s))) + ((s) > 4 ? 2 * ((s) - 4) : 0))
#pragma unroll
        for (int ss = 0; ss < 8; ++ss) {
            if (ss == 0) waitv<P1WAIT(0)>();
            if (ss == 1) waitv<P1WAIT(1)>();
            if (ss == 2) waitv<P1WAIT(2)>();
            if (ss == 3) waitv<P1WAIT(3)>();
            if (ss == 4) waitv<P1WAIT(4)>();
            if (ss == 5) waitv<P1WAIT(5)>();
            if (ss == 6) waitv<P1WAIT(6)>();
            if (ss == 7) waitv<P1WAIT(7)>();
            STAMP2_SS(ss);
            BAR();
            if (ss == 0) { const u64 tv = *(LAS u64*)(lds + L_TAG); tag = UNIFORM64(tv); STAMP(2); }
            unsigned bw = (ss % 6) * SS_BYTES + tw;
            asm volatile("" : "+v"(bw));
            const u32x4 f0 = tr2(lds, bw, 256), f1 = tr2(lds, bw + 4096, 256);
            SB();
            if (ss < NL) ISSUE_SS(K0 + ss, lw);
            if (ss == 6) {
#pragma unroll
                for (int i = 0; i < 4; ++i) *(LAS u32x4*)(lds + L_VIMG + (4 * lw + i) * 1024 + lane * 16) = ones;
            }
            if (ss >= 4) {
                const int pc = 8 * lw + 2 * (ss - 4);
                const unsigned so = 16 * ((pc + 4 * r1) & 31) * 3072 + 64 * j;
                dma2(lb + L_WQ + pc * 1024, rbQ, vwk, vwk, so, so + 16 * 3072);
            }
            csacc = mfma16(f0, ones, csacc);
            csacc = mfma16(f1, ones, csacc);
            SB();
        }
        if ((lane & 15) == 0) *(LAS f32x4*)(lds + L_CS1 + (lw >> 1) * 128 + (lw & 1) * 64 + (lane >> 4) * 16) = csacc;
        STAMP(3);
        BAR();
        STAMP(4);
        const f32x2 st = *(LAS f32x2*)(lds + L_STAT + (32 * m + t) * 8);
        mean1 = st[0]; rstd1 = st[1];
#pragma unroll
        for (int g = 0; g < 4; ++g) {
            const f32x4 cs = *(LAS f32x4*)(lds + L_CS1 + 128 + (8 * g + 4 * h2) * 4);
            const f32x4 v = *(LAS f32x4*)(lds + L_XCH + m * 4096 + g * 1024 + lane * 16);
#pragma unroll
            for (int e = 0; e < 4; ++e) { const float y = __builtin_fmaf(-mean1, cs[e], v[e]); yk[4 * g + e] = g < 2 ? y : y * rstd1; }
        }
    }
    const unsigned imgA = L_PHI + a * 8192 + (32 * m + t) * 64 + ((h2 ^ ((t >> 1) & 7)) * 8);
    const u32x4 aphi = tr2(lds, L_WPHI + a * 512 + (4 * h2 + q) * 32 + pp * 8, 256);
    {
        const u32x4 kf = {pack2(yk[0], yk[1]), pack2(yk[2], yk[3]), pack2(yk[4], yk[5]), pack2(yk[6], yk[7])};
        const f32x16 zero = {};
        const f32x16 pk = mfma(aphi, kf, zero);
        *(LAS u32x2*)(lds + imgA + (L_VIMG - L_PHI)) = (u32x2){pack2(yk[8], yk[9]), pack2(yk[10], yk[11])};
        *(LAS u32x2*)(lds + (imgA ^ 16) + (L_VIMG - L_PHI)) = (u32x2){pack2(yk[12], yk[13]), pack2(yk[14], yk[15])};
        u32x4 pos, neg;
        dual_softmax(pk, rstd1 * LOG2E, pos, neg);
        *(LAS u32x2*)(lds + imgA) = (u32x2){pos[0], pos[1]};
        *(LAS u32x2*)(lds + (imgA ^ 16)) = (u32x2){pos[2], pos[3]};
        *(LAS u32x2*)(lds + (imgA ^ 32)) = (u32x2){neg[0], neg[1]};
        *(LAS u32x2*)(lds + (imgA ^ 48)) = (u32x2){neg[2], neg[3]};
    }
    if (w >= 4) waitv<0>();
    BAR();
    STAMP(5);
    float yq[8];
    if (w < 4) {
        unsigned tq = L_WQ + trn + blk * 32;
        asm volatile("" : "+v"(tq));
        f32x16 qa = {};
        u32x4 qb[2][4];
#pragma unroll
        for (int s = 0; s < 4; ++s) qb[0][s] = tr2(lds, tq + s * 1024, 256);
        SB();
#pragma unroll
        for (int s = 0; s < 32; ++s) {
            qa = mfma(qb[(s >> 2) & 1][s & 3], xf[s], qa);
            if (s < 28) qb[((s >> 2) + 1) & 1][s & 3] = tr2(lds, tq + (s + 4) * 1024, 256);
            SB();
        }
#pragma unroll
        for (int g = 0; g < 2; ++g) *(LAS f32x4*)(lds + L_XCH + m * 4096 + g * 1024 + lane * 16) = (f32x4){qa[8 + 4 * g], qa[9 + 4 * g], qa[10 + 4 * g], qa[11 + 4 * g]};
        STAMP(6);
        BAR();
#pragma unroll
        for (int g = 0; g < 2; ++g) {
            const f32x4 c0 = *(LAS f32x4*)(lds + L_CSQ + (8 * g + 4 * h2) * 4);
#pragma unroll
            for (int e = 0; e < 4; ++e) yq[4 * g + e] = __builtin_fmaf(-mean1, c0[e], qa[4 * g + e]);
        }
    } else {
        if (lw >= 2) {
            __builtin_amdgcn_s_setprio(3);
            const int ah = lw - 2;
            f32x16 kv = {};
            unsigned aa[2], ab[2];
#pragma unroll
            for (int u = 0; u < 2; ++u) {
                const unsigned R = 8 * h2 + 4 * u + q, sw = (R >> 1) & 7;
                aa[u] = L_PHI + ah * 8192 + R * 64 + (((4 * blk + pp) ^ sw) * 8);
                ab[u] = aa[u] + (L_VIMG - L_PHI);
                asm volatile("" : "+v"(aa[u]), "+v"(ab[u]));
            }
            u32x4 A[8], B[8];
#pragma unroll
            for (int s = 0; s < 8; ++s) {
                const s16x4 a0 = __builtin_amdgcn_ds_read_tr16_b64_v4i16((LAS s16x4*)(lds + aa[0] + s * 1024));
                const s16x4 a1 = __builtin_amdgcn_ds_read_tr16_b64_v4i16((LAS s16x4*)(lds + aa[1] + s * 1024));
                const s16x4 b0 = __builtin_amdgcn_ds_read_tr16_b64_v4i16((LAS s16x4*)(lds + ab[0] + s * 1024));
                const s16x4 b1 = __builtin_amdgcn_ds_read_tr16_b64_v4i16((LAS s16x4*)(lds + ab[1] + s * 1024));
                A[s] = __builtin_bit_cast(u32x4, __builtin_shufflevector(a0, a1, 0, 1, 2, 3, 4, 5, 6, 7));
                B[s] = __builtin_bit_cast(u32x4, __builtin_shufflevector(b0, b1, 0, 1, 2, 3, 4, 5, 6, 7));
                if (s == 3) PIN();
            }
            PIN();
#pragma unroll
            for (int s = 0; s < 8; ++s) kv = mfma(A[s], B[s], kv);
            if (t <= 16) {
                const unsigned base = WS_KVP + (j * 16 + p) * KVP_BYTES + ((ah * 4 + h2) * 17 + t) * 16;
                st16(rsW, (u32x4){pack2(kv[0], kv[1]), pack2(kv[2], kv[3]), pack2(kv[4], kv[5]), pack2(kv[6], kv[7])}, base, 0);
                st16(rsW, (u32x4){pack2(kv[8], kv[9]), pack2(kv[10], kv[11]), pack2(kv[12], kv[13]), pack2(kv[14], kv[15])}, base + 34 * 16, 0);
            }
            WAITV(0);
            arrive_publish(lds, 0, 2, ws, WS_F1 + (j * 16 + p) * 256, tag, lane);
            __builtin_amdgcn_s_setprio(0);
            STAMP(6);
        } else {
            unsigned tq = L_WQ + lw * 32 + (8 * (lane >> 4) + q) * 64 + pp * 8;
            asm volatile("" : "+v"(tq));
            f32x4 qcs = {};
#pragma unroll
            for (int g = 0; g < 4; ++g) {
                u32x4 f[4];
#pragma unroll
                for (int s = 0; s < 4; ++s) f[s] = tr2(lds, tq + (4 * g + s) * 2048, 256);
                PIN();
#pragma unroll
                for (int s = 0; s < 4; ++s) qcs = mfma16(f[s], ones, qcs);
            }
            if ((lane & 15) == 0) *(LAS f32x4*)(lds + L_CSQ + lw * 64 + (lane >> 4) * 16) = qcs;
            STAMP(6);
        }
        BAR();
        {
            const rsrc_t rsO = biased_rsrc(wo, 512 * 512 * 2);
#pragma unroll
            for (int g = 0; g < 4; ++g) {
                const int i = 4 * g + lw;
                const unsigned so = 16 * ((i + r2) & 15) * 1024 + 128 * c;
                dma4(lb + L_X + i * 4096, rsO, rsO, rsO, rsO, vrow, vrow, so, so + 8 * 1024, so + 256 * 1024, so + 264 * 1024);
            }
        }
#pragma unroll
        for (int g = 0; g < 2; ++g) {
            const f32x4 c0 = *(LAS f32x4*)(lds + L_CSQ + (16 + 8 * g + 4 * h2) * 4);
            const f32x4 v = *(LAS f32x4*)(lds + L_XCH + m * 4096 + g * 1024 + lane * 16);
#pragma unroll
            for (int e = 0; e < 4; ++e) yq[4 * g + e] = __builtin_fmaf(-mean1, c0[e], v[e]);
        }
    }
    STAMP(7);
    u32x4 qpos, qneg;
    {
        const u32x4 qf = {pack2(yq[0], yq[1]), pack2(yq[2], yq[3]), pack2(yq[4], yq[5]), pack2(yq[6], yq[7])};
        const f32x16 zero = {};
        const f32x16 pq = mfma(aphi, qf, zero);
        dual_softmax(pq, rstd1 * LOG2E, qpos, qneg);
    }
    STAMP(8);
    unsigned kso[16];
#pragma unroll
    for (int i = 0; i < 16; ++i) { kso[i] = WS_KVP + (j * 16 + ((i + 4 * q4) & 15)) * KVP_BYTES; asm volatile("" : "+s"(kso[i])); }
    if (w == 0) poll_flags<PD1>(ws, WS_F1 + j * 16 * 256, 16, tag, lane);
    BAR();
    STAMP(9);
    {
        const unsigned kb = WS_KVP + j * 16 * KVP_BYTES;
        float lo = 0.f, hi = 0.f;
        unsigned v[16], z[8];
#pragma unroll
        for (int i = 0; i < 16; ++i) v[i] = ld4(rsW, tid * 4, kso[i]);
        if (w == 0) {
#pragma unroll
            for (int i = 0; i < 8; ++i) z[i] = ld4(rsW, (512 + t) * 4 + h2 * 8 * KVP_BYTES, kb + i * KVP_BYTES);
        }
        PIN();
#pragma unroll
        for (int i = 0; i < 16; ++i) { lo += blo(v[i]); hi += bhi(v[i]); }
        if (w == 0) {
            float zl = 0.f, zh = 0.f;
#pragma unroll
            for (int i = 0; i < 8; ++i) { zl += blo(z[i]); zh += bhi(z[i]); }
            zl = halves_add(zl); zh = halves_add(zh);
            if (h2 == 0) *(LAS unsigned*)(lds + L_KVA + (512 + t) * 4) = pack2(zl, zh);
        }
        *(LAS unsigned*)(lds + L_KVA + tid * 4) = pack2(lo, hi);
    }
    BAR();
    STAMP(10);
    const int npass = has3 ? 3 : 2;
    const int m2 = w & 1, kh = (w >> 1) & 1;
    const int T2 = 4 * pP + 2 * half + m2;
    const unsigned C0 = 64 * c + 32 * kh;
    {
        const unsigned col = t < 16 ? t : 16;
        const u32x4 kp = *(LAS u32x4*)(lds + L_KVA + ((a * 4 + h2) * 17 + col) * 16);
        const u32x4 kn = *(LAS u32x4*)(lds + L_KVA + ((a * 4 + 2 + h2) * 17 + col) * 16);
        f32x16 o = {};
        o = mfma(kp, qpos, o);
        o = mfma(kn, qneg, o);
        const float r = __builtin_amdgcn_rcpf(o[8] + 1e-6f);
        st16(rsW, (u32x4){pack2(o[0] * r, o[1] * r), pack2(o[2] * r, o[3] * r), pack2(o[4] * r, o[5] * r), pack2(o[6] * r, o[7] * r)},
             WS_O + ((4 * p + m) * 32 + 2 * j + a) * 1024 + lane * 16, 0);
        WAITV(0);
        BAR();
        if (tid == 0) __hip_atomic_store((GAS u64*)(ws + WS_F2 + (p * 16 + j) * 256), tag, __ATOMIC_RELAXED, __HIP_MEMORY_SCOPE_AGENT);
        SBAR();
    }
    const unsigned trp = (4 * h2 + q) * 64 + blk * 32 + pp * 8;
    const unsigned t128 = (4 * h2 + q) * 128 + (q >> 1) * 64 + blk * 32 + pp * 8;

    if (w >= 4) {
        ISSUE_STAGE(0, L_Y, 0, 4);
        STAMP(11);
    }
    if (w >= 4) {
        const rsrc_t rsD = biased_rsrc(wd, 1376 * 512 * 2);
#define ISSUE_WD(pi, halfbase) do { _Pragma("unroll") for (int g_ = 0; g_ < ((pi) == 2 ? 3 : 4); ++g_) { const int i_ = 4 * g_ + lw; if ((pi) < 2 || i_ < 11) { \
                const int ii_ = (pi) == 2 ? (i_ + r4 >= 11 ? i_ + r4 - 11 : i_ + r4) : ((i_ + r2) & 15); \
                const unsigned sa_ = 16 * (((pi) == 2 ? 64 : 32 * (pi)) + ii_) * 1024 + 128 * c, sb_ = sa_ + 16 * ((pi) == 2 ? 11 : 16) * 1024; \
                dma4(lb + (halfbase) + i_ * 4096, rsD, rsD, rsD, rsD, vrow, vrow, sa_, sa_ + 8 * 1024, sb_, sb_ + 8 * 1024); } } } while (0)
        f32x4 cs0 = {}, cs1 = {};
        const unsigned gam = lane >> 4;
        const unsigned ca0 = (lw >> 1) * 2048 + (gam >> 1) * 4096 + (8 * (gam & 1) + q) * 128 + (((lw & 1) * 64) ^ (64 * (q >> 1))) + pp * 8;
#define COLSUM_STAGE(halfbase) do { unsigned cb_ = (halfbase) + ca0; asm volatile("" : "+v"(cb_)); \
            _Pragma("unroll") for (int g_ = 0; g_ < 4; ++g_) { const u32x4 f0_ = tr2(lds, cb_ + g_ * 16384, 512), f1_ = tr2(lds, cb_ + g_ * 16384 + 32, 512), \
                                                                         f2_ = tr2(lds, cb_ + g_ * 16384 + 8192, 512), f3_ = tr2(lds, cb_ + g_ * 16384 + 8192 + 32, 512); \
                PIN(); cs0 = mfma16(f0_, ones, cs0); cs1 = mfma16(f1_, ones, cs1); cs0 = mfma16(f2_, ones, cs0); cs1 = mfma16(f3_, ones, cs1); } } while (0)
        BAR();
        STAMP(12);
        BAR();
        SBAR();
        ISSUE_STAGE(1, L_X, 0, B_EARLY);
        BAR();
        SBAR();
        ISSUE_STAGE(1, L_X, B_EARLY, 4);
        STAMP(13);
        waitv<16>();
        BAR();
        COLSUM_STAGE(L_Y);
        WAITV(0);
        lsig(lb, 8); lwait(lds, 8, 4);
        COLSUM_STAGE(L_X);
        if ((lane & 15) == 0) {
            *(LAS f32x4*)(lds + L_CS3 + ((lw & 1) * 64 + (lw >> 1) * 32 + 4 * gam) * 4) = cs0;
            *(LAS f32x4*)(lds + L_CS3 + ((lw & 1) * 64 + (lw >> 1) * 32 + 16 + 4 * gam) * 4) = cs1;
        }
        BAR();
        if (npass == 3) {
            ISSUE_UNIT(u3, L_Y);
            WAITV(0);
            SBAR();
            ISSUE_WD(0, L_X);
            {
                unsigned cb = L_Y + (lw >> 1) * 1024 + (gam >> 1) * 2048 + (8 * (gam & 1) + q) * 64 + (lw & 1) * 32 + pp * 8;
                asm volatile("" : "+v"(cb));
                f32x4 c3 = {};
#pragma unroll
                for (int g = 0; g < 4; ++g) {
                    u32x4 f[4];
#pragma unroll
                    for (int e = 0; e < 4; ++e) f[e] = tr2(lds, cb + (4 * g + e) * 4096, 256);
                    PIN();
#pragma unroll
                    for (int e = 0; e < 4; ++e) c3 = mfma16(f[e], ones, c3);
                }
                if ((lane & 15) == 0) *(LAS f32x4*)(lds + L_CS3 + (128 + (lw >> 1) * 32 + (lw & 1) * 16 + 4 * gam) * 4) = c3;
            }
            BAR();
            WAITV(0);
            BAR();
            if (lw == 0) {
                lwait(lds, 11, 4);
                if (lane == 0) __hip_atomic_store((GAS u64*)(ws + WS_F4 + 0x10000 + (p * 16 + j) * 256), tag, __ATOMIC_RELAXED, __HIP_MEMORY_SCOPE_AGENT);
            }
            SBAR();
            ISSUE_WD(1, L_Y);
            WAITV(0);
            BAR();
            if (lw == 0) { poll_flags3(ws, WS_F4 + 0x10000 + pP * 16 * 256, tag, lane); lsig(lb, 14); }
            ISSUE_WD(2, L_X);
            WAITV(0);
            BAR();
        } else {
            ISSUE_WD(0, L_Y);
            SBAR();
            ISSUE_WD(1, L_X);
            WAITV(0);
            BAR();
            SBAR();
            BAR();
            if (lw == 0) { poll_flags3(ws, WS_F4 + 0x10000 + pP * 16 * 256, tag, lane); lsig(lb, 14); }
            ISSUE_WD(2, L_Y);
            WAITV(0);
            BAR();
        }
        BAR();
    } else {
        if (PRIO_C) __builtin_amdgcn_s_setprio(PRIO_C);
        const unsigned vo = (32 * T2 + t) * 1024 + (C0 + 8 * h2) * 2;
        u32x4 xr0 = __builtin_amdgcn_raw_buffer_load_b128(rsX, vo, 0, 0);
        u32x4 xr1 = __builtin_amdgcn_raw_buffer_load_b128(rsX, vo + 32, 0, 0);
        PIN();
        STAMP(11);
        if (w == 0) poll_flags<PD2>(ws, WS_F2 + pP * 16 * 256, 16, tag, lane);
        BAR();
        STAMP(12);
        if (b == 0 && w == 1 && lane == 0) __hip_atomic_store((GAS u64*)ws, tag, __ATOMIC_RELAXED, __HIP_MEMORY_SCOPE_AGENT);
        {
            u32x4 hres0, hres1;
            u32x4 of[16];
#pragma unroll
            for (int i = 0; i < 8; ++i) of[i] = ld16(rsW, lane * 16, WS_O + (T2 * 32 + 16 * kh + ((i + r2) & 15)) * 1024);
            PIN();
            f32x16 keep, snd = {};
            { auto r = __builtin_amdgcn_permlane32_swap(xr0[0], xr0[2], false, false); xr0[0] = r[0]; xr0[2] = r[1];
              r = __builtin_amdgcn_permlane32_swap(xr0[1], xr0[3], false, false); xr0[1] = r[0]; xr0[3] = r[1];
              r = __builtin_amdgcn_permlane32_swap(xr1[0], xr1[2], false, false); xr1[0] = r[0]; xr1[2] = r[1];
              r = __builtin_amdgcn_permlane32_swap(xr1[1], xr1[3], false, false); xr1[1] = r[0]; xr1[3] = r[1]; }
#pragma unroll
            for (int e = 0; e < 4; ++e) { keep[2 * e] = blo(xr0[e]); keep[2 * e + 1] = bhi(xr0[e]); keep[8 + 2 * e] = blo(xr1[e]); keep[8 + 2 * e + 1] = bhi(xr1[e]); }
            asm volatile("" : "+v"(keep));
            gemm2<16, 4096, 1024, 0>(lds, L_X + kh * 2048 + (t128 ^ (64 * kh)), L_X + kh * 2048 + (t128 ^ (64 * (kh ^ 1))), of, keep, snd, [&](int g) {
                if (g < 4) {
#pragma unroll
                    for (int e = 0; e < 2; ++e) of[8 + 2 * g + e] = ld16(rsW, lane * 16, WS_O + (T2 * 32 + 16 * kh + ((8 + 2 * g + e + r2) & 15)) * 1024);
                    SB();
                } });
#pragma unroll
            for (int g = 0; g < 4; ++g) *(LAS f32x4*)(lds + L_XCH + w * 4096 + g * 1024 + lane * 16) = (f32x4){snd[4 * g], snd[4 * g + 1], snd[4 * g + 2], snd[4 * g + 3]};
            STAMP(13);
            BAR();
#pragma unroll
            for (int g = 0; g < 4; ++g) {
                const f32x4 v = *(LAS f32x4*)(lds + L_XCH + (w ^ 2) * 4096 + g * 1024 + lane * 16);
#pragma unroll
                for (int e = 0; e < 4; ++e) keep[4 * g + e] += v[e];
            }
#pragma unroll
            for (int e = 0; e < 4; ++e) { hres0[e] = pack2(keep[2 * e], keep[2 * e + 1]); hres1[e] = pack2(keep[8 + 2 * e], keep[8 + 2 * e + 1]); }
            const unsigned hb = WS_H + (T2 * 32 + 4 * c + 2 * kh) * 1024;
            st16(rsW, hres0, lane * 16, hb);
            st16(rsW, hres1, lane * 16, hb + 1024);
            *(LAS u32x4*)(lds + L_HRES + w * 2048 + lane * 16) = hres0;
            *(LAS u32x4*)(lds + L_HRES + w * 2048 + 1024 + lane * 16) = hres1;
            f32x2 s1 = {0.f, 0.f}, s2 = {0.f, 0.f};
#pragma unroll
            for (int e = 0; e < 8; ++e) { const f32x2 v = {keep[2 * e], keep[2 * e + 1]}; s1 += v; s2 += v * v; }
            float su = s1[0] + s1[1], sq = s2[0] + s2[1];
            su = halves_add(su); sq = halves_add(sq);
            __builtin_amdgcn_raw_buffer_store_b32(asu(h2 ? sq : su), rsW, lane * 4 + WS_HST + (T2 * 16 + 2 * c + kh) * 256, 0, 16);
            SBAR();
            WAITV(0);
            BAR();
            if (tid == 0) __hip_atomic_store((GAS u64*)(ws + WS_F3 + (pP * 16 + jP) * 256), tag, __ATOMIC_RELAXED, __HIP_MEMORY_SCOPE_AGENT);
            SBAR();
        }
        STAMP(14);
        if (w == 0) poll_flags<PD3>(ws, WS_F3 + p * 16 * 256, 16, tag, lane);
        BAR();
        STAMP(15);
        {
            const int T = 4 * p + w;
            u32x4 hf[32];
            float sv[16];
#pragma unroll
            for (int s = 0; s < 8; ++s) hf[s] = ld16(rsW, lane * 16, WS_H + (T * 32 + ((s + r3) & 31)) * 1024);
            PIN();
            float mean, r2;
            auto act_out = [&](const f32x16& ag, const f32x16& au, int cs, unsigned ks) {
                float y[16];
#pragma unroll
                for (int g = 0; g < 4; ++g) {
                    const f32x4 cg = *(LAS f32x4*)(lds + L_CS3 + (cs * 64 + 8 * g + 4 * h2) * 4), cu = *(LAS f32x4*)(lds + L_CS3 + (cs * 64 + 32 + 8 * g + 4 * h2) * 4);
#pragma unroll
                    for (int e = 0; e < 4; ++e) y[4 * g + e] = (__builtin_fmaf(-mean, cg[e], ag[4 * g + e]) * r2) * __builtin_fmaf(-mean, cu[e], au[4 * g + e]);
                }
                const unsigned ab = WS_ACT + (T * 86 + ks) * 1024;
                st16(rsW, (u32x4){pack2(y[0], y[1]), pack2(y[2], y[3]), pack2(y[4], y[5]), pack2(y[6], y[7])}, lane * 16, ab);
                st16(rsW, (u32x4){pack2(y[8], y[9]), pack2(y[10], y[11]), pack2(y[12], y[13]), pack2(y[14], y[15])}, lane * 16, ab + 1024);
            };
            {
                f32x16 g0 = {}, g1 = {}, u0 = {}, u1 = {};
                gemm4<0>(lds, L_Y + t128, L_Y + (t128 ^ 64), hf, g0, g1, u0, u1, [&](int i) {
                    if (i < 8) hf[8 + i] = ld16(rsW, lane * 16, WS_H + (T * 32 + ((8 + i + r3) & 31)) * 1024);
                    else { hf[2 * i] = ld16(rsW, lane * 16, WS_H + (T * 32 + ((2 * i + r3) & 31)) * 1024); hf[2 * i + 1] = ld16(rsW, lane * 16, WS_H + (T * 32 + ((2 * i + 1 + r3) & 31)) * 1024); }
                    if (i < 4) {
#pragma unroll
                        for (int k = 0; k < 4; ++k) sv[4 * i + k] = asf(ld4(rsW, lane * 4, WS_HST + (T * 16 + 4 * i + k) * 256));
                    }
                    if (i == 9) {
                        float tot = 0.f;
#pragma unroll
                        for (int k = 0; k < 16; ++k) tot += sv[k];
                        const u32x2 r = both_halves(tot);
                        mean = asf(r[0]) * (1.0f / 512.0f);
                        const float var = fmaxf(asf(r[1]) * (1.0f / 512.0f) - mean * mean, 0.f);
                        r2 = __builtin_amdgcn_rcpf(var + 1e-5f);
                        asm volatile("" : "+v"(mean), "+v"(r2));
                    }
                    SB(); });
                BAR();
                STAMP(16);
                gemm4<16>(lds, L_X + t128, L_X + (t128 ^ 64), hf, g0, g1, u0, u1, nohook);
                STAMP(17);
                act_out(g0, u0, 0, 4 * j);
                act_out(g1, u1, 1, 4 * j + 2);
                SBAR();
            }
            if (npass == 3) {
                f32x16 ag = {}, au = {};
                gemm2<32, 2048, 512, 0>(lds, L_Y + trp, L_Y + 1024 + trp, hf, ag, au, [&](int g) {
                    if (g == 3) { WAITV(0); arrive_publish(lds, 3, 4, ws, WS_F4 + (p * 16 + j) * 256, tag, lane); PIN(); } });
                BAR();
                STAMP(18);
                act_out(ag, au, 2, 2 * u3);
            } else {
                WAITV(0); arrive_publish(lds, 3, 4, ws, WS_F4 + (p * 16 + j) * 256, tag, lane);
            }
        }
        STAMP(19);
        {
            f32x16 a0, a1 = {};
            {
                const u32x4 hres0 = *(LAS u32x4*)(lds + L_HRES + w * 2048 + lane * 16), hres1 = *(LAS u32x4*)(lds + L_HRES + w * 2048 + 1024 + lane * 16);
#pragma unroll
                for (int e = 0; e < 4; ++e) { a0[2 * e] = blo(hres0[e]); a0[2 * e + 1] = bhi(hres0[e]); a0[8 + 2 * e] = blo(hres1[e]); a0[8 + 2 * e + 1] = bhi(hres1[e]); }
                asm volatile("" : "+v"(a0));
            }
            const unsigned tk = kh * 2048 + (t128 ^ (64 * kh)), ts = kh * 2048 + (t128 ^ (64 * (kh ^ 1)));
            const unsigned h0 = npass == 3 ? L_X : L_Y, h1 = npass == 3 ? L_Y : L_X;
            if (w == 0) poll_flags(ws, WS_F4 + pP * 16 * 256, 16, tag, lane);
            BAR();
            STAMP(20);
            u32x4 af[43];
            auto ld_act = [&](int i) {
                const int ks = i < 32 ? 32 * (i >> 4) + kh * 16 + ((i + r2) & 15) : 64 + kh * 11 + (i - 32 + r4 >= 11 ? i - 32 + r4 - 11 : i - 32 + r4);
                af[i] = ld16(rsW, lane * 16, WS_ACT + (T2 * 86 + ks) * 1024); };
            if (npass == 3) { WAITV(0); lsig(lb, 11); }
#pragma unroll
            for (int i = 0; i < 8; ++i) ld_act(i);
            SBAR();
            gemm2<16, 4096, 1024, 0>(lds, h0 + tk, h0 + ts, af, a0, a1, [&](int g) { ld_act(8 + 2 * g); ld_act(9 + 2 * g); SB(); });
            STAMP(21);
            BAR();
            STAMP(22);
            gemm2<16, 4096, 1024, 16>(lds, h1 + tk, h1 + ts, af, a0, a1, [&](int g) {
                if (g == 4) lwait(lds, 14, 1);
                ld_act(24 + 2 * g); ld_act(25 + 2 * g); SB(); });
            BAR();
            STAMP(23);
            gemm2<11, 4096, 1024, 32>(lds, h0 + tk, h0 + ts, af, a0, a1, [&](int g) { if (g < 2) { ld_act(40 + 2 * g < 43 ? 40 + 2 * g : 42); if (g == 0) ld_act(41); SB(); } });
#pragma unroll
            for (int g = 0; g < 4; ++g) *(LAS f32x4*)(lds + L_XCH + w * 4096 + g * 1024 + lane * 16) = (f32x4){a1[4 * g], a1[4 * g + 1], a1[4 * g + 2], a1[4 * g + 3]};
            BAR();
            STAMP(24);
#pragma unroll
            for (int g = 0; g < 4; ++g) {
                const f32x4 v = *(LAS f32x4*)(lds + L_XCH + (w ^ 2) * 4096 + g * 1024 + lane * 16);
#pragma unroll
                for (int e = 0; e < 4; ++e) a0[4 * g + e] += v[e];
            }
            u32x4 o0, o1;
#pragma unroll
            for (int e = 0; e < 4; ++e) { o0[e] = pack2(a0[2 * e], a0[2 * e + 1]); o1[e] = pack2(a0[8 + 2 * e], a0[8 + 2 * e + 1]); }
            { auto r = __builtin_amdgcn_permlane32_swap(o0[0], o0[2], false, false); o0[0] = r[0]; o0[2] = r[1];
              r = __builtin_amdgcn_permlane32_swap(o0[1], o0[3], false, false); o0[1] = r[0]; o0[3] = r[1];
              r = __builtin_amdgcn_permlane32_swap(o1[0], o1[2], false, false); o1[0] = r[0]; o1[2] = r[1];
              r = __builtin_amdgcn_permlane32_swap(o1[1], o1[3], false, false); o1[1] = r[0]; o1[3] = r[1]; }
            const rsrc_t rsOut = __builtin_amdgcn_make_buffer_rsrc((void*)out, 0, 2048 * 512 * 2, 0x00020000);
            int tz = threadIdx.x; asm volatile("" : "+v"(tz));
            const unsigned vz = (32 * T2 + (tz & 31)) * 1024 + (C0 + 8 * ((tz >> 5) & 1)) * 2;
            __builtin_amdgcn_raw_buffer_store_b128(o0, rsOut, vz, 0, 0);
            __builtin_amdgcn_raw_buffer_store_b128(o1, rsOut, vz + 32, 0, 0);
        }
        STAMP(25); CSTAMP(31);
    }
}

extern "C" void kernel_launch(void* const* d_in, const int* in_sizes, int n_in, void* d_out, int out_size, void* d_ws, size_t ws_size, hipStream_t stream) {
    (void)in_sizes; (void)n_in; (void)out_size; (void)ws_size;
    (void)hipFuncSetAttribute((const void*)hedgehog_block, hipFuncAttributeMaxDynamicSharedMemorySize, LDS_BYTES);
    hedgehog_block<<<256, 512, LDS_BYTES, stream>>>((const bf16_t*)d_in[0], (const bf16_t*)d_in[1], (const bf16_t*)d_in[2], (const bf16_t*)d_in[3],
                                                    (const bf16_t*)d_in[5], (const bf16_t*)d_in[6], (const bf16_t*)d_in[7], (bf16_t*)d_out, (char*)d_ws);
}
```

```cpp
#include <hip/hip_runtime.h>
#include <hip/hip_bf16.h>

#define LAS __attribute__((address_space(3)))
#define GAS __attribute__((address_space(1)))
#define DI __device__ __forceinline__
typedef unsigned short bf16_t;
typedef unsigned u32x4 __attribute__((ext_vector_type(4)));
typedef unsigned u32x2 __attribute__((ext_vector_type(2)));
typedef short s16x4 __attribute__((ext_vector_type(4)));
typedef __bf16 bf16x8 __attribute__((ext_vector_type(8)));
typedef __bf16 bf16x2 __attribute__((ext_vector_type(2)));
typedef float f32x16 __attribute__((ext_vector_type(16)));
typedef float f32x4 __attribute__((ext_vector_type(4)));
typedef float f32x2 __attribute__((ext_vector_type(2)));
typedef unsigned long long u64;
typedef __amdgpu_buffer_rsrc_t rsrc_t;

constexpr unsigned WS_F1 = 0x10000, WS_F2 = 0x20000, WS_F3 = 0x30000, WS_F4 = 0x40000;
constexpr unsigned WS_KVP = 0x80000, KVP_BYTES = 2176;
constexpr unsigned WS_HST = 0x110000;
constexpr unsigned WS_XST = 0x160000;
constexpr unsigned WS_O = 0x200000, WS_H = 0x400000, WS_ACT = 0x600000, WS_PROF = 0xC00000, WS_BYTES = 0x1000000;
constexpr unsigned L_X = 0, L_Y = 65536, L_XCH = 131072, L_MISC = 147456;
constexpr unsigned L_STAT = L_MISC, L_CS1 = L_MISC + 1024, L_CSQ = L_MISC + 1280, L_WPHI = L_MISC + 1536, L_KVA = L_MISC + 2560, L_CS3 = L_MISC + 4736, L_CNT = L_MISC + 5504, L_TAG = L_MISC + 6016, L_DUMMY = L_MISC + 6144, L_HRES = L_MISC + 8192;
constexpr unsigned L_WQ = 65536, L_PHI = 98304, L_VIMG = 114688  , SS_BYTES = 24576;
constexpr unsigned LDS_BYTES = 163840;
constexpr unsigned SPIN_CAP = 40000;
constexpr float LOG2E = 1.4426950408889634f;

DI float asf(unsigned v) { return __builtin_bit_cast(float, v); }
DI unsigned asu(float v) { return __builtin_bit_cast(unsigned, v); }
DI unsigned pack2(float lo, float hi) { bf16x2 v = {(__bf16)lo, (__bf16)hi}; return __builtin_bit_cast(unsigned, v); }
DI float blo(unsigned v) { return __builtin_bit_cast(float, v << 16); }
DI float bhi(unsigned v) { return __builtin_bit_cast(float, v & 0xffff0000u); }
DI f32x16 mfma(u32x4 a, u32x4 b, f32x16 c) { return __builtin_amdgcn_mfma_f32_32x32x16_bf16(__builtin_bit_cast(bf16x8, a), __builtin_bit_cast(bf16x8, b), c, 0, 0, 0); }
DI f32x4 mfma16(u32x4 a, u32x4 b, f32x4 c) { return __builtin_amdgcn_mfma_f32_16x16x32_bf16(__builtin_bit_cast(bf16x8, a), __builtin_bit_cast(bf16x8, b), c, 0, 0, 0); }
DI u32x4 tr2(LAS char* lds, unsigned off, unsigned second) {
    const s16x4 lo = __builtin_amdgcn_ds_read_tr16_b64_v4i16((LAS s16x4*)(lds + off));
    const s16x4 hi = __builtin_amdgcn_ds_read_tr16_b64_v4i16((LAS s16x4*)(lds + off + second));
    return __builtin_bit_cast(u32x4, __builtin_shufflevector(lo, hi, 0, 1, 2, 3, 4, 5, 6, 7));
}
DI void dma16(const rsrc_t rs, unsigned ldsaddr, unsigned voff, unsigned soff) {
    asm volatile("s_mov_b32 m0, %0\n\ts_nop 0\n\tbuffer_load_dwordx4 %1, %2, %3 offen lds" ::"s"(ldsaddr), "v"(voff), "s"(rs), "s"(soff) : "memory");
}
constexpr unsigned BIAS = 4096;
DI rsrc_t biased_rsrc(const void* p, unsigned bytes) { return __builtin_amdgcn_make_buffer_rsrc((void*)((const char*)p - BIAS), 0, bytes + BIAS, 0x00020000); }
DI void dma4(unsigned ldsaddr, const rsrc_t ra, const rsrc_t rb, const rsrc_t rc, const rsrc_t rd, unsigned va, unsigned vb, unsigned s0, unsigned s1, unsigned s2, unsigned s3) {
    asm volatile("s_mov_b32 m0, %0\n\ts_nop 0\n\t"
                 "buffer_load_dwordx4 %5, %1, %7 offen lds\n\tbuffer_load_dwordx4 %6, %2, %8 offen offset:1024 lds\n\t"
                 "buffer_load_dwordx4 %5, %3, %9 offen offset:2048 lds\n\tbuffer_load_dwordx4 %6, %4, %10 offen offset:3072 lds"
                 ::"s"(ldsaddr), "s"(ra), "s"(rb), "s"(rc), "s"(rd), "v"(va), "v"(vb), "s"(s0 + BIAS), "s"(s1 + BIAS - 1024), "s"(s2 + BIAS - 2048), "s"(s3 + BIAS - 3072) : "memory");
}
DI void dma2(unsigned ldsaddr, const rsrc_t r, unsigned va, unsigned vb, unsigned s0, unsigned s1) {
    asm volatile("s_mov_b32 m0, %0\n\ts_nop 0\n\tbuffer_load_dwordx4 %2, %1, %4 offen lds\n\tbuffer_load_dwordx4 %3, %1, %5 offen offset:1024 lds"
                 ::"s"(ldsaddr), "s"(r), "v"(va), "v"(vb), "s"(s0 + BIAS), "s"(s1 + BIAS - 1024) : "memory");
}
#define WAITV(n) asm volatile("s_waitcnt vmcnt(" #n ")" ::: "memory")
#define PIN() do { asm volatile("" ::: "memory"); __builtin_amdgcn_sched_barrier(0); } while (0)
template <int N> DI void waitv() { asm volatile("s_waitcnt vmcnt(%0)" ::"n"(N) : "memory"); }
#define B_EARLY 1
#define PD1 0
#define PD2 4
#define PD3 4
#define PRIO_C 0
#define K0 3
#define R1MODE 2
#define STAMP2_SS(ss) do { if (ss == 0) STAMP2(0); if (ss == 1) STAMP2(1); if (ss == 2) STAMP2(2); if (ss == 3) STAMP2(3); if (ss == 4) STAMP2(4); if (ss == 5) STAMP2(5); if (ss == 6) STAMP2(6); if (ss == 7) STAMP2(7); } while (0)
#define SBAR() asm volatile("s_barrier" ::: "memory")
#define BAR() asm volatile("s_waitcnt lgkmcnt(0)\n\ts_barrier" ::: "memory")
DI u32x2 both_halves(float v) { const unsigned a = asu(v); const auto r = __builtin_amdgcn_permlane32_swap(a, a, false, false); return (u32x2){r[0], r[1]}; }
DI float halves_add(float v) { const u32x2 r = both_halves(v); return asf(r[0]) + asf(r[1]); }
DI float halves_max(float v) { const u32x2 r = both_halves(v); return fmaxf(asf(r[0]), asf(r[1])); }
DI float halves_min(float v) { const u32x2 r = both_halves(v); return fminf(asf(r[0]), asf(r[1])); }
DI float dpp_ror4(float v) { return asf(__builtin_amdgcn_update_dpp(0, (int)asu(v), 0x124, 0xf, 0xf, true)); }
DI float dpp_ror8(float v) { return asf(__builtin_amdgcn_update_dpp(0, (int)asu(v), 0x128, 0xf, 0xf, true)); }
DI float rows16_add(float v) { const unsigned a = asu(v); const auto r = __builtin_amdgcn_permlane16_swap(a, a, false, false); return asf(r[0]) + asf(r[1]); }
DI float stride4_add(float v) { v += dpp_ror4(v); v += dpp_ror8(v); v = rows16_add(v); return halves_add(v); }
DI float quad_add(float v) {
    v += __builtin_bit_cast(float, __builtin_amdgcn_update_dpp(0, __builtin_bit_cast(int, v), 0xB1, 0xf, 0xf, true));
    v += __builtin_bit_cast(float, __builtin_amdgcn_update_dpp(0, __builtin_bit_cast(int, v), 0x4E, 0xf, 0xf, true));
    return v;
}
DI void st16(const rsrc_t rs, u32x4 v, unsigned voff, unsigned soff) { __builtin_amdgcn_raw_buffer_store_b128(v, rs, voff + soff, 0, 16); }
DI u32x4 ld16(const rsrc_t rs, unsigned voff, unsigned soff) { return __builtin_amdgcn_raw_buffer_load_b128(rs, voff, soff, 16); }
DI unsigned ld4(const rsrc_t rs, unsigned voff, unsigned soff) { return __builtin_amdgcn_raw_buffer_load_b32(rs, voff, soff, 16); }

DI void poll_flags3(const char* ws, unsigned off, u64 tag, int lane) {
    const int l = lane < 11 ? lane : 10;
    const GAS u64* f = (const GAS u64*)(ws + off) + (l < 6 ? l : l + 2) * 32;
#pragma unroll 1
    for (unsigned spin = 0; spin < SPIN_CAP; ++spin) {
        const u64 v = __hip_atomic_load(f, __ATOMIC_RELAXED, __HIP_MEMORY_SCOPE_AGENT);
        if (__all(v == tag)) break;
        __builtin_amdgcn_s_sleep(1);
    }
    asm volatile("" ::: "memory");
}
DI void nohook(int) {}
template <int DELAY = 0>
DI void poll_flags(const char* ws, unsigned off, int n, u64 tag, int lane) {
#pragma unroll
    for (int i = 0; i < DELAY; ++i) __builtin_amdgcn_s_sleep(2);
    const GAS u64* f = (const GAS u64*)(ws + off) + (lane < n ? lane : n - 1) * 32;
#pragma unroll 1
    for (unsigned spin = 0; spin < SPIN_CAP; ++spin) {
        const u64 v = __hip_atomic_load(f, __ATOMIC_RELAXED, __HIP_MEMORY_SCOPE_AGENT);
        if (__all(v == tag)) break;
        __builtin_amdgcn_s_sleep(1);
    }
    asm volatile("" ::: "memory");
}
DI void arrive_publish(LAS char* lds, int cnt, unsigned n, char* ws, unsigned flag_off, u64 tag, int lane) {
    if (lane == 0) {
        const unsigned old = __hip_atomic_fetch_add((LAS unsigned*)(lds + L_CNT) + cnt, 1u, __ATOMIC_RELAXED, __HIP_MEMORY_SCOPE_WORKGROUP);
        if (old == n - 1) __hip_atomic_store((GAS u64*)(ws + flag_off), tag, __ATOMIC_RELAXED, __HIP_MEMORY_SCOPE_AGENT);
    }
}
DI void lsig(unsigned lb, int idx) {
    const unsigned a = lb + L_CNT + idx * 4, one = 1;
    asm volatile("s_mov_b64 exec, 1\n\tds_add_u32 %0, %1\n\ts_mov_b64 exec, -1" ::"v"(a), "v"(one) : "memory");
}
DI void lwait(LAS char* lds, int idx, unsigned n) {
    const volatile LAS unsigned* c = (const volatile LAS unsigned*)(lds + L_CNT) + idx;
#pragma unroll 1
    for (unsigned spin = 0; spin < 64 * SPIN_CAP; ++spin) {
        if ((unsigned)__builtin_amdgcn_readfirstlane(*c) >= n) break;
        __builtin_amdgcn_s_sleep(1);
    }
    asm volatile("" ::: "memory");
}
DI void dual_softmax(const f32x16& p, float sl, u32x4& pos, u32x4& neg) {
    float mx = fmaxf(fmaxf(fmaxf(p[0], p[1]), fmaxf(p[2], p[3])), fmaxf(fmaxf(p[4], p[5]), fmaxf(p[6], p[7])));
    float mn = fminf(fminf(fminf(p[0], p[1]), fminf(p[2], p[3])), fminf(fminf(p[4], p[5]), fminf(p[6], p[7])));
    mx = halves_max(mx); mn = halves_min(mn);
    const float cx = -mx * sl, cn = mn * sl;
    f32x2 ep[4], en[4];
#pragma unroll
    for (int i = 0; i < 4; ++i) {
        const f32x2 v = {p[2 * i], p[2 * i + 1]};
        const f32x2 u = v * sl + cx, d = v * (-sl) + cn;
        ep[i] = (f32x2){__builtin_amdgcn_exp2f(u[0]), __builtin_amdgcn_exp2f(u[1])};
        en[i] = (f32x2){__builtin_amdgcn_exp2f(d[0]), __builtin_amdgcn_exp2f(d[1])};
    }
    const f32x2 sp = (ep[0] + ep[1]) + (ep[2] + ep[3]), sn = (en[0] + en[1]) + (en[2] + en[3]);
    const float rp = __builtin_amdgcn_rcpf(halves_add(sp[0] + sp[1])), rn = __builtin_amdgcn_rcpf(halves_add(sn[0] + sn[1]));
#pragma unroll
    for (int i = 0; i < 4; ++i) { const f32x2 a = ep[i] * rp, b = en[i] * rn; pos[i] = pack2(a[0], a[1]); neg[i] = pack2(b[0], b[1]); }
}

#define SB() __builtin_amdgcn_sched_barrier(0)
template <int N, int STRIDE, int SECOND, int OFF, int NA, class Hook>
DI void gemm2(LAS char* lds, unsigned b0, unsigned b1, const u32x4 (&act)[NA], f32x16& acc0, f32x16& acc1, Hook hook) {
    u32x4 fb[2][4];
    asm volatile("" : "+v"(b0), "+v"(b1));
    constexpr int NG = (N + 1) / 2;
#pragma unroll
    for (int e = 0; e < 2; ++e) { fb[0][2 * e] = tr2(lds, b0 + e * STRIDE, SECOND); fb[0][2 * e + 1] = tr2(lds, b1 + e * STRIDE, SECOND); }
    SB();
#pragma unroll
    for (int g = 0; g < NG; ++g) {
#pragma unroll
        for (int e = 0; e < 2; ++e) {
            const int i = 2 * g + e, n = i + 2;
            if (i < N) acc0 = mfma(fb[g & 1][2 * e], act[OFF + i], acc0);
            if (n < N) fb[(g + 1) & 1][2 * e] = tr2(lds, b0 + n * STRIDE, SECOND);
            SB();
            if (i < N) acc1 = mfma(fb[g & 1][2 * e + 1], act[OFF + i], acc1);
            if (n < N) fb[(g + 1) & 1][2 * e + 1] = tr2(lds, b1 + n * STRIDE, SECOND);
            SB();
        }
        hook(g);
    }
    asm volatile("" : "+v"(acc0), "+v"(acc1));
}
template <int OFF, class Hook>
DI void gemm4(LAS char* lds, unsigned b0, unsigned b1, const u32x4 (&act)[32], f32x16& g0, f32x16& g1, f32x16& u0, f32x16& u1, Hook hook) {
    u32x4 fb[2][4];
    asm volatile("" : "+v"(b0), "+v"(b1));
    fb[0][0] = tr2(lds, b0, 1024); fb[0][1] = tr2(lds, b1, 1024); fb[0][2] = tr2(lds, b0 + 2048, 1024); fb[0][3] = tr2(lds, b1 + 2048, 1024);
    SB();
#pragma unroll
    for (int i = 0; i < 16; ++i) {
        const int o = (i + 1) * 4096;
        g0 = mfma(fb[i & 1][0], act[OFF + i], g0);
        if (i < 15) fb[(i + 1) & 1][0] = tr2(lds, b0 + o, 1024);
        SB();
        g1 = mfma(fb[i & 1][1], act[OFF + i], g1);
        if (i < 15) fb[(i + 1) & 1][1] = tr2(lds, b1 + o, 1024);
        SB();
        u0 = mfma(fb[i & 1][2], act[OFF + i], u0);
        if (i < 15) fb[(i + 1) & 1][2] = tr2(lds, b0 + o + 2048, 1024);
        SB();
        u1 = mfma(fb[i & 1][3], act[OFF + i], u1);
        if (i < 15) fb[(i + 1) & 1][3] = tr2(lds, b1 + o + 2048, 1024);
        SB();
        hook(i);
    }
    asm volatile("" : "+v"(g0), "+v"(g1), "+v"(u0), "+v"(u1));
}
DI void touch_lines(const rsrc_t rs, unsigned ldsaddr, unsigned voff, unsigned soff) {
    asm volatile("s_mov_b32 m0, %0\n\ts_nop 0\n\tbuffer_load_dword %1, %2, %3 offen lds" ::"s"(ldsaddr), "v"(voff), "s"(rs), "s"(soff) : "memory");
}

#define STAMP(i) do {} while (0)
#define CSTAMP(i) do {} while (0)
#define STAMP2(i) do {} while (0)

__global__ void __launch_bounds__(512, 2)
hedgehog_block(const bf16_t* __restrict__ x, const bf16_t* __restrict__ wqkv, const bf16_t* __restrict__ wphi, const bf16_t* __restrict__ wo,
               const bf16_t* __restrict__ wg, const bf16_t* __restrict__ wu, const bf16_t* __restrict__ wd, bf16_t* __restrict__ out, char* __restrict__ ws) {
    extern __shared__ __attribute__((aligned(16))) char smem[];
    LAS char* lds = (LAS char*)smem;
    const unsigned lb = (unsigned)(size_t)lds;
    const int tid = threadIdx.x, lane = tid & 63;
    const int w = __builtin_amdgcn_readfirstlane(tid >> 6);
    const int b = blockIdx.x, xcd = b & 7, slot = b >> 3;
    const int p = 4 * (xcd >> 1) + (slot >> 3), j = 8 * (xcd & 1) + (slot & 7);
    const int pP = 2 * xcd + (slot >> 4), jP = slot & 15, c = slot & 7, half = (slot >> 3) & 1;
    const bool has3 = (j & 7) < (j < 8 ? 6 : 5);
    const int u3 = 32 + (j < 8 ? j : j - 2);
    const int t = lane & 31, h2 = lane >> 5, q = (lane & 15) >> 2, pp = lane & 3, blk = (lane >> 4) & 1;
    const int m = w & 3, a = w >> 2, lw = w & 3;
    const int q4 = slot >> 3;
    const int r1 = R1MODE == 0 ? 0 : R1MODE == 1 ? ((c + 2 * q4) & 7) : R1MODE == 2 ? 4 * (q4 & 1) : 2 * q4, r2 = (2 * c + 4 * q4) & 15, r3 = (4 * c + 8 * q4) & 31, r4 = (c + 3 * q4) % 11;
    STAMP(0); CSTAMP(30);
    const rsrc_t rsX = __builtin_amdgcn_make_buffer_rsrc((void*)x, 0, 2048 * 512 * 2, 0x00020000);
    const rsrc_t rsQ = __builtin_amdgcn_make_buffer_rsrc((void*)wqkv, 0, 512 * 1536 * 2, 0x00020000);
    const rsrc_t rsW = __builtin_amdgcn_make_buffer_rsrc((void*)ws, 0, WS_BYTES, 0x00020000);
    u64 tag = 0;
#define UNIFORM64(v) (((u64)(unsigned)__builtin_amdgcn_readfirstlane((unsigned)((v) >> 32)) << 32) | (unsigned)__builtin_amdgcn_readfirstlane((unsigned)(v)))
    const u32x4 ones = {0x3F803F80u, 0x3F803F80u, 0x3F803F80u, 0x3F803F80u};
    const unsigned c128 = ((lane & 7) ^ (4 * ((lane >> 4) & 1))) * 16;
    const unsigned vrow = (lane >> 3) * 1024 + c128;
    const unsigned trn = (8 * h2 + q) * 64 + pp * 8;

    const rsrc_t rsG = biased_rsrc(wg, 512 * 1376 * 2), rsU = biased_rsrc(wu, 512 * 1376 * 2);
    const unsigned vgu = (lane >> 2) * 2752 + (lane & 3) * 16, vgu8 = (lane >> 3) * 2752 + c128;
#define ISSUE_STAGE(st, halfbase, g0, g1) do { _Pragma("unroll") for (int g_ = (g0); g_ < (g1); ++g_) { const int i_ = 4 * g_ + lw; const unsigned so_ = 16 * ((16 * (st) + i_ + r3) & 31) * 2752 + 128 * j; \
            dma4(lb + (halfbase) + i_ * 4096, rsG, rsG, rsU, rsU, vgu8, vgu8, so_, so_ + 8 * 2752, so_, so_ + 8 * 2752); } } while (0)
#define ISSUE_UNIT(u, halfbase) do { _Pragma("unroll") for (int g_ = 0; g_ < 4; ++g_) { const int s_ = 2 * (4 * g_ + lw); \
            const unsigned sa_ = 16 * ((s_ + r3) & 31) * 2752 + 64 * (u), sb_ = 16 * ((s_ + 1 + r3) & 31) * 2752 + 64 * (u); \
            dma4(lb + (halfbase) + s_ * 2048, rsG, rsU, rsG, rsU, vgu, vgu, sa_, sa_, sb_, sb_); } } while (0)
    if (w == 3) {
        u64 pc; asm volatile("s_getpc_b64 %0" : "=s"(pc));
        const rsrc_t rsC = __builtin_amdgcn_make_buffer_rsrc((void*)(pc & ~127ull), 0, 36864, 0x00020000);
        touch_lines(rsC, lb + L_DUMMY, (slot * 9 + (lane < 9 ? lane : 0)) * 128, 0);
    }
    if (w < 3) {
        const rsrc_t rsA = __builtin_amdgcn_make_buffer_rsrc((void*)wg, 0, 512 * 1376 * 2, 0x00020000), rsB = __builtin_amdgcn_make_buffer_rsrc((void*)wu, 0, 512 * 1376 * 2, 0x00020000);
        const rsrc_t rsCc = __builtin_amdgcn_make_buffer_rsrc((void*)wd, 0, 512 * 1376 * 2, 0x00020000), rsE = __builtin_amdgcn_make_buffer_rsrc((void*)wo, 0, 512 * 512 * 2, 0x00020000);
        const unsigned blk64 = b * 3 + w;
        if (blk64 < 172) touch_lines(rsA, lb + L_DUMMY, (blk64 * 64 + lane) * 128, 0);
        else if (blk64 < 344) touch_lines(rsB, lb + L_DUMMY, ((blk64 - 172) * 64 + lane) * 128, 0);
        else if (blk64 < 516) touch_lines(rsCc, lb + L_DUMMY, ((blk64 - 344) * 64 + lane) * 128, 0);
        else if (blk64 < 580) touch_lines(rsE, lb + L_DUMMY, ((blk64 - 516) * 64 + lane) * 128, 0);
    }
    if (w == 3) {
        unsigned o = WS_KVP + (j * 16 + p) * KVP_BYTES + (lane < 17 ? lane : 0) * 128;
        o = lane == 17 ? WS_F1 + (j * 16 + p) * 256 : o;
        o = lane == 18 ? WS_F2 + (p * 16 + j) * 256 : o;
        o = lane == 19 ? WS_F3 + (pP * 16 + jP) * 256 : o;
        o = lane == 20 ? WS_F4 + (p * 16 + j) * 256 : o;
        o = lane == 21 ? WS_F4 + 0x10000 + (p * 16 + j) * 256 : o;
        o = lane == 22 ? WS_XST + (128 * p + 8 * j) * 16 : o;
        o = lane == 23 ? WS_O + ((4 * p) * 32 + 2 * j) * 1024 : o;
        o = lane == 24 ? WS_H + ((4 * pP + 2 * half) * 32 + 4 * c) * 1024 : o;
        o = lane == 25 ? WS_ACT + ((4 * p) * 86 + 4 * j) * 1024 : o;
        o = lane == 26 ? WS_ACT + ((4 * p + 3) * 86 + 4 * j) * 1024 : o;
        o = lane == 27 ? WS_HST + ((4 * pP + 2 * half) * 16 + 2 * c) * 256 : o;
        touch_lines(rsW, lb + L_DUMMY, o, 0);
    }
    u32x4 xf[32];
    float yk[16];
    float mean1, rstd1;
    if (w < 4) {
        {
            u64 epoch = __hip_atomic_load((const GAS u64*)ws, __ATOMIC_RELAXED, __HIP_MEMORY_SCOPE_AGENT);
            const unsigned R = 128 * p + 8 * j + 2 * w;
            const u32x4 v0 = __builtin_amdgcn_raw_buffer_load_b128(rsX, lane * 16, R * 1024, 0), v1 = __builtin_amdgcn_raw_buffer_load_b128(rsX, lane * 16, R * 1024 + 1024, 0);
            if (tid < 16) *(LAS unsigned*)(lds + L_CNT + tid * 4) = 0u;
            f32x2 a1 = {0.f, 0.f}, a2 = {0.f, 0.f}, b1 = {0.f, 0.f}, b2 = {0.f, 0.f};
#pragma unroll
            for (int e = 0; e < 4; ++e) {
                const f32x2 z0 = {blo(v0[e]), bhi(v0[e])}, z1 = {blo(v1[e]), bhi(v1[e])};
                a1 += z0; a2 += z0 * z0; b1 += z1; b2 += z1 * z1;
            }
            const float su0 = stride4_add(quad_add(a1[0] + a1[1])), sq0 = stride4_add(quad_add(a2[0] + a2[1]));
            const float su1 = stride4_add(quad_add(b1[0] + b1[1])), sq1 = stride4_add(quad_add(b2[0] + b2[1]));
            const float su = (lane & 1) ? su1 : su0, sq = (lane & 1) ? sq1 : sq0;
            const float mean = su * (1.0f / 512.0f), var = fmaxf(sq * (1.0f / 512.0f) - mean * mean, 0.f);
            const float rstd = __builtin_amdgcn_rsqf(var + 1e-5f);
            asm volatile("s_waitcnt vmcnt(0)" : "+v"(epoch)::"memory");
            tag = UNIFORM64(epoch) + 0x9E3779B97F4A7C15ull;
            if (tid == 0) *(LAS u64*)(lds + L_TAG) = tag;
            if (lane < 2) st16(rsW, (u32x4){asu(mean), asu(rstd), (unsigned)tag, (unsigned)(tag >> 32)}, (R + lane) * 16, WS_XST);
        }
        STAMP(1);
        const unsigned xa0 = (32 * m + t) * 128 + ((h2 ^ ((t >> 1) & 7)) * 16);
        const unsigned twA = 16384 + (blk ? 1024 + 32 : 0) + trn, twB = 16384 + (blk ? 1024 : 32) + trn;
        f32x16 accA = {}, accB = {};
        u32x4 fa[2][2], fb[2][2], xs;
        unsigned bA = twA, bB = twB, bx0 = xa0, bx1 = xa0 ^ 32, bx2 = xa0 ^ 64, bx3 = xa0 ^ 96;
        asm volatile("" : "+v"(bA), "+v"(bB), "+v"(bx0), "+v"(bx1), "+v"(bx2), "+v"(bx3));
#pragma unroll
        for (int ss = 0; ss <= 8; ++ss) {
            if (ss < 8) {
                BAR();
                STAMP2_SS(ss);
                if (ss == 0) STAMP(2);
                if (ss == 6) { xs = ld16(rsW, (128 * p + 32 * m + t) * 16, WS_XST); SB(); }
            }
#pragma unroll
            for (int e = 0; e < 2; ++e) {
                if (ss > 0) accA = mfma(fa[1][e], xf[4 * ss - 2 + e], accA);
                if (ss < 8) { xf[4 * ss + e] = *(LAS u32x4*)(lds + (e ? bx1 : bx0)); fa[0][e] = tr2(lds, bA + e * 2048, 256); }
                SB();
                if (ss > 0) accB = mfma(fb[1][e], xf[4 * ss - 2 + e], accB);
                if (ss < 8) fb[0][e] = tr2(lds, bB + e * 2048, 256);
                SB();
            }
            if (ss < 8) {
#pragma unroll
                for (int e = 0; e < 2; ++e) {
                    accA = mfma(fa[0][e], xf[4 * ss + e], accA);
                    xf[4 * ss + 2 + e] = *(LAS u32x4*)(lds + (e ? bx3 : bx2)); fa[1][e] = tr2(lds, bA + (2 + e) * 2048, 256);
                    SB();
                    accB = mfma(fb[0][e], xf[4 * ss + e], accB);
                    fb[1][e] = tr2(lds, bB + (2 + e) * 2048, 256);
                    SB();
                }
                if (ss < 7) {
                    const unsigned d = ss == 5 ? (unsigned)(-5 * (int)SS_BYTES) : SS_BYTES;
                    bA += d; bB += d; bx0 += d; bx1 += d; bx2 += d; bx3 += d;
                    asm volatile("" : "+v"(bA), "+v"(bB), "+v"(bx0), "+v"(bx1), "+v"(bx2), "+v"(bx3));
                }
            }
        }
        STAMP(3);
#pragma unroll
        for (int g = 0; g < 4; ++g) *(LAS f32x4*)(lds + L_XCH + m * 4096 + g * 1024 + lane * 16) = (f32x4){accB[4 * g], accB[4 * g + 1], accB[4 * g + 2], accB[4 * g + 3]};
#pragma unroll 1
        for (unsigned spin = 0; spin < SPIN_CAP; ++spin) {
            if (__all(xs[2] == (unsigned)tag && xs[3] == (unsigned)(tag >> 32))) break;
            __builtin_amdgcn_s_sleep(1);
            xs = ld16(rsW, (128 * p + 32 * m + t) * 16, WS_XST);
        }
        mean1 = asf(xs[0]); rstd1 = asf(xs[1]);
        if (h2 == 0) *(LAS f32x2*)(lds + L_STAT + (32 * m + t) * 8) = (f32x2){mean1, rstd1};
        BAR();
        STAMP(4);
#pragma unroll
        for (int g = 0; g < 4; ++g) {
            const f32x4 cs = *(LAS f32x4*)(lds + L_CS1 + (8 * g + 4 * h2) * 4);
#pragma unroll
            for (int e = 0; e < 4; ++e) { const float y = __builtin_fmaf(-mean1, cs[e], accA[4 * g + e]); yk[4 * g + e] = g < 2 ? y : y * rstd1; }
        }
    } else {
        const unsigned vx0 = (lane >> 3) * 1024 + (((lane & 7) ^ (lane >> 4)) * 16), vx1 = (lane >> 3) * 1024 + (((lane & 7) ^ (4 + (lane >> 4))) * 16);
        const unsigned vwk = (lane >> 2) * 3072 + (lane & 3) * 16, vwv = (lane >> 2) * 3072 + ((lane & 3) ^ 2) * 16;
        const rsrc_t rbX = biased_rsrc(x, 2048 * 512 * 2), rbQ = biased_rsrc(wqkv, 512 * 1536 * 2);
#define ISSUE_SS(ss, v) do { const unsigned sb_ = lb + ((ss) % 6) * SS_BYTES, ks_ = ((ss) + r1) & 7; \
            const unsigned sx_ = (128 * p + 32 * (v)) * 1024 + ks_ * 128, sw_ = (16 * (v) + 64 * ks_) * 3072 + (512 + 32 * j) * 2; \
            dma2(sb_ + 16384 + (v) * 2048, rbQ, vwk, vwv, sw_, sw_ + 1024); \
            dma4(sb_ + (v) * 4096, rbX, rbX, rbX, rbX, vx0, vx1, sx_, sx_ + 8 * 1024, sx_ + 16 * 1024, sx_ + 24 * 1024); } while (0)
        if (lw == 0) {
            const rsrc_t rsP = __builtin_amdgcn_make_buffer_rsrc((void*)wphi, 0, 32 * 256 * 2, 0x00020000);
            dma16(rsP, lb + L_WPHI, lane * 16, j * 1024);
        }
#pragma unroll
        for (int ss = 0; ss < K0; ++ss) ISSUE_SS(ss, lw);
        STAMP(1);
        f32x4 csacc = {};
        const unsigned tw = 16384 + ((lw & 1) ? 1024 + ((lw >> 1) ^ 1) * 32 : (lw >> 1) * 32) + (lane >> 5) * 2048 + (8 * ((lane >> 4) & 1) + q) * 64 + pp * 8;
        constexpr int NL = 8 - K0;
#define P1WAIT(s) (6 * ((((s) + K0 - 1 < 7 ? (s) + K0 - 1 : 7) - (s))) + ((s) > 4 ? 2 * ((s) - 4) : 0))
#pragma unroll
        for (int ss = 0; ss < 8; ++ss) {
            if (ss == 0) waitv<P1WAIT(0)>();
            if (ss == 1) waitv<P1WAIT(1)>();
            if (ss == 2) waitv<P1WAIT(2)>();
            if (ss == 3) waitv<P1WAIT(3)>();
            if (ss == 4) waitv<P1WAIT(4)>();
            if (ss == 5) waitv<P1WAIT(5)>();
            if (ss == 6) waitv<P1WAIT(6)>();
            if (ss == 7) waitv<P1WAIT(7)>();
            STAMP2_SS(ss);
            BAR();
            if (ss == 0) { const u64 tv = *(LAS u64*)(lds + L_TAG); tag = UNIFORM64(tv); STAMP(2); }
            unsigned bw = (ss % 6) * SS_BYTES + tw;
            asm volatile("" : "+v"(bw));
            const u32x4 f0 = tr2(lds, bw, 256), f1 = tr2(lds, bw + 4096, 256);
            SB();
            if (ss < NL) ISSUE_SS(K0 + ss, lw);
            if (ss == 6) {
#pragma unroll
                for (int i = 0; i < 4; ++i) *(LAS u32x4*)(lds + L_VIMG + (4 * lw + i) * 1024 + lane * 16) = ones;
            }
            if (ss >= 4) {
                const int pc = 8 * lw + 2 * (ss - 4);
                const unsigned so = 16 * ((pc + 4 * r1) & 31) * 3072 + 64 * j;
                dma2(lb + L_WQ + pc * 1024, rbQ, vwk, vwk, so, so + 16 * 3072);
            }
            csacc = mfma16(f0, ones, csacc);
            csacc = mfma16(f1, ones, csacc);
            SB();
        }
        if ((lane & 15) == 0) *(LAS f32x4*)(lds + L_CS1 + (lw >> 1) * 128 + (lw & 1) * 64 + (lane >> 4) * 16) = csacc;
        STAMP(3);
        BAR();
        STAMP(4);
        const f32x2 st = *(LAS f32x2*)(lds + L_STAT + (32 * m + t) * 8);
        mean1 = st[0]; rstd1 = st[1];
#pragma unroll
        for (int g = 0; g < 4; ++g) {
            const f32x4 cs = *(LAS f32x4*)(lds + L_CS1 + 128 + (8 * g + 4 * h2) * 4);
            const f32x4 v = *(LAS f32x4*)(lds + L_XCH + m * 4096 + g * 1024 + lane * 16);
#pragma unroll
            for (int e = 0; e < 4; ++e) { const float y = __builtin_fmaf(-mean1, cs[e], v[e]); yk[4 * g + e] = g < 2 ? y : y * rstd1; }
        }
    }
    const unsigned imgA = L_PHI + a * 8192 + (32 * m + t) * 64 + ((h2 ^ ((t >> 1) & 7)) * 8);
    const u32x4 aphi = tr2(lds, L_WPHI + a * 512 + (4 * h2 + q) * 32 + pp * 8, 256);
    {
        const u32x4 kf = {pack2(yk[0], yk[1]), pack2(yk[2], yk[3]), pack2(yk[4], yk[5]), pack2(yk[6], yk[7])};
        const f32x16 zero = {};
        const f32x16 pk = mfma(aphi, kf, zero);
        *(LAS u32x2*)(lds + imgA + (L_VIMG - L_PHI)) = (u32x2){pack2(yk[8], yk[9]), pack2(yk[10], yk[11])};
        *(LAS u32x2*)(lds + (imgA ^ 16) + (L_VIMG - L_PHI)) = (u32x2){pack2(yk[12], yk[13]), pack2(yk[14], yk[15])};
        u32x4 pos, neg;
        dual_softmax(pk, rstd1 * LOG2E, pos, neg);
        *(LAS u32x2*)(lds + imgA) = (u32x2){pos[0], pos[1]};
        *(LAS u32x2*)(lds + (imgA ^ 16)) = (u32x2){pos[2], pos[3]};
        *(LAS u32x2*)(lds + (imgA ^ 32)) = (u32x2){neg[0], neg[1]};
        *(LAS u32x2*)(lds + (imgA ^ 48)) = (u32x2){neg[2], neg[3]};
    }
    if (w >= 4) waitv<0>();
    BAR();
    STAMP(5);
    float yq[8];
    if (w < 4) {
        unsigned tq = L_WQ + trn + blk * 32;
        asm volatile("" : "+v"(tq));
        f32x16 qa = {};
        u32x4 qb[2][4];
#pragma unroll
        for (int s = 0; s < 4; ++s) qb[0][s] = tr2(lds, tq + s * 1024, 256);
        SB();
#pragma unroll
        for (int s = 0; s < 32; ++s) {
            qa = mfma(qb[(s >> 2) & 1][s & 3], xf[s], qa);
            if (s < 28) qb[((s >> 2) + 1) & 1][s & 3] = tr2(lds, tq + (s + 4) * 1024, 256);
            SB();
        }
#pragma unroll
        for (int g = 0; g < 2; ++g) *(LAS f32x4*)(lds + L_XCH + m * 4096 + g * 1024 + lane * 16) = (f32x4){qa[8 + 4 * g], qa[9 + 4 * g], qa[10 + 4 * g], qa[11 + 4 * g]};
        STAMP(6);
        BAR();
#pragma unroll
        for (int g = 0; g < 2; ++g) {
            const f32x4 c0 = *(LAS f32x4*)(lds + L_CSQ + (8 * g + 4 * h2) * 4);
#pragma unroll
            for (int e = 0; e < 4; ++e) yq[4 * g + e] = __builtin_fmaf(-mean1, c0[e], qa[4 * g + e]);
        }
    } else {
        if (lw >= 2) {
            __builtin_amdgcn_s_setprio(3);
            const int ah = lw - 2;
            f32x16 kv = {};
            unsigned aa[2], ab[2];
#pragma unroll
            for (int u = 0; u < 2; ++u) {
                const unsigned R = 8 * h2 + 4 * u + q, sw = (R >> 1) & 7;
                aa[u] = L_PHI + ah * 8192 + R * 64 + (((4 * blk + pp) ^ sw) * 8);
                ab[u] = aa[u] + (L_VIMG - L_PHI);
                asm volatile("" : "+v"(aa[u]), "+v"(ab[u]));
            }
            u32x4 A[8], B[8];
#pragma unroll
            for (int s = 0; s < 8; ++s) {
                const s16x4 a0 = __builtin_amdgcn_ds_read_tr16_b64_v4i16((LAS s16x4*)(lds + aa[0] + s * 1024));
                const s16x4 a1 = __builtin_amdgcn_ds_read_tr16_b64_v4i16((LAS s16x4*)(lds + aa[1] + s * 1024));
                const s16x4 b0 = __builtin_amdgcn_ds_read_tr16_b64_v4i16((LAS s16x4*)(lds + ab[0] + s * 1024));
                const s16x4 b1 = __builtin_amdgcn_ds_read_tr16_b64_v4i16((LAS s16x4*)(lds + ab[1] + s * 1024));
                A[s] = __builtin_bit_cast(u32x4, __builtin_shufflevector(a0, a1, 0, 1, 2, 3, 4, 5, 6, 7));
                B[s] = __builtin_bit_cast(u32x4, __builtin_shufflevector(b0, b1, 0, 1, 2, 3, 4, 5, 6, 7));
                if (s == 3) PIN();
            }
            PIN();
#pragma unroll
            for (int s = 0; s < 8; ++s) kv = mfma(A[s], B[s], kv);
            if (t <= 16) {
                const unsigned base = WS_KVP + (j * 16 + p) * KVP_BYTES + ((ah * 4 + h2) * 17 + t) * 16;
                st16(rsW, (u32x4){pack2(kv[0], kv[1]), pack2(kv[2], kv[3]), pack2(kv[4], kv[5]), pack2(kv[6], kv[7])}, base, 0);
                st16(rsW, (u32x4){pack2(kv[8], kv[9]), pack2(kv[10], kv[11]), pack2(kv[12], kv[13]), pack2(kv[14], kv[15])}, base + 34 * 16, 0);
            }
            WAITV(0);
            arrive_publish(lds, 0, 2, ws, WS_F1 + (j * 16 + p) * 256, tag, lane);
            __builtin_amdgcn_s_setprio(0);
            STAMP(6);
        } else {
            unsigned tq = L_WQ + lw * 32 + (8 * (lane >> 4) + q) * 64 + pp * 8;
            asm volatile("" : "+v"(tq));
            f32x4 qcs = {};
#pragma unroll
            for (int g = 0; g < 4; ++g) {
                u32x4 f[4];
#pragma unroll
                for (int s = 0; s < 4; ++s) f[s] = tr2(lds, tq + (4 * g + s) * 2048, 256);
                PIN();
#pragma unroll
                for (int s = 0; s < 4; ++s) qcs = mfma16(f[s], ones, qcs);
            }
            if ((lane & 15) == 0) *(LAS f32x4*)(lds + L_CSQ + lw * 64 + (lane >> 4) * 16) = qcs;
            STAMP(6);
        }
        BAR();
        {
            const rsrc_t rsO = biased_rsrc(wo, 512 * 512 * 2);
#pragma unroll
            for (int g = 0; g < 4; ++g) {
                const int i = 4 * g + lw;
                const unsigned so = 16 * ((i + r2) & 15) * 1024 + 128 * c;
                dma4(lb + L_X + i * 4096, rsO, rsO, rsO, rsO, vrow, vrow, so, so + 8 * 1024, so + 256 * 1024, so + 264 * 1024);
            }
        }
#pragma unroll
        for (int g = 0; g < 2; ++g) {
            const f32x4 c0 = *(LAS f32x4*)(lds + L_CSQ + (16 + 8 * g + 4 * h2) * 4);
            const f32x4 v = *(LAS f32x4*)(lds + L_XCH + m * 4096 + g * 1024 + lane * 16);
#pragma unroll
            for (int e = 0; e < 4; ++e) yq[4 * g + e] = __builtin_fmaf(-mean1, c0[e], v[e]);
        }
    }
    STAMP(7);
    u32x4 qpos, qneg;
    {
        const u32x4 qf = {pack2(yq[0], yq[1]), pack2(yq[2], yq[3]), pack2(yq[4], yq[5]), pack2(yq[6], yq[7])};
        const f32x16 zero = {};
        const f32x16 pq = mfma(aphi, qf, zero);
        dual_softmax(pq, rstd1 * LOG2E, qpos, qneg);
    }
    STAMP(8);
    unsigned kso[16];
#pragma unroll
    for (int i = 0; i < 16; ++i) { kso[i] = WS_KVP + (j * 16 + ((i + 4 * q4) & 15)) * KVP_BYTES; asm volatile("" : "+s"(kso[i])); }
    if (w == 0) poll_flags<PD1>(ws, WS_F1 + j * 16 * 256, 16, tag, lane);
    BAR();
    STAMP(9);
    {
        const unsigned kb = WS_KVP + j * 16 * KVP_BYTES;
        float lo = 0.f, hi = 0.f;
        unsigned v[16], z[8];
#pragma unroll
        for (int i = 0; i < 16; ++i) v[i] = ld4(rsW, tid * 4, kso[i]);
        if (w == 0) {
#pragma unroll
            for (int i = 0; i < 8; ++i) z[i] = ld4(rsW, (512 + t) * 4 + h2 * 8 * KVP_BYTES, kb + i * KVP_BYTES);
        }
        PIN();
#pragma unroll
        for (int i = 0; i < 16; ++i) { lo += blo(v[i]); hi += bhi(v[i]); }
        if (w == 0) {
            float zl = 0.f, zh = 0.f;
#pragma unroll
            for (int i = 0; i < 8; ++i) { zl += blo(z[i]); zh += bhi(z[i]); }
            zl = halves_add(zl); zh = halves_add(zh);
            if (h2 == 0) *(LAS unsigned*)(lds + L_KVA + (512 + t) * 4) = pack2(zl, zh);
        }
        *(LAS unsigned*)(lds + L_KVA + tid * 4) = pack2(lo, hi);
    }
    BAR();
    STAMP(10);
    const int npass = has3 ? 3 : 2;
    const int m2 = w & 1, kh = (w >> 1) & 1;
    const int T2 = 4 * pP + 2 * half + m2;
    const unsigned C0 = 64 * c + 32 * kh;
    {
        const unsigned col = t < 16 ? t : 16;
        const u32x4 kp = *(LAS u32x4*)(lds + L_KVA + ((a * 4 + h2) * 17 + col) * 16);
        const u32x4 kn = *(LAS u32x4*)(lds + L_KVA + ((a * 4 + 2 + h2) * 17 + col) * 16);
        f32x16 o = {};
        o = mfma(kp, qpos, o);
        o = mfma(kn, qneg, o);
        const float r = __builtin_amdgcn_rcpf(o[8] + 1e-6f);
        st16(rsW, (u32x4){pack2(o[0] * r, o[1] * r), pack2(o[2] * r, o[3] * r), pack2(o[4] * r, o[5] * r), pack2(o[6] * r, o[7] * r)},
             WS_O + ((4 * p + m) * 32 + 2 * j + a) * 1024 + lane * 16, 0);
        WAITV(0);
        BAR();
        if (tid == 0) __hip_atomic_store((GAS u64*)(ws + WS_F2 + (p * 16 + j) * 256), tag, __ATOMIC_RELAXED, __HIP_MEMORY_SCOPE_AGENT);
        SBAR();
    }
    const unsigned trp = (4 * h2 + q) * 64 + blk * 32 + pp * 8;
    const unsigned t128 = (4 * h2 + q) * 128 + (q >> 1) * 64 + blk * 32 + pp * 8;

    if (w >= 4) {
        ISSUE_STAGE(0, L_Y, 0, 4);
        STAMP(11);
    }
    if (w >= 4) {
        const rsrc_t rsD = biased_rsrc(wd, 1376 * 512 * 2);
#define ISSUE_WD(pi, halfbase) do { _Pragma("unroll") for (int g_ = 0; g_ < ((pi) == 2 ? 3 : 4); ++g_) { const int i_ = 4 * g_ + lw; if ((pi) < 2 || i_ < 11) { \
                const int ii_ = (pi) == 2 ? (i_ + r4 >= 11 ? i_ + r4 - 11 : i_ + r4) : ((i_ + r2) & 15); \
                const unsigned sa_ = 16 * (((pi) == 2 ? 64 : 32 * (pi)) + ii_) * 1024 + 128 * c, sb_ = sa_ + 16 * ((pi) == 2 ? 11 : 16) * 1024; \
                dma4(lb + (halfbase) + i_ * 4096, rsD, rsD, rsD, rsD, vrow, vrow, sa_, sa_ + 8 * 1024, sb_, sb_ + 8 * 1024); } } } while (0)
        f32x4 cs0 = {}, cs1 = {};
        const unsigned gam = lane >> 4;
        const unsigned ca0 = (lw >> 1) * 2048 + (gam >> 1) * 4096 + (8 * (gam & 1) + q) * 128 + (((lw & 1) * 64) ^ (64 * (q >> 1))) + pp * 8;
#define COLSUM_STAGE(halfbase) do { unsigned cb_ = (halfbase) + ca0; asm volatile("" : "+v"(cb_)); \
            _Pragma("unroll") for (int g_ = 0; g_ < 4; ++g_) { const u32x4 f0_ = tr2(lds, cb_ + g_ * 16384, 512), f1_ = tr2(lds, cb_ + g_ * 16384 + 32, 512), \
                                                                         f2_ = tr2(lds, cb_ + g_ * 16384 + 8192, 512), f3_ = tr2(lds, cb_ + g_ * 16384 + 8192 + 32, 512); \
                PIN(); cs0 = mfma16(f0_, ones, cs0); cs1 = mfma16(f1_, ones, cs1); cs0 = mfma16(f2_, ones, cs0); cs1 = mfma16(f3_, ones, cs1); } } while (0)
        BAR();
        STAMP(12);
        BAR();
        SBAR();
        ISSUE_STAGE(1, L_X, 0, B_EARLY);
        BAR();
        SBAR();
        ISSUE_STAGE(1, L_X, B_EARLY, 4);
        STAMP(13);
        waitv<16>();
        BAR();
        COLSUM_STAGE(L_Y);
        WAITV(0);
        lsig(lb, 8); lwait(lds, 8, 4);
        COLSUM_STAGE(L_X);
        if ((lane & 15) == 0) {
            *(LAS f32x4*)(lds + L_CS3 + ((lw & 1) * 64 + (lw >> 1) * 32 + 4 * gam) * 4) = cs0;
            *(LAS f32x4*)(lds + L_CS3 + ((lw & 1) * 64 + (lw >> 1) * 32 + 16 + 4 * gam) * 4) = cs1;
        }
        BAR();
        if (npass == 3) {
            ISSUE_UNIT(u3, L_Y);
            WAITV(0);
            SBAR();
            ISSUE_WD(0, L_X);
            {
                unsigned cb = L_Y + (lw >> 1) * 1024 + (gam >> 1) * 2048 + (8 * (gam & 1) + q) * 64 + (lw & 1) * 32 + pp * 8;
                asm volatile("" : "+v"(cb));
                f32x4 c3 = {};
#pragma unroll
                for (int g = 0; g < 4; ++g) {
                    u32x4 f[4];
#pragma unroll
                    for (int e = 0; e < 4; ++e) f[e] = tr2(lds, cb + (4 * g + e) * 4096, 256);
                    PIN();
#pragma unroll
                    for (int e = 0; e < 4; ++e) c3 = mfma16(f[e], ones, c3);
                }
                if ((lane & 15) == 0) *(LAS f32x4*)(lds + L_CS3 + (128 + (lw >> 1) * 32 + (lw & 1) * 16 + 4 * gam) * 4) = c3;
            }
            BAR();
            WAITV(0);
            BAR();
            if (lw == 0) {
                lwait(lds, 11, 4);
                if (lane == 0) __hip_atomic_store((GAS u64*)(ws + WS_F4 + 0x10000 + (p * 16 + j) * 256), tag, __ATOMIC_RELAXED, __HIP_MEMORY_SCOPE_AGENT);
            }
            SBAR();
            ISSUE_WD(1, L_Y);
            WAITV(0);
            BAR();
            if (lw == 0) { poll_flags3(ws, WS_F4 + 0x10000 + pP * 16 * 256, tag, lane); lsig(lb, 14); }
            ISSUE_WD(2, L_X);
            WAITV(0);
            BAR();
        } else {
            ISSUE_WD(0, L_Y);
            SBAR();
            ISSUE_WD(1, L_X);
            WAITV(0);
            BAR();
            SBAR();
            BAR();
            if (lw == 0) { poll_flags3(ws, WS_F4 + 0x10000 + pP * 16 * 256, tag, lane); lsig(lb, 14); }
            ISSUE_WD(2, L_Y);
            WAITV(0);
            BAR();
        }
        BAR();
    } else {
        if (PRIO_C) __builtin_amdgcn_s_setprio(PRIO_C);
        const unsigned vo = (32 * T2 + t) * 1024 + (C0 + 8 * h2) * 2;
        u32x4 xr0 = __builtin_amdgcn_raw_buffer_load_b128(rsX, vo, 0, 0);
        u32x4 xr1 = __builtin_amdgcn_raw_buffer_load_b128(rsX, vo + 32, 0, 0);
        PIN();
        STAMP(11);
        if (w == 0) poll_flags<PD2>(ws, WS_F2 + pP * 16 * 256, 16, tag, lane);
        BAR();
        STAMP(12);
        if (b == 0 && w == 1 && lane == 0) __hip_atomic_store((GAS u64*)ws, tag, __ATOMIC_RELAXED, __HIP_MEMORY_SCOPE_AGENT);
        {
            u32x4 hres0, hres1;
            u32x4 of[16];
#pragma unroll
            for (int i = 0; i < 8; ++i) of[i] = ld16(rsW, lane * 16, WS_O + (T2 * 32 + 16 * kh + ((i + r2) & 15)) * 1024);
            PIN();
            f32x16 keep, snd = {};
            { auto r = __builtin_amdgcn_permlane32_swap(xr0[0], xr0[2], false, false); xr0[0] = r[0]; xr0[2] = r[1];
              r = __builtin_amdgcn_permlane32_swap(xr0[1], xr0[3], false, false); xr0[1] = r[0]; xr0[3] = r[1];
              r = __builtin_amdgcn_permlane32_swap(xr1[0], xr1[2], false, false); xr1[0] = r[0]; xr1[2] = r[1];
              r = __builtin_amdgcn_permlane32_swap(xr1[1], xr1[3], false, false); xr1[1] = r[0]; xr1[3] = r[1]; }
#pragma unroll
            for (int e = 0; e < 4; ++e) { keep[2 * e] = blo(xr0[e]); keep[2 * e + 1] = bhi(xr0[e]); keep[8 + 2 * e] = blo(xr1[e]); keep[8 + 2 * e + 1] = bhi(xr1[e]); }
            asm volatile("" : "+v"(keep));
            gemm2<16, 4096, 1024, 0>(lds, L_X + kh * 2048 + (t128 ^ (64 * kh)), L_X + kh * 2048 + (t128 ^ (64 * (kh ^ 1))), of, keep, snd, [&](int g) {
                if (g < 4) {
#pragma unroll
                    for (int e = 0; e < 2; ++e) of[8 + 2 * g + e] = ld16(rsW, lane * 16, WS_O + (T2 * 32 + 16 * kh + ((8 + 2 * g + e + r2) & 15)) * 1024);
                    SB();
                } });
#pragma unroll
            for (int g = 0; g < 4; ++g) *(LAS f32x4*)(lds + L_XCH + w * 4096 + g * 1024 + lane * 16) = (f32x4){snd[4 * g], snd[4 * g + 1], snd[4 * g + 2], snd[4 * g + 3]};
            STAMP(13);
            BAR();
#pragma unroll
            for (int g = 0; g < 4; ++g) {
                const f32x4 v = *(LAS f32x4*)(lds + L_XCH + (w ^ 2) * 4096 + g * 1024 + lane * 16);
#pragma unroll
                for (int e = 0; e < 4; ++e) keep[4 * g + e] += v[e];
            }
#pragma unroll
            for (int e = 0; e < 4; ++e) { hres0[e] = pack2(keep[2 * e], keep[2 * e + 1]); hres1[e] = pack2(keep[8 + 2 * e], keep[8 + 2 * e + 1]); }
            const unsigned hb = WS_H + (T2 * 32 + 4 * c + 2 * kh) * 1024;
            st16(rsW, hres0, lane * 16, hb);
            st16(rsW, hres1, lane * 16, hb + 1024);
            *(LAS u32x4*)(lds + L_HRES + w * 2048 + lane * 16) = hres0;
            *(LAS u32x4*)(lds + L_HRES + w * 2048 + 1024 + lane * 16) = hres1;
            f32x2 s1 = {0.f, 0.f}, s2 = {0.f, 0.f};
#pragma unroll
            for (int e = 0; e < 8; ++e) { const f32x2 v = {keep[2 * e], keep[2 * e + 1]}; s1 += v; s2 += v * v; }
            float su = s1[0] + s1[1], sq = s2[0] + s2[1];
            su = halves_add(su); sq = halves_add(sq);
            __builtin_amdgcn_raw_buffer_store_b32(asu(h2 ? sq : su), rsW, lane * 4 + WS_HST + (T2 * 16 + 2 * c + kh) * 256, 0, 16);
            SBAR();
            WAITV(0);
            BAR();
            if (tid == 0) __hip_atomic_store((GAS u64*)(ws + WS_F3 + (pP * 16 + jP) * 256), tag, __ATOMIC_RELAXED, __HIP_MEMORY_SCOPE_AGENT);
            SBAR();
        }
        STAMP(14);
        if (w == 0) poll_flags<PD3>(ws, WS_F3 + p * 16 * 256, 16, tag, lane);
        BAR();
        STAMP(15);
        {
            const int T = 4 * p + w;
            u32x4 hf[32];
            float sv[16];
#pragma unroll
            for (int s = 0; s < 8; ++s) hf[s] = ld16(rsW, lane * 16, WS_H + (T * 32 + ((s + r3) & 31)) * 1024);
            PIN();
            float mean, r2;
            auto act_out = [&](const f32x16& ag, const f32x16& au, int cs, unsigned ks) {
                float y[16];
#pragma unroll
                for (int g = 0; g < 4; ++g) {
                    const f32x4 cg = *(LAS f32x4*)(lds + L_CS3 + (cs * 64 + 8 * g + 4 * h2) * 4), cu = *(LAS f32x4*)(lds + L_CS3 + (cs * 64 + 32 + 8 * g + 4 * h2) * 4);
#pragma unroll
                    for (int e = 0; e < 4; ++e) y[4 * g + e] = (__builtin_fmaf(-mean, cg[e], ag[4 * g + e]) * r2) * __builtin_fmaf(-mean, cu[e], au[4 * g + e]);
                }
                const unsigned ab = WS_ACT + (T * 86 + ks) * 1024;
                st16(rsW, (u32x4){pack2(y[0], y[1]), pack2(y[2], y[3]), pack2(y[4], y[5]), pack2(y[6], y[7])}, lane * 16, ab);
                st16(rsW, (u32x4){pack2(y[8], y[9]), pack2(y[10], y[11]), pack2(y[12], y[13]), pack2(y[14], y[15])}, lane * 16, ab + 1024);
            };
            {
                f32x16 g0 = {}, g1 = {}, u0 = {}, u1 = {};
                gemm4<0>(lds, L_Y + t128, L_Y + (t128 ^ 64), hf, g0, g1, u0, u1, [&](int i) {
                    if (i < 8) hf[8 + i] = ld16(rsW, lane * 16, WS_H + (T * 32 + ((8 + i + r3) & 31)) * 1024);
                    else { hf[2 * i] = ld16(rsW, lane * 16, WS_H + (T * 32 + ((2 * i + r3) & 31)) * 1024); hf[2 * i + 1] = ld16(rsW, lane * 16, WS_H + (T * 32 + ((2 * i + 1 + r3) & 31)) * 1024); }
                    if (i < 4) {
#pragma unroll
                        for (int k = 0; k < 4; ++k) sv[4 * i + k] = asf(ld4(rsW, lane * 4, WS_HST + (T * 16 + 4 * i + k) * 256));
                    }
                    if (i == 9) {
                        float tot = 0.f;
#pragma unroll
                        for (int k = 0; k < 16; ++k) tot += sv[k];
                        const u32x2 r = both_halves(tot);
                        mean = asf(r[0]) * (1.0f / 512.0f);
                        const float var = fmaxf(asf(r[1]) * (1.0f / 512.0f) - mean * mean, 0.f);
                        r2 = __builtin_amdgcn_rcpf(var + 1e-5f);
                        asm volatile("" : "+v"(mean), "+v"(r2));
                    }
                    SB(); });
                BAR();
                STAMP(16);
                gemm4<16>(lds, L_X + t128, L_X + (t128 ^ 64), hf, g0, g1, u0, u1, nohook);
                STAMP(17);
                act_out(g0, u0, 0, 4 * j);
                act_out(g1, u1, 1, 4 * j + 2);
                SBAR();
            }
            if (npass == 3) {
                f32x16 ag = {}, au = {};
                gemm2<32, 2048, 512, 0>(lds, L_Y + trp, L_Y + 1024 + trp, hf, ag, au, [&](int g) {
                    if (g == 3) { WAITV(0); arrive_publish(lds, 3, 4, ws, WS_F4 + (p * 16 + j) * 256, tag, lane); PIN(); } });
                BAR();
                STAMP(18);
                act_out(ag, au, 2, 2 * u3);
            } else {
                WAITV(0); arrive_publish(lds, 3, 4, ws, WS_F4 + (p * 16 + j) * 256, tag, lane);
            }
        }
        STAMP(19);
        {
            f32x16 a0, a1 = {};
            {
                const u32x4 hres0 = *(LAS u32x4*)(lds + L_HRES + w * 2048 + lane * 16), hres1 = *(LAS u32x4*)(lds + L_HRES + w * 2048 + 1024 + lane * 16);
#pragma unroll
                for (int e = 0; e < 4; ++e) { a0[2 * e] = blo(hres0[e]); a0[2 * e + 1] = bhi(hres0[e]); a0[8 + 2 * e] = blo(hres1[e]); a0[8 + 2 * e + 1] = bhi(hres1[e]); }
                asm volatile("" : "+v"(a0));
            }
            const unsigned tk = kh * 2048 + (t128 ^ (64 * kh)), ts = kh * 2048 + (t128 ^ (64 * (kh ^ 1)));
            const unsigned h0 = npass == 3 ? L_X : L_Y, h1 = npass == 3 ? L_Y : L_X;
            if (w == 0) poll_flags(ws, WS_F4 + pP * 16 * 256, 16, tag, lane);
            BAR();
            STAMP(20);
            u32x4 af[43];
            auto ld_act = [&](int i) {
                const int ks = i < 32 ? 32 * (i >> 4) + kh * 16 + ((i + r2) & 15) : 64 + kh * 11 + (i - 32 + r4 >= 11 ? i - 32 + r4 - 11 : i - 32 + r4);
                af[i] = ld16(rsW, lane * 16, WS_ACT + (T2 * 86 + ks) * 1024); };
            if (npass == 3) { WAITV(0); lsig(lb, 11); }
#pragma unroll
            for (int i = 0; i < 8; ++i) ld_act(i);
            SBAR();
            gemm2<16, 4096, 1024, 0>(lds, h0 + tk, h0 + ts, af, a0, a1, [&](int g) { ld_act(8 + 2 * g); ld_act(9 + 2 * g); SB(); });
            STAMP(21);
            BAR();
            STAMP(22);
            gemm2<16, 4096, 1024, 16>(lds, h1 + tk, h1 + ts, af, a0, a1, [&](int g) {
                if (g == 4) lwait(lds, 14, 1);
                ld_act(24 + 2 * g); ld_act(25 + 2 * g); SB(); });
            BAR();
            STAMP(23);
            gemm2<11, 4096, 1024, 32>(lds, h0 + tk, h0 + ts, af, a0, a1, [&](int g) { if (g < 2) { ld_act(40 + 2 * g < 43 ? 40 + 2 * g : 42); if (g == 0) ld_act(41); SB(); } });
#pragma unroll
            for (int g = 0; g < 4; ++g) *(LAS f32x4*)(lds + L_XCH + w * 4096 + g * 1024 + lane * 16) = (f32x4){a1[4 * g], a1[4 * g + 1], a1[4 * g + 2], a1[4 * g + 3]};
            BAR();
            STAMP(24);
#pragma unroll
            for (int g = 0; g < 4; ++g) {
                const f32x4 v = *(LAS f32x4*)(lds + L_XCH + (w ^ 2) * 4096 + g * 1024 + lane * 16);
#pragma unroll
                for (int e = 0; e < 4; ++e) a0[4 * g + e] += v[e];
            }
            u32x4 o0, o1;
#pragma unroll
            for (int e = 0; e < 4; ++e) { o0[e] = pack2(a0[2 * e], a0[2 * e + 1]); o1[e] = pack2(a0[8 + 2 * e], a0[8 + 2 * e + 1]); }
            { auto r = __builtin_amdgcn_permlane32_swap(o0[0], o0[2], false, false); o0[0] = r[0]; o0[2] = r[1];
              r = __builtin_amdgcn_permlane32_swap(o0[1], o0[3], false, false); o0[1] = r[0]; o0[3] = r[1];
              r = __builtin_amdgcn_permlane32_swap(o1[0], o1[2], false, false); o1[0] = r[0]; o1[2] = r[1];
              r = __builtin_amdgcn_permlane32_swap(o1[1], o1[3], false, false); o1[1] = r[0]; o1[3] = r[1]; }
            const rsrc_t rsOut = __builtin_amdgcn_make_buffer_rsrc((void*)out, 0, 2048 * 512 * 2, 0x00020000);
            int tz = threadIdx.x; asm volatile("" : "+v"(tz));
            const unsigned vz = (32 * T2 + (tz & 31)) * 1024 + (C0 + 8 * ((tz >> 5) & 1)) * 2;
            __builtin_amdgcn_raw_buffer_store_b128(o0, rsOut, vz, 0, 0);
            __builtin_amdgcn_raw_buffer_store_b128(o1, rsOut, vz + 32, 0, 0);
        }
        STAMP(25); CSTAMP(31);
    }
}

extern "C" void kernel_launch(void* const* d_in, const int* in_sizes, int n_in, void* d_out, int out_size, void* d_ws, size_t ws_size, hipStream_t stream) {
    (void)in_sizes; (void)n_in; (void)out_size; (void)ws_size;
    (void)hipFuncSetAttribute((const void*)hedgehog_block, hipFuncAttributeMaxDynamicSharedMemorySize, LDS_BYTES);
    hedgehog_block<<<256, 512, LDS_BYTES, stream>>>((const bf16_t*)d_in[0], (const bf16_t*)d_in[1], (const bf16_t*)d_in[2], (const bf16_t*)d_in[3],
                                                    (const bf16_t*)d_in[5], (const bf16_t*)d_in[6], (const bf16_t*)d_in[7], (bf16_t*)d_out, (char*)d_ws);
}
```

```cpp
#include <hip/hip_runtime.h>
#include <hip/hip_bf16.h>

#define LAS __attribute__((address_space(3)))
#define GAS __attribute__((address_space(1)))
#define DI __device__ __forceinline__
typedef unsigned short bf16_t;
typedef unsigned u32x4 __attribute__((ext_vector_type(4)));
typedef unsigned u32x2 __attribute__((ext_vector_type(2)));
typedef short s16x4 __attribute__((ext_vector_type(4)));
typedef __bf16 bf16x8 __attribute__((ext_vector_type(8)));
typedef __bf16 bf16x2 __attribute__((ext_vector_type(2)));
typedef float f32x16 __attribute__((ext_vector_type(16)));
typedef float f32x4 __attribute__((ext_vector_type(4)));
typedef float f32x2 __attribute__((ext_vector_type(2)));
typedef unsigned long long u64;
typedef __amdgpu_buffer_rsrc_t rsrc_t;

constexpr unsigned WS_F1 = 0x10000, WS_F2 = 0x20000, WS_F3 = 0x30000, WS_F4 = 0x40000;
constexpr unsigned WS_KVP = 0x80000, KVP_BYTES = 2176;
constexpr unsigned WS_HST = 0x110000;
constexpr unsigned WS_XST = 0x160000;
constexpr unsigned WS_O = 0x200000, WS_H = 0x400000, WS_ACT = 0x600000, WS_PROF = 0xC00000, WS_BYTES = 0x1000000;
constexpr unsigned L_X = 0, L_Y = 65536, L_XCH = 131072, L_MISC = 147456;
constexpr unsigned L_STAT = L_MISC, L_CS1 = L_MISC + 1024, L_CSQ = L_MISC + 1280, L_WPHI = L_MISC + 1536, L_KVA = L_MISC + 2560, L_CS3 = L_MISC + 4736, L_CNT = L_MISC + 5504, L_TAG = L_MISC + 6016, L_DUMMY = L_MISC + 6144, L_HRES = L_MISC + 8192;
constexpr unsigned L_WQ = 65536, L_PHI = 98304, L_VIMG = 114688  , SS_BYTES = 24576;
constexpr unsigned LDS_BYTES = 163840;
constexpr unsigned SPIN_CAP = 40000;
constexpr float LOG2E = 1.4426950408889634f;

DI float asf(unsigned v) { return __builtin_bit_cast(float, v); }
DI unsigned asu(float v) { return __builtin_bit_cast(unsigned, v); }
DI unsigned pack2(float lo, float hi) { bf16x2 v = {(__bf16)lo, (__bf16)hi}; return __builtin_bit_cast(unsigned, v); }
DI float blo(unsigned v) { return __builtin_bit_cast(float, v << 16); }
DI float bhi(unsigned v) { return __builtin_bit_cast(float, v & 0xffff0000u); }
DI f32x16 mfma(u32x4 a, u32x4 b, f32x16 c) { return __builtin_amdgcn_mfma_f32_32x32x16_bf16(__builtin_bit_cast(bf16x8, a), __builtin_bit_cast(bf16x8, b), c, 0, 0, 0); }
DI f32x4 mfma16(u32x4 a, u32x4 b, f32x4 c) { return __builtin_amdgcn_mfma_f32_16x16x32_bf16(__builtin_bit_cast(bf16x8, a), __builtin_bit_cast(bf16x8, b), c, 0, 0, 0); }
DI u32x4 tr2(LAS char* lds, unsigned off, unsigned second) {
    const s16x4 lo = __builtin_amdgcn_ds_read_tr16_b64_v4i16((LAS s16x4*)(lds + off));
    const s16x4 hi = __builtin_amdgcn_ds_read_tr16_b64_v4i16((LAS s16x4*)(lds + off + second));
    return __builtin_bit_cast(u32x4, __builtin_shufflevector(lo, hi, 0, 1, 2, 3, 4, 5, 6, 7));
}
DI void dma16(const rsrc_t rs, unsigned ldsaddr, unsigned voff, unsigned soff) {
    asm volatile("s_mov_b32 m0, %0\n\ts_nop 0\n\tbuffer_load_dwordx4 %1, %2, %3 offen lds" ::"s"(ldsaddr), "v"(voff), "s"(rs), "s"(soff) : "memory");
}
constexpr unsigned BIAS = 4096;
DI rsrc_t biased_rsrc(const void* p, unsigned bytes) { return __builtin_amdgcn_make_buffer_rsrc((void*)((const char*)p - BIAS), 0, bytes + BIAS, 0x00020000); }
DI void dma4(unsigned ldsaddr, const rsrc_t ra, const rsrc_t rb, const rsrc_t rc, const rsrc_t rd, unsigned va, unsigned vb, unsigned s0, unsigned s1, unsigned s2, unsigned s3) {
    asm volatile("s_mov_b32 m0, %0\n\ts_nop 0\n\t"
                 "buffer_load_dwordx4 %5, %1, %7 offen lds\n\tbuffer_load_dwordx4 %6, %2, %8 offen offset:1024 lds\n\t"
                 "buffer_load_dwordx4 %5, %3, %9 offen offset:2048 lds\n\tbuffer_load_dwordx4 %6, %4, %10 offen offset:3072 lds"
                 ::"s"(ldsaddr), "s"(ra), "s"(rb), "s"(rc), "s"(rd), "v"(va), "v"(vb), "s"(s0 + BIAS), "s"(s1 + BIAS - 1024), "s"(s2 + BIAS - 2048), "s"(s3 + BIAS - 3072) : "memory");
}
DI void dma2(unsigned ldsaddr, const rsrc_t r, unsigned va, unsigned vb, unsigned s0, unsigned s1) {
    asm volatile("s_mov_b32 m0, %0\n\ts_nop 0\n\tbuffer_load_dwordx4 %2, %1, %4 offen lds\n\tbuffer_load_dwordx4 %3, %1, %5 offen offset:1024 lds"
                 ::"s"(ldsaddr), "s"(r), "v"(va), "v"(vb), "s"(s0 + BIAS), "s"(s1 + BIAS - 1024) : "memory");
}
#define WAITV(n) asm volatile("s_waitcnt vmcnt(" #n ")" ::: "memory")
#define PIN() do { asm volatile("" ::: "memory"); __builtin_amdgcn_sched_barrier(0); } while (0)
template <int N> DI void waitv() { asm volatile("s_waitcnt vmcnt(%0)" ::"n"(N) : "memory"); }
#define B_EARLY 1
#define PD1 0
#define PD2 4
#define PD3 4
#define PRIO_C 0
#define K0 3
#define R1MODE 2
#define STAMP2_SS(ss) do { if (ss == 0) STAMP2(0); if (ss == 1) STAMP2(1); if (ss == 2) STAMP2(2); if (ss == 3) STAMP2(3); if (ss == 4) STAMP2(4); if (ss == 5) STAMP2(5); if (ss == 6) STAMP2(6); if (ss == 7) STAMP2(7); } while (0)
#define SBAR() asm volatile("s_barrier" ::: "memory")
#define BAR() asm volatile("s_waitcnt lgkmcnt(0)\n\ts_barrier" ::: "memory")
DI u32x2 both_halves(float v) { const unsigned a = asu(v); const auto r = __builtin_amdgcn_permlane32_swap(a, a, false, false); return (u32x2){r[0], r[1]}; }
DI float halves_add(float v) { const u32x2 r = both_halves(v); return asf(r[0]) + asf(r[1]); }
DI float halves_max(float v) { const u32x2 r = both_halves(v); return fmaxf(asf(r[0]), asf(r[1])); }
DI float halves_min(float v) { const u32x2 r = both_halves(v); return fminf(asf(r[0]), asf(r[1])); }
DI float dpp_ror4(float v) { return asf(__builtin_amdgcn_update_dpp(0, (int)asu(v), 0x124, 0xf, 0xf, true)); }
DI float dpp_ror8(float v) { return asf(__builtin_amdgcn_update_dpp(0, (int)asu(v), 0x128, 0xf, 0xf, true)); }
DI float rows16_add(float v) { const unsigned a = asu(v); const auto r = __builtin_amdgcn_permlane16_swap(a, a, false, false); return asf(r[0]) + asf(r[1]); }
DI float stride4_add(float v) { v += dpp_ror4(v); v += dpp_ror8(v); v = rows16_add(v); return halves_add(v); }
DI float quad_add(float v) {
    v += __builtin_bit_cast(float, __builtin_amdgcn_update_dpp(0, __builtin_bit_cast(int, v), 0xB1, 0xf, 0xf, true));
    v += __builtin_bit_cast(float, __builtin_amdgcn_update_dpp(0, __builtin_bit_cast(int, v), 0x4E, 0xf, 0xf, true));
    return v;
}
DI void st16(const rsrc_t rs, u32x4 v, unsigned voff, unsigned soff) { __builtin_amdgcn_raw_buffer_store_b128(v, rs, voff + soff, 0, 16); }
DI u32x4 ld16(const rsrc_t rs, unsigned voff, unsigned soff) { return __builtin_amdgcn_raw_buffer_load_b128(rs, voff, soff, 16); }
DI unsigned ld4(const rsrc_t rs, unsigned voff, unsigned soff) { return __builtin_amdgcn_raw_buffer_load_b32(rs, voff, soff, 16); }

DI void poll_flags3(const char* ws, unsigned off, u64 tag, int lane) {
    const int l = lane < 11 ? lane : 10;
    const GAS u64* f = (const GAS u64*)(ws + off) + (l < 6 ? l : l + 2) * 32;
#pragma unroll 1
    for (unsigned spin = 0; spin < SPIN_CAP; ++spin) {
        const u64 v = __hip_atomic_load(f, __ATOMIC_RELAXED, __HIP_MEMORY_SCOPE_AGENT);
        if (__all(v == tag)) break;
        __builtin_amdgcn_s_sleep(1);
    }
    asm volatile("" ::: "memory");
}
DI void nohook(int) {}
template <int DELAY = 0>
DI void poll_flags(const char* ws, unsigned off, int n, u64 tag, int lane) {
#pragma unroll
    for (int i = 0; i < DELAY; ++i) __builtin_amdgcn_s_sleep(2);
    const GAS u64* f = (const GAS u64*)(ws + off) + (lane < n ? lane : n - 1) * 32;
#pragma unroll 1
    for (unsigned spin = 0; spin < SPIN_CAP; ++spin) {
        const u64 v = __hip_atomic_load(f, __ATOMIC_RELAXED, __HIP_MEMORY_SCOPE_AGENT);
        if (__all(v == tag)) break;
        __builtin_amdgcn_s_sleep(1);
    }
    asm volatile("" ::: "memory");
}
DI void arrive_publish(LAS char* lds, int cnt, unsigned n, char* ws, unsigned flag_off, u64 tag, int lane) {
    if (lane == 0) {
        const unsigned old = __hip_atomic_fetch_add((LAS unsigned*)(lds + L_CNT) + cnt, 1u, __ATOMIC_RELAXED, __HIP_MEMORY_SCOPE_WORKGROUP);
        if (old == n - 1) __hip_atomic_store((GAS u64*)(ws + flag_off), tag, __ATOMIC_RELAXED, __HIP_MEMORY_SCOPE_AGENT);
    }
}
DI void lsig(unsigned lb, int idx) {
    const unsigned a = lb + L_CNT + idx * 4, one = 1;
    asm volatile("s_mov_b64 exec, 1\n\tds_add_u32 %0, %1\n\ts_mov_b64 exec, -1" ::"v"(a), "v"(one) : "memory");
}
DI void lwait(LAS char* lds, int idx, unsigned n) {
    const volatile LAS unsigned* c = (const volatile LAS unsigned*)(lds + L_CNT) + idx;
#pragma unroll 1
    for (unsigned spin = 0; spin < 64 * SPIN_CAP; ++spin) {
        if ((unsigned)__builtin_amdgcn_readfirstlane(*c) >= n) break;
        __builtin_amdgcn_s_sleep(1);
    }
    asm volatile("" ::: "memory");
}
DI void dual_softmax(const f32x16& p, float sl, u32x4& pos, u32x4& neg) {
    float mx = fmaxf(fmaxf(fmaxf(p[0], p[1]), fmaxf(p[2], p[3])), fmaxf(fmaxf(p[4], p[5]), fmaxf(p[6], p[7])));
    float mn = fminf(fminf(fminf(p[0], p[1]), fminf(p[2], p[3])), fminf(fminf(p[4], p[5]), fminf(p[6], p[7])));
    mx = halves_max(mx); mn = halves_min(mn);
    const float cx = -mx * sl, cn = mn * sl;
    f32x2 ep[4], en[4];
#pragma unroll
    for (int i = 0; i < 4; ++i) {
        const f32x2 v = {p[2 * i], p[2 * i + 1]};
        const f32x2 u = v * sl + cx, d = v * (-sl) + cn;
        ep[i] = (f32x2){__builtin_amdgcn_exp2f(u[0]), __builtin_amdgcn_exp2f(u[1])};
        en[i] = (f32x2){__builtin_amdgcn_exp2f(d[0]), __builtin_amdgcn_exp2f(d[1])};
    }
    const f32x2 sp = (ep[0] + ep[1]) + (ep[2] + ep[3]), sn = (en[0] + en[1]) + (en[2] + en[3]);
    const float rp = __builtin_amdgcn_rcpf(halves_add(sp[0] + sp[1])), rn = __builtin_amdgcn_rcpf(halves_add(sn[0] + sn[1]));
#pragma unroll
    for (int i = 0; i < 4; ++i) { const f32x2 a = ep[i] * rp, b = en[i] * rn; pos[i] = pack2(a[0], a[1]); neg[i] = pack2(b[0], b[1]); }
}

#define SB() __builtin_amdgcn_sched_barrier(0)
template <int N, int STRIDE, int SECOND, int OFF, int NA, class Hook>
DI void gemm2(LAS char* lds, unsigned b0, unsigned b1, const u32x4 (&act)[NA], f32x16& acc0, f32x16& acc1, Hook hook) {
    u32x4 fb[2][4];
    asm volatile("" : "+v"(b0), "+v"(b1));
    constexpr int NG = (N + 1) / 2;
#pragma unroll
    for (int e = 0; e < 2; ++e) { fb[0][2 * e] = tr2(lds, b0 + e * STRIDE, SECOND); fb[0][2 * e + 1] = tr2(lds, b1 + e * STRIDE, SECOND); }
    SB();
#pragma unroll
    for (int g = 0; g < NG; ++g) {
#pragma unroll
        for (int e = 0; e < 2; ++e) {
            const int i = 2 * g + e, n = i + 2;
            if (i < N) acc0 = mfma(fb[g & 1][2 * e], act[OFF + i], acc0);
            if (n < N) fb[(g + 1) & 1][2 * e] = tr2(lds, b0 + n * STRIDE, SECOND);
            SB();
            if (i < N) acc1 = mfma(fb[g & 1][2 * e + 1], act[OFF + i], acc1);
            if (n < N) fb[(g + 1) & 1][2 * e + 1] = tr2(lds, b1 + n * STRIDE, SECOND);
            SB();
        }
        hook(g);
    }
    asm volatile("" : "+v"(acc0), "+v"(acc1));
}
template <int OFF, class Hook>
DI void gemm4(LAS char* lds, unsigned b0, unsigned b1, const u32x4 (&act)[32], f32x16& g0, f32x16& g1, f32x16& u0, f32x16& u1, Hook hook) {
    u32x4 fb[2][4];
    asm volatile("" : "+v"(b0), "+v"(b1));
    fb[0][0] = tr2(lds, b0, 1024); fb[0][1] = tr2(lds, b1, 1024); fb[0][2] = tr2(lds, b0 + 2048, 1024); fb[0][3] = tr2(lds, b1 + 2048, 1024);
    SB();
#pragma unroll
    for (int i = 0; i < 16; ++i) {
        const int o = (i + 1) * 4096;
        g0 = mfma(fb[i & 1][0], act[OFF + i], g0);
        if (i < 15) fb[(i + 1) & 1][0] = tr2(lds, b0 + o, 1024);
        SB();
        g1 = mfma(fb[i & 1][1], act[OFF + i], g1);
        if (i < 15) fb[(i + 1) & 1][1] = tr2(lds, b1 + o, 1024);
        SB();
        u0 = mfma(fb[i & 1][2], act[OFF + i], u0);
        if (i < 15) fb[(i + 1) & 1][2] = tr2(lds, b0 + o + 2048, 1024);
        SB();
        u1 = mfma(fb[i & 1][3], act[OFF + i], u1);
        if (i < 15) fb[(i + 1) & 1][3] = tr2(lds, b1 + o + 2048, 1024);
        SB();
        hook(i);
    }
    asm volatile("" : "+v"(g0), "+v"(g1), "+v"(u0), "+v"(u1));
}
DI void touch_lines(const rsrc_t rs, unsigned ldsaddr, unsigned voff, unsigned soff) {
    asm volatile("s_mov_b32 m0, %0\n\ts_nop 0\n\tbuffer_load_dword %1, %2, %3 offen lds" ::"s"(ldsaddr), "v"(voff), "s"(rs), "s"(soff) : "memory");
}

#define STAMP(i) do {} while (0)
#define CSTAMP(i) do {} while (0)
#define STAMP2(i) do {} while (0)

__global__ void __launch_bounds__(512, 2)
hedgehog_block(const bf16_t* __restrict__ x, const bf16_t* __restrict__ wqkv, const bf16_t* __restrict__ wphi, const bf16_t* __restrict__ wo,
               const bf16_t* __restrict__ wg, const bf16_t* __restrict__ wu, const bf16_t* __restrict__ wd, bf16_t* __restrict__ out, char* __restrict__ ws) {
    extern __shared__ __attribute__((aligned(16))) char smem[];
    LAS char* lds = (LAS char*)smem;
    const unsigned lb = (unsigned)(size_t)lds;
    const int tid = threadIdx.x, lane = tid & 63;
    const int w = __builtin_amdgcn_readfirstlane(tid >> 6);
    const int b = blockIdx.x, xcd = b & 7, slot = b >> 3;
    const int p = 4 * (xcd >> 1) + (slot >> 3), j = 8 * (xcd & 1) + (slot & 7);
    const int pP = 2 * xcd + (slot >> 4), jP = slot & 15, c = slot & 7, half = (slot >> 3) & 1;
    const bool has3 = (j & 7) < (j < 8 ? 6 : 5);
    const int u3 = 32 + (j < 8 ? j : j - 2);
    const int t = lane & 31, h2 = lane >> 5, q = (lane & 15) >> 2, pp = lane & 3, blk = (lane >> 4) & 1;
    const int m = w & 3, a = w >> 2, lw = w & 3;
    const int q4 = slot >> 3;
    const int r1 = R1MODE == 0 ? 0 : R1MODE == 1 ? ((c + 2 * q4) & 7) : R1MODE == 2 ? 4 * (q4 & 1) : 2 * q4, r2 = (2 * c + 4 * q4) & 15, r3 = (4 * c + 8 * q4) & 31, r4 = (c + 3 * q4) % 11;
    STAMP(0); CSTAMP(30);
    const rsrc_t rsX = __builtin_amdgcn_make_buffer_rsrc((void*)x, 0, 2048 * 512 * 2, 0x00020000);
    const rsrc_t rsQ = __builtin_amdgcn_make_buffer_rsrc((void*)wqkv, 0, 512 * 1536 * 2, 0x00020000);
    const rsrc_t rsW = __builtin_amdgcn_make_buffer_rsrc((void*)ws, 0, WS_BYTES, 0x00020000);
    u64 tag = 0;
#define UNIFORM64(v) (((u64)(unsigned)__builtin_amdgcn_readfirstlane((unsigned)((v) >> 32)) << 32) | (unsigned)__builtin_amdgcn_readfirstlane((unsigned)(v)))
    const u32x4 ones = {0x3F803F80u, 0x3F803F80u, 0x3F803F80u, 0x3F803F80u};
    const unsigned c128 = ((lane & 7) ^ (4 * ((lane >> 4) & 1))) * 16;
    const unsigned vrow = (lane >> 3) * 1024 + c128;
    const unsigned trn = (8 * h2 + q) * 64 + pp * 8;

    const rsrc_t rsG = biased_rsrc(wg, 512 * 1376 * 2), rsU = biased_rsrc(wu, 512 * 1376 * 2);
    const unsigned vgu = (lane >> 2) * 2752 + (lane & 3) * 16, vgu8 = (lane >> 3) * 2752 + c128;
#define ISSUE_STAGE(st, halfbase, g0, g1) do { _Pragma("unroll") for (int g_ = (g0); g_ < (g1); ++g_) { const int i_ = 4 * g_ + lw; const unsigned so_ = 16 * ((16 * (st) + i_ + r3) & 31) * 2752 + 128 * j; \
            dma4(lb + (halfbase) + i_ * 4096, rsG, rsG, rsU, rsU, vgu8, vgu8, so_, so_ + 8 * 2752, so_, so_ + 8 * 2752); } } while (0)
#define ISSUE_UNIT(u, halfbase) do { _Pragma("unroll") for (int g_ = 0; g_ < 4; ++g_) { const int s_ = 2 * (4 * g_ + lw); \
            const unsigned sa_ = 16 * ((s_ + r3) & 31) * 2752 + 64 * (u), sb_ = 16 * ((s_ + 1 + r3) & 31) * 2752 + 64 * (u); \
            dma4(lb + (halfbase) + s_ * 2048, rsG, rsU, rsG, rsU, vgu, vgu, sa_, sa_, sb_, sb_); } } while (0)
    if (w == 3) {
        u64 pc; asm volatile("s_getpc_b64 %0" : "=s"(pc));
        const rsrc_t rsC = __builtin_amdgcn_make_buffer_rsrc((void*)(pc & ~127ull), 0, 36864, 0x00020000);
        touch_lines(rsC, lb + L_DUMMY, (slot * 9 + (lane < 9 ? lane : 0)) * 128, 0);
    }
    u32x4 xf[32];
    float yk[16];
    float mean1, rstd1;
    if (w < 4) {
        {
            u64 epoch = __hip_atomic_load((const GAS u64*)ws, __ATOMIC_RELAXED, __HIP_MEMORY_SCOPE_AGENT);
            const unsigned R = 128 * p + 8 * j + 2 * w;
            const u32x4 v0 = __builtin_amdgcn_raw_buffer_load_b128(rsX, lane * 16, R * 1024, 0), v1 = __builtin_amdgcn_raw_buffer_load_b128(rsX, lane * 16, R * 1024 + 1024, 0);
            if (tid < 16) *(LAS unsigned*)(lds + L_CNT + tid * 4) = 0u;
            f32x2 a1 = {0.f, 0.f}, a2 = {0.f, 0.f}, b1 = {0.f, 0.f}, b2 = {0.f, 0.f};
#pragma unroll
            for (int e = 0; e < 4; ++e) {
                const f32x2 z0 = {blo(v0[e]), bhi(v0[e])}, z1 = {blo(v1[e]), bhi(v1[e])};
                a1 += z0; a2 += z0 * z0; b1 += z1; b2 += z1 * z1;
            }
            const float su0 = stride4_add(quad_add(a1[0] + a1[1])), sq0 = stride4_add(quad_add(a2[0] + a2[1]));
            const float su1 = stride4_add(quad_add(b1[0] + b1[1])), sq1 = stride4_add(quad_add(b2[0] + b2[1]));
            const float su = (lane & 1) ? su1 : su0, sq = (lane & 1) ? sq1 : sq0;
            const float mean = su * (1.0f / 512.0f), var = fmaxf(sq * (1.0f / 512.0f) - mean * mean, 0.f);
            const float rstd = __builtin_amdgcn_rsqf(var + 1e-5f);
            asm volatile("s_waitcnt vmcnt(0)" : "+v"(epoch)::"memory");
            tag = UNIFORM64(epoch) + 0x9E3779B97F4A7C15ull;
            if (tid == 0) *(LAS u64*)(lds + L_TAG) = tag;
            if (lane < 2) st16(rsW, (u32x4){asu(mean), asu(rstd), (unsigned)tag, (unsigned)(tag >> 32)}, (R + lane) * 16, WS_XST);
        }
        STAMP(1);
        const unsigned xa0 = (32 * m + t) * 128 + ((h2 ^ ((t >> 1) & 7)) * 16);
        const unsigned twA = 16384 + (blk ? 1024 + 32 : 0) + trn, twB = 16384 + (blk ? 1024 : 32) + trn;
        f32x16 accA = {}, accB = {};
        u32x4 fa[2][2], fb[2][2], xs;
        unsigned bA = twA, bB = twB, bx0 = xa0, bx1 = xa0 ^ 32, bx2 = xa0 ^ 64, bx3 = xa0 ^ 96;
        asm volatile("" : "+v"(bA), "+v"(bB), "+v"(bx0), "+v"(bx1), "+v"(bx2), "+v"(bx3));
#pragma unroll
        for (int ss = 0; ss <= 8; ++ss) {
            if (ss < 8) {
                BAR();
                STAMP2_SS(ss);
                if (ss == 0) STAMP(2);
                if (ss == 5) {
            if (w < 3) {
                const rsrc_t rsA = __builtin_amdgcn_make_buffer_rsrc((void*)wg, 0, 512 * 1376 * 2, 0x00020000), rsB = __builtin_amdgcn_make_buffer_rsrc((void*)wu, 0, 512 * 1376 * 2, 0x00020000);
                const rsrc_t rsCc = __builtin_amdgcn_make_buffer_rsrc((void*)wd, 0, 512 * 1376 * 2, 0x00020000), rsE = __builtin_amdgcn_make_buffer_rsrc((void*)wo, 0, 512 * 512 * 2, 0x00020000);
                const unsigned blk64 = b * 3 + w;
                if (blk64 < 172) touch_lines(rsA, lb + L_DUMMY, (blk64 * 64 + lane) * 128, 0);
                else if (blk64 < 344) touch_lines(rsB, lb + L_DUMMY, ((blk64 - 172) * 64 + lane) * 128, 0);
                else if (blk64 < 516) touch_lines(rsCc, lb + L_DUMMY, ((blk64 - 344) * 64 + lane) * 128, 0);
                else if (blk64 < 580) touch_lines(rsE, lb + L_DUMMY, ((blk64 - 516) * 64 + lane) * 128, 0);
            }
            if (w == 3) {
                unsigned o = WS_KVP + (j * 16 + p) * KVP_BYTES + (lane < 17 ? lane : 0) * 128;
                o = lane == 17 ? WS_F1 + (j * 16 + p) * 256 : o;
                o = lane == 18 ? WS_F2 + (p * 16 + j) * 256 : o;
                o = lane == 19 ? WS_F3 + (pP * 16 + jP) * 256 : o;
                o = lane == 20 ? WS_F4 + (p * 16 + j) * 256 : o;
                o = lane == 21 ? WS_F4 + 0x10000 + (p * 16 + j) * 256 : o;
                o = lane == 22 ? WS_XST + (128 * p + 8 * j) * 16 : o;
                o = lane == 23 ? WS_O + ((4 * p) * 32 + 2 * j) * 1024 : o;
                o = lane == 24 ? WS_H + ((4 * pP + 2 * half) * 32 + 4 * c) * 1024 : o;
                o = lane == 25 ? WS_ACT + ((4 * p) * 86 + 4 * j) * 1024 : o;
                o = lane == 26 ? WS_ACT + ((4 * p + 3) * 86 + 4 * j) * 1024 : o;
                o = lane == 27 ? WS_HST + ((4 * pP + 2 * half) * 16 + 2 * c) * 256 : o;
                touch_lines(rsW, lb + L_DUMMY, o, 0);
            }
                }
                if (ss == 6) { xs = ld16(rsW, (128 * p + 32 * m + t) * 16, WS_XST); SB(); }
            }
#pragma unroll
            for (int e = 0; e < 2; ++e) {
                if (ss > 0) accA = mfma(fa[1][e], xf[4 * ss - 2 + e], accA);
                if (ss < 8) { xf[4 * ss + e] = *(LAS u32x4*)(lds + (e ? bx1 : bx0)); fa[0][e] = tr2(lds, bA + e * 2048, 256); }
                SB();
                if (ss > 0) accB = mfma(fb[1][e], xf[4 * ss - 2 + e], accB);
                if (ss < 8) fb[0][e] = tr2(lds, bB + e * 2048, 256);
                SB();
            }
            if (ss < 8) {
#pragma unroll
                for (int e = 0; e < 2; ++e) {
                    accA = mfma(fa[0][e], xf[4 * ss + e], accA);
                    xf[4 * ss + 2 + e] = *(LAS u32x4*)(lds + (e ? bx3 : bx2)); fa[1][e] = tr2(lds, bA + (2 + e) * 2048, 256);
                    SB();
                    accB = mfma(fb[0][e], xf[4 * ss + e], accB);
                    fb[1][e] = tr2(lds, bB + (2 + e) * 2048, 256);
                    SB();
                }
                if (ss < 7) {
                    const unsigned d = ss == 5 ? (unsigned)(-5 * (int)SS_BYTES) : SS_BYTES;
                    bA += d; bB += d; bx0 += d; bx1 += d; bx2 += d; bx3 += d;
                    asm volatile("" : "+v"(bA), "+v"(bB), "+v"(bx0), "+v"(bx1), "+v"(bx2), "+v"(bx3));
                }
            }
        }
        STAMP(3);
#pragma unroll
        for (int g = 0; g < 4; ++g) *(LAS f32x4*)(lds + L_XCH + m * 4096 + g * 1024 + lane * 16) = (f32x4){accB[4 * g], accB[4 * g + 1], accB[4 * g + 2], accB[4 * g + 3]};
#pragma unroll 1
        for (unsigned spin = 0; spin < SPIN_CAP; ++spin) {
            if (__all(xs[2] == (unsigned)tag && xs[3] == (unsigned)(tag >> 32))) break;
            __builtin_amdgcn_s_sleep(1);
            xs = ld16(rsW, (128 * p + 32 * m + t) * 16, WS_XST);
        }
        mean1 = asf(xs[0]); rstd1 = asf(xs[1]);
        if (h2 == 0) *(LAS f32x2*)(lds + L_STAT + (32 * m + t) * 8) = (f32x2){mean1, rstd1};
        BAR();
        STAMP(4);
#pragma unroll
        for (int g = 0; g < 4; ++g) {
            const f32x4 cs = *(LAS f32x4*)(lds + L_CS1 + (8 * g + 4 * h2) * 4);
#pragma unroll
            for (int e = 0; e < 4; ++e) { const float y = __builtin_fmaf(-mean1, cs[e], accA[4 * g + e]); yk[4 * g + e] = g < 2 ? y : y * rstd1; }
        }
    } else {
        const unsigned vx0 = (lane >> 3) * 1024 + (((lane & 7) ^ (lane >> 4)) * 16), vx1 = (lane >> 3) * 1024 + (((lane & 7) ^ (4 + (lane >> 4))) * 16);
        const unsigned vwk = (lane >> 2) * 3072 + (lane & 3) * 16, vwv = (lane >> 2) * 3072 + ((lane & 3) ^ 2) * 16;
        const rsrc_t rbX = biased_rsrc(x, 2048 * 512 * 2), rbQ = biased_rsrc(wqkv, 512 * 1536 * 2);
#define ISSUE_SS(ss, v) do { const unsigned sb_ = lb + ((ss) % 6) * SS_BYTES, ks_ = ((ss) + r1) & 7; \
            const unsigned sx_ = (128 * p + 32 * (v)) * 1024 + ks_ * 128, sw_ = (16 * (v) + 64 * ks_) * 3072 + (512 + 32 * j) * 2; \
            dma2(sb_ + 16384 + (v) * 2048, rbQ, vwk, vwv, sw_, sw_ + 1024); \
            dma4(sb_ + (v) * 4096, rbX, rbX, rbX, rbX, vx0, vx1, sx_, sx_ + 8 * 1024, sx_ + 16 * 1024, sx_ + 24 * 1024); } while (0)
        if (lw == 0) {
            const rsrc_t rsP = __builtin_amdgcn_make_buffer_rsrc((void*)wphi, 0, 32 * 256 * 2, 0x00020000);
            dma16(rsP, lb + L_WPHI, lane * 16, j * 1024);
        }
#pragma unroll
        for (int ss = 0; ss < K0; ++ss) ISSUE_SS(ss, lw);
        STAMP(1);
        f32x4 csacc = {};
        const unsigned tw = 16384 + ((lw & 1) ? 1024 + ((lw >> 1) ^ 1) * 32 : (lw >> 1) * 32) + (lane >> 5) * 2048 + (8 * ((lane >> 4) & 1) + q) * 64 + pp * 8;
        constexpr int NL = 8 - K0;
#define P1WAIT(s) (6 * ((((s) + K0 - 1 < 7 ? (s) + K0 - 1 : 7) - (s))) + ((s) > 4 ? 2 * ((s) - 4) : 0))
#pragma unroll
        for (int ss = 0; ss < 8; ++ss) {
            if (ss == 0) waitv<P1WAIT(0)>();
            if (ss == 1) waitv<P1WAIT(1)>();
            if (ss == 2) waitv<P1WAIT(2)>();
            if (ss == 3) waitv<P1WAIT(3)>();
            if (ss == 4) waitv<P1WAIT(4)>();
            if (ss == 5) waitv<P1WAIT(5)>();
            if (ss == 6) waitv<P1WAIT(6)>();
            if (ss == 7) waitv<P1WAIT(7)>();
            STAMP2_SS(ss);
            BAR();
            if (ss == 0) { const u64 tv = *(LAS u64*)(lds + L_TAG); tag = UNIFORM64(tv); STAMP(2); }
            unsigned bw = (ss % 6) * SS_BYTES + tw;
            asm volatile("" : "+v"(bw));
            const u32x4 f0 = tr2(lds, bw, 256), f1 = tr2(lds, bw + 4096, 256);
            SB();
            if (ss < NL) ISSUE_SS(K0 + ss, lw);
            if (ss == 6) {
#pragma unroll
                for (int i = 0; i < 4; ++i) *(LAS u32x4*)(lds + L_VIMG + (4 * lw + i) * 1024 + lane * 16) = ones;
            }
            if (ss >= 4) {
                const int pc = 8 * lw + 2 * (ss - 4);
                const unsigned so = 16 * ((pc + 4 * r1) & 31) * 3072 + 64 * j;
                dma2(lb + L_WQ + pc * 1024, rbQ, vwk, vwk, so, so + 16 * 3072);
            }
            csacc = mfma16(f0, ones, csacc);
            csacc = mfma16(f1, ones, csacc);
            SB();
        }
        if ((lane & 15) == 0) *(LAS f32x4*)(lds + L_CS1 + (lw >> 1) * 128 + (lw & 1) * 64 + (lane >> 4) * 16) = csacc;
        STAMP(3);
        BAR();
        STAMP(4);
        const f32x2 st = *(LAS f32x2*)(lds + L_STAT + (32 * m + t) * 8);
        mean1 = st[0]; rstd1 = st[1];
#pragma unroll
        for (int g = 0; g < 4; ++g) {
            const f32x4 cs = *(LAS f32x4*)(lds + L_CS1 + 128 + (8 * g + 4 * h2) * 4);
            const f32x4 v = *(LAS f32x4*)(lds + L_XCH + m * 4096 + g * 1024 + lane * 16);
#pragma unroll
            for (int e = 0; e < 4; ++e) { const float y = __builtin_fmaf(-mean1, cs[e], v[e]); yk[4 * g + e] = g < 2 ? y : y * rstd1; }
        }
    }
    const unsigned imgA = L_PHI + a * 8192 + (32 * m + t) * 64 + ((h2 ^ ((t >> 1) & 7)) * 8);
    const u32x4 aphi = tr2(lds, L_WPHI + a * 512 + (4 * h2 + q) * 32 + pp * 8, 256);
    {
        const u32x4 kf = {pack2(yk[0], yk[1]), pack2(yk[2], yk[3]), pack2(yk[4], yk[5]), pack2(yk[6], yk[7])};
        const f32x16 zero = {};
        const f32x16 pk = mfma(aphi, kf, zero);
        *(LAS u32x2*)(lds + imgA + (L_VIMG - L_PHI)) = (u32x2){pack2(yk[8], yk[9]), pack2(yk[10], yk[11])};
        *(LAS u32x2*)(lds + (imgA ^ 16) + (L_VIMG - L_PHI)) = (u32x2){pack2(yk[12], yk[13]), pack2(yk[14], yk[15])};
        u32x4 pos, neg;
        dual_softmax(pk, rstd1 * LOG2E, pos, neg);
        *(LAS u32x2*)(lds + imgA) = (u32x2){pos[0], pos[1]};
        *(LAS u32x2*)(lds + (imgA ^ 16)) = (u32x2){pos[2], pos[3]};
        *(LAS u32x2*)(lds + (imgA ^ 32)) = (u32x2){neg[0], neg[1]};
        *(LAS u32x2*)(lds + (imgA ^ 48)) = (u32x2){neg[2], neg[3]};
    }
    if (w >= 4) waitv<0>();
    BAR();
    STAMP(5);
    float yq[8];
    if (w < 4) {
        unsigned tq = L_WQ + trn + blk * 32;
        asm volatile("" : "+v"(tq));
        f32x16 qa = {};
        u32x4 qb[2][4];
#pragma unroll
        for (int s = 0; s < 4; ++s) qb[0][s] = tr2(lds, tq + s * 1024, 256);
        SB();
#pragma unroll
        for (int s = 0; s < 32; ++s) {
            qa = mfma(qb[(s >> 2) & 1][s & 3], xf[s], qa);
            if (s < 28) qb[((s >> 2) + 1) & 1][s & 3] = tr2(lds, tq + (s + 4) * 1024, 256);
            SB();
        }
#pragma unroll
        for (int g = 0; g < 2; ++g) *(LAS f32x4*)(lds + L_XCH + m * 4096 + g * 1024 + lane * 16) = (f32x4){qa[8 + 4 * g], qa[9 + 4 * g], qa[10 + 4 * g], qa[11 + 4 * g]};
        STAMP(6);
        BAR();
#pragma unroll
        for (int g = 0; g < 2; ++g) {
            const f32x4 c0 = *(LAS f32x4*)(lds + L_CSQ + (8 * g + 4 * h2) * 4);
#pragma unroll
            for (int e = 0; e < 4; ++e) yq[4 * g + e] = __builtin_fmaf(-mean1, c0[e], qa[4 * g + e]);
        }
    } else {
        if (lw >= 2) {
            __builtin_amdgcn_s_setprio(3);
            const int ah = lw - 2;
            f32x16 kv = {};
            unsigned aa[2], ab[2];
#pragma unroll
            for (int u = 0; u < 2; ++u) {
                const unsigned R = 8 * h2 + 4 * u + q, sw = (R >> 1) & 7;
                aa[u] = L_PHI + ah * 8192 + R * 64 + (((4 * blk + pp) ^ sw) * 8);
                ab[u] = aa[u] + (L_VIMG - L_PHI);
                asm volatile("" : "+v"(aa[u]), "+v"(ab[u]));
            }
            u32x4 A[8], B[8];
#pragma unroll
            for (int s = 0; s < 8; ++s) {
                const s16x4 a0 = __builtin_amdgcn_ds_read_tr16_b64_v4i16((LAS s16x4*)(lds + aa[0] + s * 1024));
                const s16x4 a1 = __builtin_amdgcn_ds_read_tr16_b64_v4i16((LAS s16x4*)(lds + aa[1] + s * 1024));
                const s16x4 b0 = __builtin_amdgcn_ds_read_tr16_b64_v4i16((LAS s16x4*)(lds + ab[0] + s * 1024));
                const s16x4 b1 = __builtin_amdgcn_ds_read_tr16_b64_v4i16((LAS s16x4*)(lds + ab[1] + s * 1024));
                A[s] = __builtin_bit_cast(u32x4, __builtin_shufflevector(a0, a1, 0, 1, 2, 3, 4, 5, 6, 7));
                B[s] = __builtin_bit_cast(u32x4, __builtin_shufflevector(b0, b1, 0, 1, 2, 3, 4, 5, 6, 7));
                if (s == 3) PIN();
            }
            PIN();
#pragma unroll
            for (int s = 0; s < 8; ++s) kv = mfma(A[s], B[s], kv);
            if (t <= 16) {
                const unsigned base = WS_KVP + (j * 16 + p) * KVP_BYTES + ((ah * 4 + h2) * 17 + t) * 16;
                st16(rsW, (u32x4){pack2(kv[0], kv[1]), pack2(kv[2], kv[3]), pack2(kv[4], kv[5]), pack2(kv[6], kv[7])}, base, 0);
                st16(rsW, (u32x4){pack2(kv[8], kv[9]), pack2(kv[10], kv[11]), pack2(kv[12], kv[13]), pack2(kv[14], kv[15])}, base + 34 * 16, 0);
            }
            WAITV(0);
            arrive_publish(lds, 0, 2, ws, WS_F1 + (j * 16 + p) * 256, tag, lane);
            __builtin_amdgcn_s_setprio(0);
            STAMP(6);
        } else {
            unsigned tq = L_WQ + lw * 32 + (8 * (lane >> 4) + q) * 64 + pp * 8;
            asm volatile("" : "+v"(tq));
            f32x4 qcs = {};
#pragma unroll
            for (int g = 0; g < 4; ++g) {
                u32x4 f[4];
#pragma unroll
                for (int s = 0; s < 4; ++s) f[s] = tr2(lds, tq + (4 * g + s) * 2048, 256);
                PIN();
#pragma unroll
                for (int s = 0; s < 4; ++s) qcs = mfma16(f[s], ones, qcs);
            }
            if ((lane & 15) == 0) *(LAS f32x4*)(lds + L_CSQ + lw * 64 + (lane >> 4) * 16) = qcs;
            STAMP(6);
        }
        BAR();
        {
            const rsrc_t rsO = biased_rsrc(wo, 512 * 512 * 2);
#pragma unroll
            for (int g = 0; g < 4; ++g) {
                const int i = 4 * g + lw;
                const unsigned so = 16 * ((i + r2) & 15) * 1024 + 128 * c;
                dma4(lb + L_X + i * 4096, rsO, rsO, rsO, rsO, vrow, vrow, so, so + 8 * 1024, so + 256 * 1024, so + 264 * 1024);
            }
        }
#pragma unroll
        for (int g = 0; g < 2; ++g) {
            const f32x4 c0 = *(LAS f32x4*)(lds + L_CSQ + (16 + 8 * g + 4 * h2) * 4);
            const f32x4 v = *(LAS f32x4*)(lds + L_XCH + m * 4096 + g * 1024 + lane * 16);
#pragma unroll
            for (int e = 0; e < 4; ++e) yq[4 * g + e] = __builtin_fmaf(-mean1, c0[e], v[e]);
        }
    }
    STAMP(7);
    u32x4 qpos, qneg;
    {
        const u32x4 qf = {pack2(yq[0], yq[1]), pack2(yq[2], yq[3]), pack2(yq[4], yq[5]), pack2(yq[6], yq[7])};
        const f32x16 zero = {};
        const f32x16 pq = mfma(aphi, qf, zero);
        dual_softmax(pq, rstd1 * LOG2E, qpos, qneg);
    }
    STAMP(8);
    unsigned kso[16];
#pragma unroll
    for (int i = 0; i < 16; ++i) { kso[i] = WS_KVP + (j * 16 + ((i + 4 * q4) & 15)) * KVP_BYTES; asm volatile("" : "+s"(kso[i])); }
    if (w == 0) poll_flags<PD1>(ws, WS_F1 + j * 16 * 256, 16, tag, lane);
    BAR();
    STAMP(9);
    {
        const unsigned kb = WS_KVP + j * 16 * KVP_BYTES;
        float lo = 0.f, hi = 0.f;
        unsigned v[16], z[8];
#pragma unroll
        for (int i = 0; i < 16; ++i) v[i] = ld4(rsW, tid * 4, kso[i]);
        if (w == 0) {
#pragma unroll
            for (int i = 0; i < 8; ++i) z[i] = ld4(rsW, (512 + t) * 4 + h2 * 8 * KVP_BYTES, kb + i * KVP_BYTES);
        }
        PIN();
#pragma unroll
        for (int i = 0; i < 16; ++i) { lo += blo(v[i]); hi += bhi(v[i]); }
        if (w == 0) {
            float zl = 0.f, zh = 0.f;
#pragma unroll
            for (int i = 0; i < 8; ++i) { zl += blo(z[i]); zh += bhi(z[i]); }
            zl = halves_add(zl); zh = halves_add(zh);
            if (h2 == 0) *(LAS unsigned*)(lds + L_KVA + (512 + t) * 4) = pack2(zl, zh);
        }
        *(LAS unsigned*)(lds + L_KVA + tid * 4) = pack2(lo, hi);
    }
    BAR();
    STAMP(10);
    const int npass = has3 ? 3 : 2;
    const int m2 = w & 1, kh = (w >> 1) & 1;
    const int T2 = 4 * pP + 2 * half + m2;
    const unsigned C0 = 64 * c + 32 * kh;
    {
        const unsigned col = t < 16 ? t : 16;
        const u32x4 kp = *(LAS u32x4*)(lds + L_KVA + ((a * 4 + h2) * 17 + col) * 16);
        const u32x4 kn = *(LAS u32x4*)(lds + L_KVA + ((a * 4 + 2 + h2) * 17 + col) * 16);
        f32x16 o = {};
        o = mfma(kp, qpos, o);
        o = mfma(kn, qneg, o);
        const float r = __builtin_amdgcn_rcpf(o[8] + 1e-6f);
        st16(rsW, (u32x4){pack2(o[0] * r, o[1] * r), pack2(o[2] * r, o[3] * r), pack2(o[4] * r, o[5] * r), pack2(o[6] * r, o[7] * r)},
             WS_O + ((4 * p + m) * 32 + 2 * j + a) * 1024 + lane * 16, 0);
        WAITV(0);
        BAR();
        if (tid == 0) __hip_atomic_store((GAS u64*)(ws + WS_F2 + (p * 16 + j) * 256), tag, __ATOMIC_RELAXED, __HIP_MEMORY_SCOPE_AGENT);
        SBAR();
    }
    const unsigned trp = (4 * h2 + q) * 64 + blk * 32 + pp * 8;
    const unsigned t128 = (4 * h2 + q) * 128 + (q >> 1) * 64 + blk * 32 + pp * 8;

    if (w >= 4) {
        ISSUE_STAGE(0, L_Y, 0, 4);
        STAMP(11);
    }
    if (w >= 4) {
        const rsrc_t rsD = biased_rsrc(wd, 1376 * 512 * 2);
#define ISSUE_WD(pi, halfbase) do { _Pragma("unroll") for (int g_ = 0; g_ < ((pi) == 2 ? 3 : 4); ++g_) { const int i_ = 4 * g_ + lw; if ((pi) < 2 || i_ < 11) { \
                const int ii_ = (pi) == 2 ? (i_ + r4 >= 11 ? i_ + r4 - 11 : i_ + r4) : ((i_ + r2) & 15); \
                const unsigned sa_ = 16 * (((pi) == 2 ? 64 : 32 * (pi)) + ii_) * 1024 + 128 * c, sb_ = sa_ + 16 * ((pi) == 2 ? 11 : 16) * 1024; \
                dma4(lb + (halfbase) + i_ * 4096, rsD, rsD, rsD, rsD, vrow, vrow, sa_, sa_ + 8 * 1024, sb_, sb_ + 8 * 1024); } } } while (0)
        f32x4 cs0 = {}, cs1 = {};
        const unsigned gam = lane >> 4;
        const unsigned ca0 = (lw >> 1) * 2048 + (gam >> 1) * 4096 + (8 * (gam & 1) + q) * 128 + (((lw & 1) * 64) ^ (64 * (q >> 1))) + pp * 8;
#define COLSUM_STAGE(halfbase) do { unsigned cb_ = (halfbase) + ca0; asm volatile("" : "+v"(cb_)); \
            _Pragma("unroll") for (int g_ = 0; g_ < 4; ++g_) { const u32x4 f0_ = tr2(lds, cb_ + g_ * 16384, 512), f1_ = tr2(lds, cb_ + g_ * 16384 + 32, 512), \
                                                                         f2_ = tr2(lds, cb_ + g_ * 16384 + 8192, 512), f3_ = tr2(lds, cb_ + g_ * 16384 + 8192 + 32, 512); \
                PIN(); cs0 = mfma16(f0_, ones, cs0); cs1 = mfma16(f1_, ones, cs1); cs0 = mfma16(f2_, ones, cs0); cs1 = mfma16(f3_, ones, cs1); } } while (0)
        BAR();
        STAMP(12);
        BAR();
        SBAR();
        ISSUE_STAGE(1, L_X, 0, B_EARLY);
        BAR();
        SBAR();
        ISSUE_STAGE(1, L_X, B_EARLY, 4);
        STAMP(13);
        waitv<16>();
        BAR();
        COLSUM_STAGE(L_Y);
        WAITV(0);
        lsig(lb, 8); lwait(lds, 8, 4);
        COLSUM_STAGE(L_X);
        if ((lane & 15) == 0) {
            *(LAS f32x4*)(lds + L_CS3 + ((lw & 1) * 64 + (lw >> 1) * 32 + 4 * gam) * 4) = cs0;
            *(LAS f32x4*)(lds + L_CS3 + ((lw & 1) * 64 + (lw >> 1) * 32 + 16 + 4 * gam) * 4) = cs1;
        }
        BAR();
        if (npass == 3) {
            ISSUE_UNIT(u3, L_Y);
            WAITV(0);
            SBAR();
            ISSUE_WD(0, L_X);
            {
                unsigned cb = L_Y + (lw >> 1) * 1024 + (gam >> 1) * 2048 + (8 * (gam & 1) + q) * 64 + (lw & 1) * 32 + pp * 8;
                asm volatile("" : "+v"(cb));
                f32x4 c3 = {};
#pragma unroll
                for (int g = 0; g < 4; ++g) {
                    u32x4 f[4];
#pragma unroll
                    for (int e = 0; e < 4; ++e) f[e] = tr2(lds, cb + (4 * g + e) * 4096, 256);
                    PIN();
#pragma unroll
                    for (int e = 0; e < 4; ++e) c3 = mfma16(f[e], ones, c3);
                }
                if ((lane & 15) == 0) *(LAS f32x4*)(lds + L_CS3 + (128 + (lw >> 1) * 32 + (lw & 1) * 16 + 4 * gam) * 4) = c3;
            }
            BAR();
            WAITV(0);
            BAR();
            if (lw == 0) {
                lwait(lds, 11, 4);
                if (lane == 0) __hip_atomic_store((GAS u64*)(ws + WS_F4 + 0x10000 + (p * 16 + j) * 256), tag, __ATOMIC_RELAXED, __HIP_MEMORY_SCOPE_AGENT);
            }
            SBAR();
            ISSUE_WD(1, L_Y);
            WAITV(0);
            BAR();
            if (lw == 0) { poll_flags3(ws, WS_F4 + 0x10000 + pP * 16 * 256, tag, lane); lsig(lb, 14); }
            ISSUE_WD(2, L_X);
            WAITV(0);
            BAR();
        } else {
            ISSUE_WD(0, L_Y);
            SBAR();
            ISSUE_WD(1, L_X);
            WAITV(0);
            BAR();
            SBAR();
            BAR();
            if (lw == 0) { poll_flags3(ws, WS_F4 + 0x10000 + pP * 16 * 256, tag, lane); lsig(lb, 14); }
            ISSUE_WD(2, L_Y);
            WAITV(0);
            BAR();
        }
        BAR();
    } else {
        if (PRIO_C) __builtin_amdgcn_s_setprio(PRIO_C);
        const unsigned vo = (32 * T2 + t) * 1024 + (C0 + 8 * h2) * 2;
        u32x4 xr0 = __builtin_amdgcn_raw_buffer_load_b128(rsX, vo, 0, 0);
        u32x4 xr1 = __builtin_amdgcn_raw_buffer_load_b128(rsX, vo + 32, 0, 0);
        PIN();
        STAMP(11);
        if (w == 0) poll_flags<PD2>(ws, WS_F2 + pP * 16 * 256, 16, tag, lane);
        BAR();
        STAMP(12);
        if (b == 0 && w == 1 && lane == 0) __hip_atomic_store((GAS u64*)ws, tag, __ATOMIC_RELAXED, __HIP_MEMORY_SCOPE_AGENT);
        {
            u32x4 hres0, hres1;
            u32x4 of[16];
#pragma unroll
            for (int i = 0; i < 8; ++i) of[i] = ld16(rsW, lane * 16, WS_O + (T2 * 32 + 16 * kh + ((i + r2) & 15)) * 1024);
            PIN();
            f32x16 keep, snd = {};
            { auto r = __builtin_amdgcn_permlane32_swap(xr0[0], xr0[2], false, false); xr0[0] = r[0]; xr0[2] = r[1];
              r = __builtin_amdgcn_permlane32_swap(xr0[1], xr0[3], false, false); xr0[1] = r[0]; xr0[3] = r[1];
              r = __builtin_amdgcn_permlane32_swap(xr1[0], xr1[2], false, false); xr1[0] = r[0]; xr1[2] = r[1];
              r = __builtin_amdgcn_permlane32_swap(xr1[1], xr1[3], false, false); xr1[1] = r[0]; xr1[3] = r[1]; }
#pragma unroll
            for (int e = 0; e < 4; ++e) { keep[2 * e] = blo(xr0[e]); keep[2 * e + 1] = bhi(xr0[e]); keep[8 + 2 * e] = blo(xr1[e]); keep[8 + 2 * e + 1] = bhi(xr1[e]); }
            asm volatile("" : "+v"(keep));
            gemm2<16, 4096, 1024, 0>(lds, L_X + kh * 2048 + (t128 ^ (64 * kh)), L_X + kh * 2048 + (t128 ^ (64 * (kh ^ 1))), of, keep, snd, [&](int g) {
                if (g < 4) {
#pragma unroll
                    for (int e = 0; e < 2; ++e) of[8 + 2 * g + e] = ld16(rsW, lane * 16, WS_O + (T2 * 32 + 16 * kh + ((8 + 2 * g + e + r2) & 15)) * 1024);
                    SB();
                } });
#pragma unroll
            for (int g = 0; g < 4; ++g) *(LAS f32x4*)(lds + L_XCH + w * 4096 + g * 1024 + lane * 16) = (f32x4){snd[4 * g], snd[4 * g + 1], snd[4 * g + 2], snd[4 * g + 3]};
            STAMP(13);
            BAR();
#pragma unroll
            for (int g = 0; g < 4; ++g) {
                const f32x4 v = *(LAS f32x4*)(lds + L_XCH + (w ^ 2) * 4096 + g * 1024 + lane * 16);
#pragma unroll
                for (int e = 0; e < 4; ++e) keep[4 * g + e] += v[e];
            }
#pragma unroll
            for (int e = 0; e < 4; ++e) { hres0[e] = pack2(keep[2 * e], keep[2 * e + 1]); hres1[e] = pack2(keep[8 + 2 * e], keep[8 + 2 * e + 1]); }
            const unsigned hb = WS_H + (T2 * 32 + 4 * c + 2 * kh) * 1024;
            st16(rsW, hres0, lane * 16, hb);
            st16(rsW, hres1, lane * 16, hb + 1024);
            *(LAS u32x4*)(lds + L_HRES + w * 2048 + lane * 16) = hres0;
            *(LAS u32x4*)(lds + L_HRES + w * 2048 + 1024 + lane * 16) = hres1;
            f32x2 s1 = {0.f, 0.f}, s2 = {0.f, 0.f};
#pragma unroll
            for (int e = 0; e < 8; ++e) { const f32x2 v = {keep[2 * e], keep[2 * e + 1]}; s1 += v; s2 += v * v; }
            float su = s1[0] + s1[1], sq = s2[0] + s2[1];
            su = halves_add(su); sq = halves_add(sq);
            __builtin_amdgcn_raw_buffer_store_b32(asu(h2 ? sq : su), rsW, lane * 4 + WS_HST + (T2 * 16 + 2 * c + kh) * 256, 0, 16);
            SBAR();
            WAITV(0);
            BAR();
            if (tid == 0) __hip_atomic_store((GAS u64*)(ws + WS_F3 + (pP * 16 + jP) * 256), tag, __ATOMIC_RELAXED, __HIP_MEMORY_SCOPE_AGENT);
            SBAR();
        }
        STAMP(14);
        if (w == 0) poll_flags<PD3>(ws, WS_F3 + p * 16 * 256, 16, tag, lane);
        BAR();
        STAMP(15);
        {
            const int T = 4 * p + w;
            u32x4 hf[32];
            float sv[16];
#pragma unroll
            for (int s = 0; s < 8; ++s) hf[s] = ld16(rsW, lane * 16, WS_H + (T * 32 + ((s + r3) & 31)) * 1024);
            PIN();
            float mean, r2;
            auto act_out = [&](const f32x16& ag, const f32x16& au, int cs, unsigned ks) {
                float y[16];
#pragma unroll
                for (int g = 0; g < 4; ++g) {
                    const f32x4 cg = *(LAS f32x4*)(lds + L_CS3 + (cs * 64 + 8 * g + 4 * h2) * 4), cu = *(LAS f32x4*)(lds + L_CS3 + (cs * 64 + 32 + 8 * g + 4 * h2) * 4);
#pragma unroll
                    for (int e = 0; e < 4; ++e) y[4 * g + e] = (__builtin_fmaf(-mean, cg[e], ag[4 * g + e]) * r2) * __builtin_fmaf(-mean, cu[e], au[4 * g + e]);
                }
                const unsigned ab = WS_ACT + (T * 86 + ks) * 1024;
                st16(rsW, (u32x4){pack2(y[0], y[1]), pack2(y[2], y[3]), pack2(y[4], y[5]), pack2(y[6], y[7])}, lane * 16, ab);
                st16(rsW, (u32x4){pack2(y[8], y[9]), pack2(y[10], y[11]), pack2(y[12], y[13]), pack2(y[14], y[15])}, lane * 16, ab + 1024);
            };
            {
                f32x16 g0 = {}, g1 = {}, u0 = {}, u1 = {};
                gemm4<0>(lds, L_Y + t128, L_Y + (t128 ^ 64), hf, g0, g1, u0, u1, [&](int i) {
                    if (i < 8) hf[8 + i] = ld16(rsW, lane * 16, WS_H + (T * 32 + ((8 + i + r3) & 31)) * 1024);
                    else { hf[2 * i] = ld16(rsW, lane * 16, WS_H + (T * 32 + ((2 * i + r3) & 31)) * 1024); hf[2 * i + 1] = ld16(rsW, lane * 16, WS_H + (T * 32 + ((2 * i + 1 + r3) & 31)) * 1024); }
                    if (i < 4) {
#pragma unroll
                        for (int k = 0; k < 4; ++k) sv[4 * i + k] = asf(ld4(rsW, lane * 4, WS_HST + (T * 16 + 4 * i + k) * 256));
                    }
                    if (i == 9) {
                        float tot = 0.f;
#pragma unroll
                        for (int k = 0; k < 16; ++k) tot += sv[k];
                        const u32x2 r = both_halves(tot);
                        mean = asf(r[0]) * (1.0f / 512.0f);
                        const float var = fmaxf(asf(r[1]) * (1.0f / 512.0f) - mean * mean, 0.f);
                        r2 = __builtin_amdgcn_rcpf(var + 1e-5f);
                        asm volatile("" : "+v"(mean), "+v"(r2));
                    }
                    SB(); });
                BAR();
                STAMP(16);
                gemm4<16>(lds, L_X + t128, L_X + (t128 ^ 64), hf, g0, g1, u0, u1, nohook);
                STAMP(17);
                act_out(g0, u0, 0, 4 * j);
                act_out(g1, u1, 1, 4 * j + 2);
                SBAR();
            }
            if (npass == 3) {
                f32x16 ag = {}, au = {};
                gemm2<32, 2048, 512, 0>(lds, L_Y + trp, L_Y + 1024 + trp, hf, ag, au, [&](int g) {
                    if (g == 3) { WAITV(0); arrive_publish(lds, 3, 4, ws, WS_F4 + (p * 16 + j) * 256, tag, lane); PIN(); } });
                BAR();
                STAMP(18);
                act_out(ag, au, 2, 2 * u3);
            } else {
                WAITV(0); arrive_publish(lds, 3, 4, ws, WS_F4 + (p * 16 + j) * 256, tag, lane);
            }
        }
        STAMP(19);
        {
            f32x16 a0, a1 = {};
            {
                const u32x4 hres0 = *(LAS u32x4*)(lds + L_HRES + w * 2048 + lane * 16), hres1 = *(LAS u32x4*)(lds + L_HRES + w * 2048 + 1024 + lane * 16);
#pragma unroll
                for (int e = 0; e < 4; ++e) { a0[2 * e] = blo(hres0[e]); a0[2 * e + 1] = bhi(hres0[e]); a0[8 + 2 * e] = blo(hres1[e]); a0[8 + 2 * e + 1] = bhi(hres1[e]); }
                asm volatile("" : "+v"(a0));
            }
            const unsigned tk = kh * 2048 + (t128 ^ (64 * kh)), ts = kh * 2048 + (t128 ^ (64 * (kh ^ 1)));
            const unsigned h0 = npass == 3 ? L_X : L_Y, h1 = npass == 3 ? L_Y : L_X;
            if (w == 0) poll_flags(ws, WS_F4 + pP * 16 * 256, 16, tag, lane);
            BAR();
            STAMP(20);
            u32x4 af[43];
            auto ld_act = [&](int i) {
                const int ks = i < 32 ? 32 * (i >> 4) + kh * 16 + ((i + r2) & 15) : 64 + kh * 11 + (i - 32 + r4 >= 11 ? i - 32 + r4 - 11 : i - 32 + r4);
                af[i] = ld16(rsW, lane * 16, WS_ACT + (T2 * 86 + ks) * 1024); };
            if (npass == 3) { WAITV(0); lsig(lb, 11); }
#pragma unroll
            for (int i = 0; i < 8; ++i) ld_act(i);
            SBAR();
            gemm2<16, 4096, 1024, 0>(lds, h0 + tk, h0 + ts, af, a0, a1, [&](int g) { ld_act(8 + 2 * g); ld_act(9 + 2 * g); SB(); });
            STAMP(21);
            BAR();
            STAMP(22);
            gemm2<16, 4096, 1024, 16>(lds, h1 + tk, h1 + ts, af, a0, a1, [&](int g) {
                if (g == 4) lwait(lds, 14, 1);
                ld_act(24 + 2 * g); ld_act(25 + 2 * g); SB(); });
            BAR();
            STAMP(23);
            gemm2<11, 4096, 1024, 32>(lds, h0 + tk, h0 + ts, af, a0, a1, [&](int g) { if (g < 2) { ld_act(40 + 2 * g < 43 ? 40 + 2 * g : 42); if (g == 0) ld_act(41); SB(); } });
#pragma unroll
            for (int g = 0; g < 4; ++g) *(LAS f32x4*)(lds + L_XCH + w * 4096 + g * 1024 + lane * 16) = (f32x4){a1[4 * g], a1[4 * g + 1], a1[4 * g + 2], a1[4 * g + 3]};
            BAR();
            STAMP(24);
#pragma unroll
            for (int g = 0; g < 4; ++g) {
                const f32x4 v = *(LAS f32x4*)(lds + L_XCH + (w ^ 2) * 4096 + g * 1024 + lane * 16);
#pragma unroll
                for (int e = 0; e < 4; ++e) a0[4 * g + e] += v[e];
            }
            u32x4 o0, o1;
#pragma unroll
            for (int e = 0; e < 4; ++e) { o0[e] = pack2(a0[2 * e], a0[2 * e + 1]); o1[e] = pack2(a0[8 + 2 * e], a0[8 + 2 * e + 1]); }
            { auto r = __builtin_amdgcn_permlane32_swap(o0[0], o0[2], false, false); o0[0] = r[0]; o0[2] = r[1];
              r = __builtin_amdgcn_permlane32_swap(o0[1], o0[3], false, false); o0[1] = r[0]; o0[3] = r[1];
              r = __builtin_amdgcn_permlane32_swap(o1[0], o1[2], false, false); o1[0] = r[0]; o1[2] = r[1];
              r = __builtin_amdgcn_permlane32_swap(o1[1], o1[3], false, false); o1[1] = r[0]; o1[3] = r[1]; }
            const rsrc_t rsOut = __builtin_amdgcn_make_buffer_rsrc((void*)out, 0, 2048 * 512 * 2, 0x00020000);
            int tz = threadIdx.x; asm volatile("" : "+v"(tz));
            const unsigned vz = (32 * T2 + (tz & 31)) * 1024 + (C0 + 8 * ((tz >> 5) & 1)) * 2;
            __builtin_amdgcn_raw_buffer_store_b128(o0, rsOut, vz, 0, 0);
            __builtin_amdgcn_raw_buffer_store_b128(o1, rsOut, vz + 32, 0, 0);
        }
        STAMP(25); CSTAMP(31);
    }
}

extern "C" void kernel_launch(void* const* d_in, const int* in_sizes, int n_in, void* d_out, int out_size, void* d_ws, size_t ws_size, hipStream_t stream) {
    (void)in_sizes; (void)n_in; (void)out_size; (void)ws_size;
    (void)hipFuncSetAttribute((const void*)hedgehog_block, hipFuncAttributeMaxDynamicSharedMemorySize, LDS_BYTES);
    hedgehog_block<<<256, 512, LDS_BYTES, stream>>>((const bf16_t*)d_in[0], (const bf16_t*)d_in[1], (const bf16_t*)d_in[2], (const bf16_t*)d_in[3],
                                                    (const bf16_t*)d_in[5], (const bf16_t*)d_in[6], (const bf16_t*)d_in[7], (bf16_t*)d_out, (char*)d_ws);
}
```

```cpp
#include <hip/hip_runtime.h>
#include <hip/hip_bf16.h>

#define LAS __attribute__((address_space(3)))
#define GAS __attribute__((address_space(1)))
#define DI __device__ __forceinline__
typedef unsigned short bf16_t;
typedef unsigned u32x4 __attribute__((ext_vector_type(4)));
typedef unsigned u32x2 __attribute__((ext_vector_type(2)));
typedef short s16x4 __attribute__((ext_vector_type(4)));
typedef __bf16 bf16x8 __attribute__((ext_vector_type(8)));
typedef __bf16 bf16x2 __attribute__((ext_vector_type(2)));
typedef float f32x16 __attribute__((ext_vector_type(16)));
typedef float f32x4 __attribute__((ext_vector_type(4)));
typedef float f32x2 __attribute__((ext_vector_type(2)));
typedef unsigned long long u64;
typedef __amdgpu_buffer_rsrc_t rsrc_t;

constexpr unsigned WS_F1 = 0x10000, WS_F2 = 0x20000, WS_F3 = 0x30000, WS_F4 = 0x40000;
constexpr unsigned WS_KVP = 0x80000, KVP_BYTES = 2176;
constexpr unsigned WS_HST = 0x110000;
constexpr unsigned WS_XST = 0x160000;
constexpr unsigned WS_O = 0x200000, WS_H = 0x400000, WS_ACT = 0x600000, WS_PROF = 0xC00000, WS_BYTES = 0x1000000;
constexpr unsigned L_X = 0, L_Y = 65536, L_XCH = 131072, L_MISC = 147456;
constexpr unsigned L_STAT = L_MISC, L_CS1 = L_MISC + 1024, L_CSQ = L_MISC + 1280, L_WPHI = L_MISC + 1536, L_KVA = L_MISC + 2560, L_CS3 = L_MISC + 4736, L_CNT = L_MISC + 5504, L_TAG = L_MISC + 6016, L_DUMMY = L_MISC + 6144, L_HRES = L_MISC + 8192;
constexpr unsigned L_WQ = 65536, L_PHI = 98304, L_VIMG = 114688  , SS_BYTES = 24576;
constexpr unsigned LDS_BYTES = 163840;
constexpr unsigned SPIN_CAP = 40000;
constexpr float LOG2E = 1.4426950408889634f;

DI float asf(unsigned v) { return __builtin_bit_cast(float, v); }
DI unsigned asu(float v) { return __builtin_bit_cast(unsigned, v); }
DI unsigned pack2(float lo, float hi) { bf16x2 v = {(__bf16)lo, (__bf16)hi}; return __builtin_bit_cast(unsigned, v); }
DI float blo(unsigned v) { return __builtin_bit_cast(float, v << 16); }
DI float bhi(unsigned v) { return __builtin_bit_cast(float, v & 0xffff0000u); }
DI f32x16 mfma(u32x4 a, u32x4 b, f32x16 c) { return __builtin_amdgcn_mfma_f32_32x32x16_bf16(__builtin_bit_cast(bf16x8, a), __builtin_bit_cast(bf16x8, b), c, 0, 0, 0); }
DI f32x4 mfma16(u32x4 a, u32x4 b, f32x4 c) { return __builtin_amdgcn_mfma_f32_16x16x32_bf16(__builtin_bit_cast(bf16x8, a), __builtin_bit_cast(bf16x8, b), c, 0, 0, 0); }
DI u32x4 tr2(LAS char* lds, unsigned off, unsigned second) {
    const s16x4 lo = __builtin_amdgcn_ds_read_tr16_b64_v4i16((LAS s16x4*)(lds + off));
    const s16x4 hi = __builtin_amdgcn_ds_read_tr16_b64_v4i16((LAS s16x4*)(lds + off + second));
    return __builtin_bit_cast(u32x4, __builtin_shufflevector(lo, hi, 0, 1, 2, 3, 4, 5, 6, 7));
}
DI void dma16(const rsrc_t rs, unsigned ldsaddr, unsigned voff, unsigned soff) {
    asm volatile("s_mov_b32 m0, %0\n\ts_nop 0\n\tbuffer_load_dwordx4 %1, %2, %3 offen lds" ::"s"(ldsaddr), "v"(voff), "s"(rs), "s"(soff) : "memory");
}
constexpr unsigned BIAS = 4096;
DI rsrc_t biased_rsrc(const void* p, unsigned bytes) { return __builtin_amdgcn_make_buffer_rsrc((void*)((const char*)p - BIAS), 0, bytes + BIAS, 0x00020000); }
DI void dma4(unsigned ldsaddr, const rsrc_t ra, const rsrc_t rb, const rsrc_t rc, const rsrc_t rd, unsigned va, unsigned vb, unsigned s0, unsigned s1, unsigned s2, unsigned s3) {
    asm volatile("s_mov_b32 m0, %0\n\ts_nop 0\n\t"
                 "buffer_load_dwordx4 %5, %1, %7 offen lds\n\tbuffer_load_dwordx4 %6, %2, %8 offen offset:1024 lds\n\t"
                 "buffer_load_dwordx4 %5, %3, %9 offen offset:2048 lds\n\tbuffer_load_dwordx4 %6, %4, %10 offen offset:3072 lds"
                 ::"s"(ldsaddr), "s"(ra), "s"(rb), "s"(rc), "s"(rd), "v"(va), "v"(vb), "s"(s0 + BIAS), "s"(s1 + BIAS - 1024), "s"(s2 + BIAS - 2048), "s"(s3 + BIAS - 3072) : "memory");
}
DI void dma2(unsigned ldsaddr, const rsrc_t r, unsigned va, unsigned vb, unsigned s0, unsigned s1) {
    asm volatile("s_mov_b32 m0, %0\n\ts_nop 0\n\tbuffer_load_dwordx4 %2, %1, %4 offen lds\n\tbuffer_load_dwordx4 %3, %1, %5 offen offset:1024 lds"
                 ::"s"(ldsaddr), "s"(r), "v"(va), "v"(vb), "s"(s0 + BIAS), "s"(s1 + BIAS - 1024) : "memory");
}
#define WAITV(n) asm volatile("s_waitcnt vmcnt(" #n ")" ::: "memory")
#define PIN() do { asm volatile("" ::: "memory"); __builtin_amdgcn_sched_barrier(0); } while (0)
template <int N> DI void waitv() { asm volatile("s_waitcnt vmcnt(%0)" ::"n"(N) : "memory"); }
#define B_EARLY 1
#define PD1 0
#define PD2 4
#define PD3 4
#define PRIO_C 0
#define K0 3
#define R1MODE 2
#define STAMP2_SS(ss) do { if (ss == 0) STAMP2(0); if (ss == 1) STAMP2(1); if (ss == 2) STAMP2(2); if (ss == 3) STAMP2(3); if (ss == 4) STAMP2(4); if (ss == 5) STAMP2(5); if (ss == 6) STAMP2(6); if (ss == 7) STAMP2(7); } while (0)
#define SBAR() asm volatile("s_barrier" ::: "memory")
#define BAR() asm volatile("s_waitcnt lgkmcnt(0)\n\ts_barrier" ::: "memory")
DI u32x2 both_halves(float v) { const unsigned a = asu(v); const auto r = __builtin_amdgcn_permlane32_swap(a, a, false, false); return (u32x2){r[0], r[1]}; }
DI float halves_add(float v) { const u32x2 r = both_halves(v); return asf(r[0]) + asf(r[1]); }
DI float halves_max(float v) { const u32x2 r = both_halves(v); return fmaxf(asf(r[0]), asf(r[1])); }
DI float halves_min(float v) { const u32x2 r = both_halves(v); return fminf(asf(r[0]), asf(r[1])); }
DI float dpp_ror4(float v) { return asf(__builtin_amdgcn_update_dpp(0, (int)asu(v), 0x124, 0xf, 0xf, true)); }
DI float dpp_ror8(float v) { return asf(__builtin_amdgcn_update_dpp(0, (int)asu(v), 0x128, 0xf, 0xf, true)); }
DI float rows16_add(float v) { const unsigned a = asu(v); const auto r = __builtin_amdgcn_permlane16_swap(a, a, false, false); return asf(r[0]) + asf(r[1]); }
DI float stride4_add(float v) { v += dpp_ror4(v); v += dpp_ror8(v); v = rows16_add(v); return halves_add(v); }
DI float quad_add(float v) {
    v += __builtin_bit_cast(float, __builtin_amdgcn_update_dpp(0, __builtin_bit_cast(int, v), 0xB1, 0xf, 0xf, true));
    v += __builtin_bit_cast(float, __builtin_amdgcn_update_dpp(0, __builtin_bit_cast(int, v), 0x4E, 0xf, 0xf, true));
    return v;
}
DI void st16(const rsrc_t rs, u32x4 v, unsigned voff, unsigned soff) { __builtin_amdgcn_raw_buffer_store_b128(v, rs, voff + soff, 0, 16); }
DI u32x4 ld16(const rsrc_t rs, unsigned voff, unsigned soff) { return __builtin_amdgcn_raw_buffer_load_b128(rs, voff, soff, 16); }
DI unsigned ld4(const rsrc_t rs, unsigned voff, unsigned soff) { return __builtin_amdgcn_raw_buffer_load_b32(rs, voff, soff, 16); }

DI void poll_flags3(const char* ws, unsigned off, u64 tag, int lane) {
    const int l = lane < 11 ? lane : 10;
    const GAS u64* f = (const GAS u64*)(ws + off) + (l < 6 ? l : l + 2) * 32;
#pragma unroll 1
    for (unsigned spin = 0; spin < SPIN_CAP; ++spin) {
        const u64 v = __hip_atomic_load(f, __ATOMIC_RELAXED, __HIP_MEMORY_SCOPE_AGENT);
        if (__all(v == tag)) break;
        __builtin_amdgcn_s_sleep(1);
    }
    asm volatile("" ::: "memory");
}
DI void nohook(int) {}
template <int DELAY = 0>
DI void poll_flags(const char* ws, unsigned off, int n, u64 tag, int lane) {
#pragma unroll
    for (int i = 0; i < DELAY; ++i) __builtin_amdgcn_s_sleep(2);
    const GAS u64* f = (const GAS u64*)(ws + off) + (lane < n ? lane : n - 1) * 32;
#pragma unroll 1
    for (unsigned spin = 0; spin < SPIN_CAP; ++spin) {
        const u64 v = __hip_atomic_load(f, __ATOMIC_RELAXED, __HIP_MEMORY_SCOPE_AGENT);
        if (__all(v == tag)) break;
        __builtin_amdgcn_s_sleep(1);
    }
    asm volatile("" ::: "memory");
}
DI void arrive_publish(LAS char* lds, int cnt, unsigned n, char* ws, unsigned flag_off, u64 tag, int lane) {
    if (lane == 0) {
        const unsigned old = __hip_atomic_fetch_add((LAS unsigned*)(lds + L_CNT) + cnt, 1u, __ATOMIC_RELAXED, __HIP_MEMORY_SCOPE_WORKGROUP);
        if (old == n - 1) __hip_atomic_store((GAS u64*)(ws + flag_off), tag, __ATOMIC_RELAXED, __HIP_MEMORY_SCOPE_AGENT);
    }
}
DI void lsig(unsigned lb, int idx) {
    const unsigned a = lb + L_CNT + idx * 4, one = 1;
    asm volatile("s_mov_b64 exec, 1\n\tds_add_u32 %0, %1\n\ts_mov_b64 exec, -1" ::"v"(a), "v"(one) : "memory");
}
DI void lwait(LAS char* lds, int idx, unsigned n) {
    const volatile LAS unsigned* c = (const volatile LAS unsigned*)(lds + L_CNT) + idx;
#pragma unroll 1
    for (unsigned spin = 0; spin < 64 * SPIN_CAP; ++spin) {
        if ((unsigned)__builtin_amdgcn_readfirstlane(*c) >= n) break;
        __builtin_amdgcn_s_sleep(1);
    }
    asm volatile("" ::: "memory");
}
DI void dual_softmax(const f32x16& p, float sl, u32x4& pos, u32x4& neg) {
    float mx = fmaxf(fmaxf(fmaxf(p[0], p[1]), fmaxf(p[2], p[3])), fmaxf(fmaxf(p[4], p[5]), fmaxf(p[6], p[7])));
    float mn = fminf(fminf(fminf(p[0], p[1]), fminf(p[2], p[3])), fminf(fminf(p[4], p[5]), fminf(p[6], p[7])));
    mx = halves_max(mx); mn = halves_min(mn);
    const float cx = -mx * sl, cn = mn * sl;
    f32x2 ep[4], en[4];
#pragma unroll
    for (int i = 0; i < 4; ++i) {
        const f32x2 v = {p[2 * i], p[2 * i + 1]};
        const f32x2 u = v * sl + cx, d = v * (-sl) + cn;
        ep[i] = (f32x2){__builtin_amdgcn_exp2f(u[0]), __builtin_amdgcn_exp2f(u[1])};
        en[i] = (f32x2){__builtin_amdgcn_exp2f(d[0]), __builtin_amdgcn_exp2f(d[1])};
    }
    const f32x2 sp = (ep[0] + ep[1]) + (ep[2] + ep[3]), sn = (en[0] + en[1]) + (en[2] + en[3]);
    const float rp = __builtin_amdgcn_rcpf(halves_add(sp[0] + sp[1])), rn = __builtin_amdgcn_rcpf(halves_add(sn[0] + sn[1]));
#pragma unroll
    for (int i = 0; i < 4; ++i) { const f32x2 a = ep[i] * rp, b = en[i] * rn; pos[i] = pack2(a[0], a[1]); neg[i] = pack2(b[0], b[1]); }
}

#define SB() __builtin_amdgcn_sched_barrier(0)
template <int N, int STRIDE, int SECOND, int OFF, int NA, class Hook>
DI void gemm2(LAS char* lds, unsigned b0, unsigned b1, const u32x4 (&act)[NA], f32x16& acc0, f32x16& acc1, Hook hook) {
    u32x4 fb[2][4];
    asm volatile("" : "+v"(b0), "+v"(b1));
    constexpr int NG = (N + 1) / 2;
#pragma unroll
    for (int e = 0; e < 2; ++e) { fb[0][2 * e] = tr2(lds, b0 + e * STRIDE, SECOND); fb[0][2 * e + 1] = tr2(lds, b1 + e * STRIDE, SECOND); }
    SB();
#pragma unroll
    for (int g = 0; g < NG; ++g) {
#pragma unroll
        for (int e = 0; e < 2; ++e) {
            const int i = 2 * g + e, n = i + 2;
            if (i < N) acc0 = mfma(fb[g & 1][2 * e], act[OFF + i], acc0);
            if (n < N) fb[(g + 1) & 1][2 * e] = tr2(lds, b0 + n * STRIDE, SECOND);
            SB();
            if (i < N) acc1 = mfma(fb[g & 1][2 * e + 1], act[OFF + i], acc1);
            if (n < N) fb[(g + 1) & 1][2 * e + 1] = tr2(lds, b1 + n * STRIDE, SECOND);
            SB();
        }
        hook(g);
    }
    asm volatile("" : "+v"(acc0), "+v"(acc1));
}
template <int OFF, class Hook>
DI void gemm4(LAS char* lds, unsigned b0, unsigned b1, const u32x4 (&act)[32], f32x16& g0, f32x16& g1, f32x16& u0, f32x16& u1, Hook hook) {
    u32x4 fb[2][4];
    asm volatile("" : "+v"(b0), "+v"(b1));
    fb[0][0] = tr2(lds, b0, 1024); fb[0][1] = tr2(lds, b1, 1024); fb[0][2] = tr2(lds, b0 + 2048, 1024); fb[0][3] = tr2(lds, b1 + 2048, 1024);
    SB();
#pragma unroll
    for (int i = 0; i < 16; ++i) {
        const int o = (i + 1) * 4096;
        g0 = mfma(fb[i & 1][0], act[OFF + i], g0);
        if (i < 15) fb[(i + 1) & 1][0] = tr2(lds, b0 + o, 1024);
        SB();
        g1 = mfma(fb[i & 1][1], act[OFF + i], g1);
        if (i < 15) fb[(i + 1) & 1][1] = tr2(lds, b1 + o, 1024);
        SB();
        u0 = mfma(fb[i & 1][2], act[OFF + i], u0);
        if (i < 15) fb[(i + 1) & 1][2] = tr2(lds, b0 + o + 2048, 1024);
        SB();
        u1 = mfma(fb[i & 1][3], act[OFF + i], u1);
        if (i < 15) fb[(i + 1) & 1][3] = tr2(lds, b1 + o + 2048, 1024);
        SB();
        hook(i);
    }
    asm volatile("" : "+v"(g0), "+v"(g1), "+v"(u0), "+v"(u1));
}
DI void touch_lines(const rsrc_t rs, unsigned ldsaddr, unsigned voff, unsigned soff) {
    asm volatile("s_mov_b32 m0, %0\n\ts_nop 0\n\tbuffer_load_dword %1, %2, %3 offen lds" ::"s"(ldsaddr), "v"(voff), "s"(rs), "s"(soff) : "memory");
}

#define STAMP(i) do {} while (0)
#define CSTAMP(i) do {} while (0)
#define STAMP2(i) do {} while (0)

__global__ void __launch_bounds__(512, 2)
hedgehog_block(const bf16_t* __restrict__ x, const bf16_t* __restrict__ wqkv, const bf16_t* __restrict__ wphi, const bf16_t* __restrict__ wo,
               const bf16_t* __restrict__ wg, const bf16_t* __restrict__ wu, const bf16_t* __restrict__ wd, bf16_t* __restrict__ out, char* __restrict__ ws) {
    extern __shared__ __attribute__((aligned(16))) char smem[];
    LAS char* lds = (LAS char*)smem;
    const unsigned lb = (unsigned)(size_t)lds;
    const int tid = threadIdx.x, lane = tid & 63;
    const int w = __builtin_amdgcn_readfirstlane(tid >> 6);
    const int b = blockIdx.x, xcd = b & 7, slot = b >> 3;
    const int p = 4 * (xcd >> 1) + (slot >> 3), j = 8 * (xcd & 1) + (slot & 7);
    const int pP = 2 * xcd + (slot >> 4), jP = slot & 15, c = slot & 7, half = (slot >> 3) & 1;
    const bool has3 = (j & 7) < (j < 8 ? 6 : 5);
    const int u3 = 32 + (j < 8 ? j : j - 2);
    const int t = lane & 31, h2 = lane >> 5, q = (lane & 15) >> 2, pp = lane & 3, blk = (lane >> 4) & 1;
    const int m = w & 3, a = w >> 2, lw = w & 3;
    const int q4 = slot >> 3;
    const int r1 = R1MODE == 0 ? 0 : R1MODE == 1 ? ((c + 2 * q4) & 7) : R1MODE == 2 ? 4 * (q4 & 1) : 2 * q4, r2 = (2 * c + 4 * q4) & 15, r3 = (4 * c + 8 * q4) & 31, r4 = (c + 3 * q4) % 11;
    STAMP(0); CSTAMP(30);
    const rsrc_t rsX = __builtin_amdgcn_make_buffer_rsrc((void*)x, 0, 2048 * 512 * 2, 0x00020000);
    const rsrc_t rsQ = __builtin_amdgcn_make_buffer_rsrc((void*)wqkv, 0, 512 * 1536 * 2, 0x00020000);
    const rsrc_t rsW = __builtin_amdgcn_make_buffer_rsrc((void*)ws, 0, WS_BYTES, 0x00020000);
    u64 tag = 0;
#define UNIFORM64(v) (((u64)(unsigned)__builtin_amdgcn_readfirstlane((unsigned)((v) >> 32)) << 32) | (unsigned)__builtin_amdgcn_readfirstlane((unsigned)(v)))
    const u32x4 ones = {0x3F803F80u, 0x3F803F80u, 0x3F803F80u, 0x3F803F80u};
    const unsigned c128 = ((lane & 7) ^ (4 * ((lane >> 4) & 1))) * 16;
    const unsigned vrow = (lane >> 3) * 1024 + c128;
    const unsigned trn = (8 * h2 + q) * 64 + pp * 8;

    const rsrc_t rsG = biased_rsrc(wg, 512 * 1376 * 2), rsU = biased_rsrc(wu, 512 * 1376 * 2);
    const unsigned vgu = (lane >> 2) * 2752 + (lane & 3) * 16, vgu8 = (lane >> 3) * 2752 + c128;
#define ISSUE_STAGE(st, halfbase, g0, g1) do { _Pragma("unroll") for (int g_ = (g0); g_ < (g1); ++g_) { const int i_ = 4 * g_ + lw; const unsigned so_ = 16 * ((16 * (st) + i_ + r3) & 31) * 2752 + 128 * j; \
            dma4(lb + (halfbase) + i_ * 4096, rsG, rsG, rsU, rsU, vgu8, vgu8, so_, so_ + 8 * 2752, so_, so_ + 8 * 2752); } } while (0)
#define ISSUE_UNIT(u, halfbase) do { _Pragma("unroll") for (int g_ = 0; g_ < 4; ++g_) { const int s_ = 2 * (4 * g_ + lw); \
            const unsigned sa_ = 16 * ((s_ + r3) & 31) * 2752 + 64 * (u), sb_ = 16 * ((s_ + 1 + r3) & 31) * 2752 + 64 * (u); \
            dma4(lb + (halfbase) + s_ * 2048, rsG, rsU, rsG, rsU, vgu, vgu, sa_, sa_, sb_, sb_); } } while (0)
    if (w == 3) {
        u64 pc; asm volatile("s_getpc_b64 %0" : "=s"(pc));
        const rsrc_t rsC = __builtin_amdgcn_make_buffer_rsrc((void*)(pc & ~127ull), 0, 36864, 0x00020000);
        touch_lines(rsC, lb + L_DUMMY, (slot * 9 + (lane < 9 ? lane : 0)) * 128, 0);
    }
    u32x4 xf[32];
    float yk[16];
    float mean1, rstd1;
    if (w < 4) {
        {
            u64 epoch = __hip_atomic_load((const GAS u64*)ws, __ATOMIC_RELAXED, __HIP_MEMORY_SCOPE_AGENT);
            const unsigned R = 128 * p + 8 * j + 2 * w;
            const u32x4 v0 = __builtin_amdgcn_raw_buffer_load_b128(rsX, lane * 16, R * 1024, 0), v1 = __builtin_amdgcn_raw_buffer_load_b128(rsX, lane * 16, R * 1024 + 1024, 0);
            if (tid < 16) *(LAS unsigned*)(lds + L_CNT + tid * 4) = 0u;
            f32x2 a1 = {0.f, 0.f}, a2 = {0.f, 0.f}, b1 = {0.f, 0.f}, b2 = {0.f, 0.f};
#pragma unroll
            for (int e = 0; e < 4; ++e) {
                const f32x2 z0 = {blo(v0[e]), bhi(v0[e])}, z1 = {blo(v1[e]), bhi(v1[e])};
                a1 += z0; a2 += z0 * z0; b1 += z1; b2 += z1 * z1;
            }
            const float su0 = stride4_add(quad_add(a1[0] + a1[1])), sq0 = stride4_add(quad_add(a2[0] + a2[1]));
            const float su1 = stride4_add(quad_add(b1[0] + b1[1])), sq1 = stride4_add(quad_add(b2[0] + b2[1]));
            const float su = (lane & 1) ? su1 : su0, sq = (lane & 1) ? sq1 : sq0;
            const float mean = su * (1.0f / 512.0f), var = fmaxf(sq * (1.0f / 512.0f) - mean * mean, 0.f);
            const float rstd = __builtin_amdgcn_rsqf(var + 1e-5f);
            asm volatile("s_waitcnt vmcnt(0)" : "+v"(epoch)::"memory");
            tag = UNIFORM64(epoch) + 0x9E3779B97F4A7C15ull;
            if (tid == 0) *(LAS u64*)(lds + L_TAG) = tag;
            if (lane < 2) st16(rsW, (u32x4){asu(mean), asu(rstd), (unsigned)tag, (unsigned)(tag >> 32)}, (R + lane) * 16, WS_XST);
        }
        STAMP(1);
        const unsigned xa0 = (32 * m + t) * 128 + ((h2 ^ ((t >> 1) & 7)) * 16);
        const unsigned twA = 16384 + (blk ? 1024 + 32 : 0) + trn, twB = 16384 + (blk ? 1024 : 32) + trn;
        f32x16 accA = {}, accB = {};
        u32x4 fa[2][2], fb[2][2], xs;
        unsigned bA = twA, bB = twB, bx0 = xa0, bx1 = xa0 ^ 32, bx2 = xa0 ^ 64, bx3 = xa0 ^ 96;
        asm volatile("" : "+v"(bA), "+v"(bB), "+v"(bx0), "+v"(bx1), "+v"(bx2), "+v"(bx3));
#pragma unroll
        for (int ss = 0; ss <= 8; ++ss) {
            if (ss < 8) {
                BAR();
                STAMP2_SS(ss);
                if (ss == 0) STAMP(2);
                if (ss == 2) {
            if (w < 3) {
                const rsrc_t rsA = __builtin_amdgcn_make_buffer_rsrc((void*)wg, 0, 512 * 1376 * 2, 0x00020000), rsB = __builtin_amdgcn_make_buffer_rsrc((void*)wu, 0, 512 * 1376 * 2, 0x00020000);
                const rsrc_t rsCc = __builtin_amdgcn_make_buffer_rsrc((void*)wd, 0, 512 * 1376 * 2, 0x00020000), rsE = __builtin_amdgcn_make_buffer_rsrc((void*)wo, 0, 512 * 512 * 2, 0x00020000);
                const unsigned blk64 = b * 3 + w;
                if (blk64 < 172) touch_lines(rsA, lb + L_DUMMY, (blk64 * 64 + lane) * 128, 0);
                else if (blk64 < 344) touch_lines(rsB, lb + L_DUMMY, ((blk64 - 172) * 64 + lane) * 128, 0);
                else if (blk64 < 516) touch_lines(rsCc, lb + L_DUMMY, ((blk64 - 344) * 64 + lane) * 128, 0);
                else if (blk64 < 580) touch_lines(rsE, lb + L_DUMMY, ((blk64 - 516) * 64 + lane) * 128, 0);
            }
            if (w == 3) {
                unsigned o = WS_KVP + (j * 16 + p) * KVP_BYTES + (lane < 17 ? lane : 0) * 128;
                o = lane == 17 ? WS_F1 + (j * 16 + p) * 256 : o;
                o = lane == 18 ? WS_F2 + (p * 16 + j) * 256 : o;
                o = lane == 19 ? WS_F3 + (pP * 16 + jP) * 256 : o;
                o = lane == 20 ? WS_F4 + (p * 16 + j) * 256 : o;
                o = lane == 21 ? WS_F4 + 0x10000 + (p * 16 + j) * 256 : o;
                o = lane == 23 ? WS_O + ((4 * p) * 32 + 2 * j) * 1024 : o;
                o = lane == 24 ? WS_H + ((4 * pP + 2 * half) * 32 + 4 * c) * 1024 : o;
                o = lane == 25 ? WS_ACT + ((4 * p) * 86 + 4 * j) * 1024 : o;
                o = lane == 26 ? WS_ACT + ((4 * p + 3) * 86 + 4 * j) * 1024 : o;
                o = lane == 27 ? WS_HST + ((4 * pP + 2 * half) * 16 + 2 * c) * 256 : o;
                touch_lines(rsW, lb + L_DUMMY, o, 0);
            }
                }
                if (ss == 6) { xs = ld16(rsW, (128 * p + 32 * m + t) * 16, WS_XST); SB(); }
            }
#pragma unroll
            for (int e = 0; e < 2; ++e) {
                if (ss > 0) accA = mfma(fa[1][e], xf[4 * ss - 2 + e], accA);
                if (ss < 8) { xf[4 * ss + e] = *(LAS u32x4*)(lds + (e ? bx1 : bx0)); fa[0][e] = tr2(lds, bA + e * 2048, 256); }
                SB();
                if (ss > 0) accB = mfma(fb[1][e], xf[4 * ss - 2 + e], accB);
                if (ss < 8) fb[0][e] = tr2(lds, bB + e * 2048, 256);
                SB();
            }
            if (ss < 8) {
#pragma unroll
                for (int e = 0; e < 2; ++e) {
                    accA = mfma(fa[0][e], xf[4 * ss + e], accA);
                    xf[4 * ss + 2 + e] = *(LAS u32x4*)(lds + (e ? bx3 : bx2)); fa[1][e] = tr2(lds, bA + (2 + e) * 2048, 256);
                    SB();
                    accB = mfma(fb[0][e], xf[4 * ss + e], accB);
                    fb[1][e] = tr2(lds, bB + (2 + e) * 2048, 256);
                    SB();
                }
                if (ss < 7) {
                    const unsigned d = ss == 5 ? (unsigned)(-5 * (int)SS_BYTES) : SS_BYTES;
                    bA += d; bB += d; bx0 += d; bx1 += d; bx2 += d; bx3 += d;
                    asm volatile("" : "+v"(bA), "+v"(bB), "+v"(bx0), "+v"(bx1), "+v"(bx2), "+v"(bx3));
                }
            }
        }
        STAMP(3);
#pragma unroll
        for (int g = 0; g < 4; ++g) *(LAS f32x4*)(lds + L_XCH + m * 4096 + g * 1024 + lane * 16) = (f32x4){accB[4 * g], accB[4 * g + 1], accB[4 * g + 2], accB[4 * g + 3]};
#pragma unroll 1
        for (unsigned spin = 0; spin < SPIN_CAP; ++spin) {
            if (__all(xs[2] == (unsigned)tag && xs[3] == (unsigned)(tag >> 32))) break;
            __builtin_amdgcn_s_sleep(1);
            xs = ld16(rsW, (128 * p + 32 * m + t) * 16, WS_XST);
        }
        mean1 = asf(xs[0]); rstd1 = asf(xs[1]);
        if (h2 == 0) *(LAS f32x2*)(lds + L_STAT + (32 * m + t) * 8) = (f32x2){mean1, rstd1};
        BAR();
        STAMP(4);
#pragma unroll
        for (int g = 0; g < 4; ++g) {
            const f32x4 cs = *(LAS f32x4*)(lds + L_CS1 + (8 * g + 4 * h2) * 4);
#pragma unroll
            for (int e = 0; e < 4; ++e) { const float y = __builtin_fmaf(-mean1, cs[e], accA[4 * g + e]); yk[4 * g + e] = g < 2 ? y : y * rstd1; }
        }
    } else {
        const unsigned vx0 = (lane >> 3) * 1024 + (((lane & 7) ^ (lane >> 4)) * 16), vx1 = (lane >> 3) * 1024 + (((lane & 7) ^ (4 + (lane >> 4))) * 16);
        const unsigned vwk = (lane >> 2) * 3072 + (lane & 3) * 16, vwv = (lane >> 2) * 3072 + ((lane & 3) ^ 2) * 16;
        const rsrc_t rbX = biased_rsrc(x, 2048 * 512 * 2), rbQ = biased_rsrc(wqkv, 512 * 1536 * 2);
#define ISSUE_SS(ss, v) do { const unsigned sb_ = lb + ((ss) % 6) * SS_BYTES, ks_ = ((ss) + r1) & 7; \
            const unsigned sx_ = (128 * p + 32 * (v)) * 1024 + ks_ * 128, sw_ = (16 * (v) + 64 * ks_) * 3072 + (512 + 32 * j) * 2; \
            dma2(sb_ + 16384 + (v) * 2048, rbQ, vwk, vwv, sw_, sw_ + 1024); \
            dma4(sb_ + (v) * 4096, rbX, rbX, rbX, rbX, vx0, vx1, sx_, sx_ + 8 * 1024, sx_ + 16 * 1024, sx_ + 24 * 1024); } while (0)
        if (lw == 0) {
            const rsrc_t rsP = __builtin_amdgcn_make_buffer_rsrc((void*)wphi, 0, 32 * 256 * 2, 0x00020000);
            dma16(rsP, lb + L_WPHI, lane * 16, j * 1024);
        }
#pragma unroll
        for (int ss = 0; ss < 2; ++ss) ISSUE_SS(ss, lw);
        STAMP(1);
        f32x4 csacc = {};
        const unsigned tw = 16384 + ((lw & 1) ? 1024 + ((lw >> 1) ^ 1) * 32 : (lw >> 1) * 32) + (lane >> 5) * 2048 + (8 * ((lane >> 4) & 1) + q) * 64 + pp * 8;
#define P1WAIT(s) ((s) == 0 ? 6 : 6 * ((((s) + 2 < 7 ? (s) + 2 : 7) - (s))) + ((s) > 4 ? 2 * ((s) - 4) : 0))
#pragma unroll
        for (int ss = 0; ss < 8; ++ss) {
            if (ss == 0) waitv<P1WAIT(0)>();
            if (ss == 1) waitv<P1WAIT(1)>();
            if (ss == 2) waitv<P1WAIT(2)>();
            if (ss == 3) waitv<P1WAIT(3)>();
            if (ss == 4) waitv<P1WAIT(4)>();
            if (ss == 5) waitv<P1WAIT(5)>();
            if (ss == 6) waitv<P1WAIT(6)>();
            if (ss == 7) waitv<P1WAIT(7)>();
            STAMP2_SS(ss);
            BAR();
            if (ss == 0) { const u64 tv = *(LAS u64*)(lds + L_TAG); tag = UNIFORM64(tv); STAMP(2); }
            unsigned bw = (ss % 6) * SS_BYTES + tw;
            asm volatile("" : "+v"(bw));
            const u32x4 f0 = tr2(lds, bw, 256), f1 = tr2(lds, bw + 4096, 256);
            SB();
            if (ss == 0) ISSUE_SS(2, lw);
            if (ss < 5) ISSUE_SS(3 + ss, lw);
            if (ss == 6) {
#pragma unroll
                for (int i = 0; i < 4; ++i) *(LAS u32x4*)(lds + L_VIMG + (4 * lw + i) * 1024 + lane * 16) = ones;
            }
            if (ss >= 4) {
                const int pc = 8 * lw + 2 * (ss - 4);
                const unsigned so = 16 * ((pc + 4 * r1) & 31) * 3072 + 64 * j;
                dma2(lb + L_WQ + pc * 1024, rbQ, vwk, vwk, so, so + 16 * 3072);
            }
            csacc = mfma16(f0, ones, csacc);
            csacc = mfma16(f1, ones, csacc);
            SB();
        }
        if ((lane & 15) == 0) *(LAS f32x4*)(lds + L_CS1 + (lw >> 1) * 128 + (lw & 1) * 64 + (lane >> 4) * 16) = csacc;
        STAMP(3);
        BAR();
        STAMP(4);
        const f32x2 st = *(LAS f32x2*)(lds + L_STAT + (32 * m + t) * 8);
        mean1 = st[0]; rstd1 = st[1];
#pragma unroll
        for (int g = 0; g < 4; ++g) {
            const f32x4 cs = *(LAS f32x4*)(lds + L_CS1 + 128 + (8 * g + 4 * h2) * 4);
            const f32x4 v = *(LAS f32x4*)(lds + L_XCH + m * 4096 + g * 1024 + lane * 16);
#pragma unroll
            for (int e = 0; e < 4; ++e) { const float y = __builtin_fmaf(-mean1, cs[e], v[e]); yk[4 * g + e] = g < 2 ? y : y * rstd1; }
        }
    }
    const unsigned imgA = L_PHI + a * 8192 + (32 * m + t) * 64 + ((h2 ^ ((t >> 1) & 7)) * 8);
    const u32x4 aphi = tr2(lds, L_WPHI + a * 512 + (4 * h2 + q) * 32 + pp * 8, 256);
    {
        const u32x4 kf = {pack2(yk[0], yk[1]), pack2(yk[2], yk[3]), pack2(yk[4], yk[5]), pack2(yk[6], yk[7])};
        const f32x16 zero = {};
        const f32x16 pk = mfma(aphi, kf, zero);
        *(LAS u32x2*)(lds + imgA + (L_VIMG - L_PHI)) = (u32x2){pack2(yk[8], yk[9]), pack2(yk[10], yk[11])};
        *(LAS u32x2*)(lds + (imgA ^ 16) + (L_VIMG - L_PHI)) = (u32x2){pack2(yk[12], yk[13]), pack2(yk[14], yk[15])};
        u32x4 pos, neg;
        dual_softmax(pk, rstd1 * LOG2E, pos, neg);
        *(LAS u32x2*)(lds + imgA) = (u32x2){pos[0], pos[1]};
        *(LAS u32x2*)(lds + (imgA ^ 16)) = (u32x2){pos[2], pos[3]};
        *(LAS u32x2*)(lds + (imgA ^ 32)) = (u32x2){neg[0], neg[1]};
        *(LAS u32x2*)(lds + (imgA ^ 48)) = (u32x2){neg[2], neg[3]};
    }
    if (w >= 4) waitv<0>();
    BAR();
    STAMP(5);
    float yq[8];
    if (w < 4) {
        unsigned tq = L_WQ + trn + blk * 32;
        asm volatile("" : "+v"(tq));
        f32x16 qa = {};
        u32x4 qb[2][4];
#pragma unroll
        for (int s = 0; s < 4; ++s) qb[0][s] = tr2(lds, tq + s * 1024, 256);
        SB();
#pragma unroll
        for (int s = 0; s < 32; ++s) {
            qa = mfma(qb[(s >> 2) & 1][s & 3], xf[s], qa);
            if (s < 28) qb[((s >> 2) + 1) & 1][s & 3] = tr2(lds, tq + (s + 4) * 1024, 256);
            SB();
        }
#pragma unroll
        for (int g = 0; g < 2; ++g) *(LAS f32x4*)(lds + L_XCH + m * 4096 + g * 1024 + lane * 16) = (f32x4){qa[8 + 4 * g], qa[9 + 4 * g], qa[10 + 4 * g], qa[11 + 4 * g]};
        STAMP(6);
        BAR();
        {
            const rsrc_t rbX2 = biased_rsrc(x, 2048 * 512 * 2);
            const unsigned vt = (lane >> 3) * 1024 + (((lane & 7) ^ ((lane >> 3) & 7)) * 16), so = (64 * (2 * pP + half) + 16 * w) * 1024 + 128 * c;
            dma2(lb + L_HRES + w * 2048, rbX2, vt, vt, so, so + 8 * 1024);
        }
#pragma unroll
        for (int g = 0; g < 2; ++g) {
            const f32x4 c0 = *(LAS f32x4*)(lds + L_CSQ + (8 * g + 4 * h2) * 4);
#pragma unroll
            for (int e = 0; e < 4; ++e) yq[4 * g + e] = __builtin_fmaf(-mean1, c0[e], qa[4 * g + e]);
        }
    } else {
        if (lw >= 2) {
            __builtin_amdgcn_s_setprio(3);
            const int ah = lw - 2;
            f32x16 kv = {};
            unsigned aa[2], ab[2];
#pragma unroll
            for (int u = 0; u < 2; ++u) {
                const unsigned R = 8 * h2 + 4 * u + q, sw = (R >> 1) & 7;
                aa[u] = L_PHI + ah * 8192 + R * 64 + (((4 * blk + pp) ^ sw) * 8);
                ab[u] = aa[u] + (L_VIMG - L_PHI);
                asm volatile("" : "+v"(aa[u]), "+v"(ab[u]));
            }
            u32x4 A[8], B[8];
#pragma unroll
            for (int s = 0; s < 8; ++s) {
                const s16x4 a0 = __builtin_amdgcn_ds_read_tr16_b64_v4i16((LAS s16x4*)(lds + aa[0] + s * 1024));
                const s16x4 a1 = __builtin_amdgcn_ds_read_tr16_b64_v4i16((LAS s16x4*)(lds + aa[1] + s * 1024));
                const s16x4 b0 = __builtin_amdgcn_ds_read_tr16_b64_v4i16((LAS s16x4*)(lds + ab[0] + s * 1024));
                const s16x4 b1 = __builtin_amdgcn_ds_read_tr16_b64_v4i16((LAS s16x4*)(lds + ab[1] + s * 1024));
                A[s] = __builtin_bit_cast(u32x4, __builtin_shufflevector(a0, a1, 0, 1, 2, 3, 4, 5, 6, 7));
                B[s] = __builtin_bit_cast(u32x4, __builtin_shufflevector(b0, b1, 0, 1, 2, 3, 4, 5, 6, 7));
                if (s == 3) PIN();
            }
            PIN();
#pragma unroll
            for (int s = 0; s < 8; ++s) kv = mfma(A[s], B[s], kv);
            if (t <= 16) {
                const unsigned base = WS_KVP + (j * 16 + p) * KVP_BYTES + ((ah * 4 + h2) * 17 + t) * 16;
                st16(rsW, (u32x4){pack2(kv[0], kv[1]), pack2(kv[2], kv[3]), pack2(kv[4], kv[5]), pack2(kv[6], kv[7])}, base, 0);
                st16(rsW, (u32x4){pack2(kv[8], kv[9]), pack2(kv[10], kv[11]), pack2(kv[12], kv[13]), pack2(kv[14], kv[15])}, base + 34 * 16, 0);
            }
            WAITV(0);
            arrive_publish(lds, 0, 2, ws, WS_F1 + (j * 16 + p) * 256, tag, lane);
            __builtin_amdgcn_s_setprio(0);
            STAMP(6);
        } else {
            unsigned tq = L_WQ + lw * 32 + (8 * (lane >> 4) + q) * 64 + pp * 8;
            asm volatile("" : "+v"(tq));
            f32x4 qcs = {};
#pragma unroll
            for (int g = 0; g < 4; ++g) {
                u32x4 f[4];
#pragma unroll
                for (int s = 0; s < 4; ++s) f[s] = tr2(lds, tq + (4 * g + s) * 2048, 256);
                PIN();
#pragma unroll
                for (int s = 0; s < 4; ++s) qcs = mfma16(f[s], ones, qcs);
            }
            if ((lane & 15) == 0) *(LAS f32x4*)(lds + L_CSQ + lw * 64 + (lane >> 4) * 16) = qcs;
            STAMP(6);
        }
        BAR();
        {
            const rsrc_t rsO = biased_rsrc(wo, 512 * 512 * 2);
#pragma unroll
            for (int g = 0; g < 4; ++g) {
                const int i = 4 * g + lw;
                const unsigned so = 16 * ((i + r2) & 15) * 1024 + 128 * c;
                dma4(lb + L_X + i * 4096, rsO, rsO, rsO, rsO, vrow, vrow, so, so + 8 * 1024, so + 256 * 1024, so + 264 * 1024);
            }
        }
#pragma unroll
        for (int g = 0; g < 2; ++g) {
            const f32x4 c0 = *(LAS f32x4*)(lds + L_CSQ + (16 + 8 * g + 4 * h2) * 4);
            const f32x4 v = *(LAS f32x4*)(lds + L_XCH + m * 4096 + g * 1024 + lane * 16);
#pragma unroll
            for (int e = 0; e < 4; ++e) yq[4 * g + e] = __builtin_fmaf(-mean1, c0[e], v[e]);
        }
    }
    STAMP(7);
    u32x4 qpos, qneg;
    {
        const u32x4 qf = {pack2(yq[0], yq[1]), pack2(yq[2], yq[3]), pack2(yq[4], yq[5]), pack2(yq[6], yq[7])};
        const f32x16 zero = {};
        const f32x16 pq = mfma(aphi, qf, zero);
        dual_softmax(pq, rstd1 * LOG2E, qpos, qneg);
    }
    STAMP(8);
    unsigned kso[16];
#pragma unroll
    for (int i = 0; i < 16; ++i) { kso[i] = WS_KVP + (j * 16 + ((i + 4 * q4) & 15)) * KVP_BYTES; asm volatile("" : "+s"(kso[i])); }
    if (w == 0) poll_flags<PD1>(ws, WS_F1 + j * 16 * 256, 16, tag, lane);
    BAR();
    STAMP(9);
    {
        const unsigned kb = WS_KVP + j * 16 * KVP_BYTES;
        float lo = 0.f, hi = 0.f;
        unsigned v[16], z[8];
#pragma unroll
        for (int i = 0; i < 16; ++i) v[i] = ld4(rsW, tid * 4, kso[i]);
        if (w == 0) {
#pragma unroll
            for (int i = 0; i < 8; ++i) z[i] = ld4(rsW, (512 + t) * 4 + h2 * 8 * KVP_BYTES, kb + i * KVP_BYTES);
        }
        PIN();
#pragma unroll
        for (int i = 0; i < 16; ++i) { lo += blo(v[i]); hi += bhi(v[i]); }
        if (w == 0) {
            float zl = 0.f, zh = 0.f;
#pragma unroll
            for (int i = 0; i < 8; ++i) { zl += blo(z[i]); zh += bhi(z[i]); }
            zl = halves_add(zl); zh = halves_add(zh);
            if (h2 == 0) *(LAS unsigned*)(lds + L_KVA + (512 + t) * 4) = pack2(zl, zh);
        }
        *(LAS unsigned*)(lds + L_KVA + tid * 4) = pack2(lo, hi);
    }
    BAR();
    STAMP(10);
    const int npass = has3 ? 3 : 2;
    const int m2 = w & 1, kh = (w >> 1) & 1;
    const int T2 = 4 * pP + 2 * half + m2;
    const unsigned C0 = 64 * c + 32 * kh;
    {
        const unsigned col = t < 16 ? t : 16;
        const u32x4 kp = *(LAS u32x4*)(lds + L_KVA + ((a * 4 + h2) * 17 + col) * 16);
        const u32x4 kn = *(LAS u32x4*)(lds + L_KVA + ((a * 4 + 2 + h2) * 17 + col) * 16);
        f32x16 o = {};
        o = mfma(kp, qpos, o);
        o = mfma(kn, qneg, o);
        const float r = __builtin_amdgcn_rcpf(o[8] + 1e-6f);
        st16(rsW, (u32x4){pack2(o[0] * r, o[1] * r), pack2(o[2] * r, o[3] * r), pack2(o[4] * r, o[5] * r), pack2(o[6] * r, o[7] * r)},
             WS_O + ((4 * p + m) * 32 + 2 * j + a) * 1024 + lane * 16, 0);
        WAITV(0);
        BAR();
        if (tid == 0) __hip_atomic_store((GAS u64*)(ws + WS_F2 + (p * 16 + j) * 256), tag, __ATOMIC_RELAXED, __HIP_MEMORY_SCOPE_AGENT);
        SBAR();
    }
    const unsigned trp = (4 * h2 + q) * 64 + blk * 32 + pp * 8;
    const unsigned t128 = (4 * h2 + q) * 128 + (q >> 1) * 64 + blk * 32 + pp * 8;

    if (w >= 4) {
        ISSUE_STAGE(0, L_Y, 0, 4);
        STAMP(11);
    }
    if (w >= 4) {
        const rsrc_t rsD = biased_rsrc(wd, 1376 * 512 * 2);
#define ISSUE_WD(pi, halfbase) do { _Pragma("unroll") for (int g_ = 0; g_ < ((pi) == 2 ? 3 : 4); ++g_) { const int i_ = 4 * g_ + lw; if ((pi) < 2 || i_ < 11) { \
                const int ii_ = (pi) == 2 ? (i_ + r4 >= 11 ? i_ + r4 - 11 : i_ + r4) : ((i_ + r2) & 15); \
                const unsigned sa_ = 16 * (((pi) == 2 ? 64 : 32 * (pi)) + ii_) * 1024 + 128 * c, sb_ = sa_ + 16 * ((pi) == 2 ? 11 : 16) * 1024; \
                dma4(lb + (halfbase) + i_ * 4096, rsD, rsD, rsD, rsD, vrow, vrow, sa_, sa_ + 8 * 1024, sb_, sb_ + 8 * 1024); } } } while (0)
        f32x4 cs0 = {}, cs1 = {};
        const unsigned gam = lane >> 4;
        const unsigned ca0 = (lw >> 1) * 2048 + (gam >> 1) * 4096 + (8 * (gam & 1) + q) * 128 + (((lw & 1) * 64) ^ (64 * (q >> 1))) + pp * 8;
#define COLSUM_STAGE(halfbase) do { unsigned cb_ = (halfbase) + ca0; asm volatile("" : "+v"(cb_)); \
            _Pragma("unroll") for (int g_ = 0; g_ < 4; ++g_) { const u32x4 f0_ = tr2(lds, cb_ + g_ * 16384, 512), f1_ = tr2(lds, cb_ + g_ * 16384 + 32, 512), \
                                                                         f2_ = tr2(lds, cb_ + g_ * 16384 + 8192, 512), f3_ = tr2(lds, cb_ + g_ * 16384 + 8192 + 32, 512); \
                PIN(); cs0 = mfma16(f0_, ones, cs0); cs1 = mfma16(f1_, ones, cs1); cs0 = mfma16(f2_, ones, cs0); cs1 = mfma16(f3_, ones, cs1); } } while (0)
        BAR();
        STAMP(12);
        BAR();
        SBAR();
        ISSUE_STAGE(1, L_X, 0, B_EARLY);
        BAR();
        SBAR();
        ISSUE_STAGE(1, L_X, B_EARLY, 4);
        STAMP(13);
        waitv<16>();
        BAR();
        COLSUM_STAGE(L_Y);
        WAITV(0);
        lsig(lb, 8); lwait(lds, 8, 4);
        COLSUM_STAGE(L_X);
        if ((lane & 15) == 0) {
            *(LAS f32x4*)(lds + L_CS3 + ((lw & 1) * 64 + (lw >> 1) * 32 + 4 * gam) * 4) = cs0;
            *(LAS f32x4*)(lds + L_CS3 + ((lw & 1) * 64 + (lw >> 1) * 32 + 16 + 4 * gam) * 4) = cs1;
        }
        BAR();
        if (npass == 3) {
            ISSUE_UNIT(u3, L_Y);
            WAITV(0);
            SBAR();
            ISSUE_WD(0, L_X);
            {
                unsigned cb = L_Y + (lw >> 1) * 1024 + (gam >> 1) * 2048 + (8 * (gam & 1) + q) * 64 + (lw & 1) * 32 + pp * 8;
                asm volatile("" : "+v"(cb));
                f32x4 c3 = {};
#pragma unroll
                for (int g = 0; g < 4; ++g) {
                    u32x4 f[4];
#pragma unroll
                    for (int e = 0; e < 4; ++e) f[e] = tr2(lds, cb + (4 * g + e) * 4096, 256);
                    PIN();
#pragma unroll
                    for (int e = 0; e < 4; ++e) c3 = mfma16(f[e], ones, c3);
                }
                if ((lane & 15) == 0) *(LAS f32x4*)(lds + L_CS3 + (128 + (lw >> 1) * 32 + (lw & 1) * 16 + 4 * gam) * 4) = c3;
            }
            BAR();
            WAITV(0);
            BAR();
            if (lw == 0) {
                lwait(lds, 11, 4);
                if (lane == 0) __hip_atomic_store((GAS u64*)(ws + WS_F4 + 0x10000 + (p * 16 + j) * 256), tag, __ATOMIC_RELAXED, __HIP_MEMORY_SCOPE_AGENT);
            }
            SBAR();
            ISSUE_WD(1, L_Y);
            WAITV(0);
            BAR();
            if (lw == 0) { poll_flags3(ws, WS_F4 + 0x10000 + pP * 16 * 256, tag, lane); lsig(lb, 14); }
            ISSUE_WD(2, L_X);
            WAITV(0);
            BAR();
        } else {
            ISSUE_WD(0, L_Y);
            SBAR();
            ISSUE_WD(1, L_X);
            WAITV(0);
            BAR();
            SBAR();
            BAR();
            if (lw == 0) { poll_flags3(ws, WS_F4 + 0x10000 + pP * 16 * 256, tag, lane); lsig(lb, 14); }
            ISSUE_WD(2, L_Y);
            WAITV(0);
            BAR();
        }
        BAR();
    } else {
        if (PRIO_C) __builtin_amdgcn_s_setprio(PRIO_C);
        f32x16 keep;
#pragma unroll
        for (int g = 0; g < 4; ++g) {
            const u32x2 v = *(LAS u32x2*)(lds + L_HRES + (32 * m2 + t) * 128 + (((4 * kh + g) ^ (t & 7)) * 16) + 8 * h2);
            keep[4 * g] = blo(v[0]); keep[4 * g + 1] = bhi(v[0]); keep[4 * g + 2] = blo(v[1]); keep[4 * g + 3] = bhi(v[1]);
        }
        asm volatile("" : "+v"(keep));
        STAMP(11);
        if (w == 0) poll_flags<PD2>(ws, WS_F2 + pP * 16 * 256, 16, tag, lane);
        BAR();
        STAMP(12);
        if (b == 0 && w == 1 && lane == 0) __hip_atomic_store((GAS u64*)ws, tag, __ATOMIC_RELAXED, __HIP_MEMORY_SCOPE_AGENT);
        {
            u32x4 hres0, hres1;
            u32x4 of[16];
#pragma unroll
            for (int i = 0; i < 8; ++i) of[i] = ld16(rsW, lane * 16, WS_O + (T2 * 32 + 16 * kh + ((i + r2) & 15)) * 1024);
            PIN();
            f32x16 snd = {};
            gemm2<16, 4096, 1024, 0>(lds, L_X + kh * 2048 + (t128 ^ (64 * kh)), L_X + kh * 2048 + (t128 ^ (64 * (kh ^ 1))), of, keep, snd, [&](int g) {
                if (g < 4) {
#pragma unroll
                    for (int e = 0; e < 2; ++e) of[8 + 2 * g + e] = ld16(rsW, lane * 16, WS_O + (T2 * 32 + 16 * kh + ((8 + 2 * g + e + r2) & 15)) * 1024);
                    SB();
                } });
#pragma unroll
            for (int g = 0; g < 4; ++g) *(LAS f32x4*)(lds + L_XCH + w * 4096 + g * 1024 + lane * 16) = (f32x4){snd[4 * g], snd[4 * g + 1], snd[4 * g + 2], snd[4 * g + 3]};
            STAMP(13);
            BAR();
#pragma unroll
            for (int g = 0; g < 4; ++g) {
                const f32x4 v = *(LAS f32x4*)(lds + L_XCH + (w ^ 2) * 4096 + g * 1024 + lane * 16);
#pragma unroll
                for (int e = 0; e < 4; ++e) keep[4 * g + e] += v[e];
            }
#pragma unroll
            for (int e = 0; e < 4; ++e) { hres0[e] = pack2(keep[2 * e], keep[2 * e + 1]); hres1[e] = pack2(keep[8 + 2 * e], keep[8 + 2 * e + 1]); }
            const unsigned hb = WS_H + (T2 * 32 + 4 * c + 2 * kh) * 1024;
            st16(rsW, hres0, lane * 16, hb);
            st16(rsW, hres1, lane * 16, hb + 1024);
            *(LAS u32x4*)(lds + L_HRES + w * 2048 + lane * 16) = hres0;
            *(LAS u32x4*)(lds + L_HRES + w * 2048 + 1024 + lane * 16) = hres1;
            f32x2 s1 = {0.f, 0.f}, s2 = {0.f, 0.f};
#pragma unroll
            for (int e = 0; e < 8; ++e) { const f32x2 v = {keep[2 * e], keep[2 * e + 1]}; s1 += v; s2 += v * v; }
            float su = s1[0] + s1[1], sq = s2[0] + s2[1];
            su = halves_add(su); sq = halves_add(sq);
            __builtin_amdgcn_raw_buffer_store_b32(asu(h2 ? sq : su), rsW, lane * 4 + WS_HST + (T2 * 16 + 2 * c + kh) * 256, 0, 16);
            SBAR();
            WAITV(0);
            BAR();
            if (tid == 0) __hip_atomic_store((GAS u64*)(ws + WS_F3 + (pP * 16 + jP) * 256), tag, __ATOMIC_RELAXED, __HIP_MEMORY_SCOPE_AGENT);
            SBAR();
        }
        STAMP(14);
        if (w == 0) poll_flags<PD3>(ws, WS_F3 + p * 16 * 256, 16, tag, lane);
        BAR();
        STAMP(15);
        {
            const int T = 4 * p + w;
            u32x4 hf[32];
            float sv[16];
#pragma unroll
            for (int s = 0; s < 8; ++s) hf[s] = ld16(rsW, lane * 16, WS_H + (T * 32 + ((s + r3) & 31)) * 1024);
            PIN();
            float mean, r2;
            auto act_out = [&](const f32x16& ag, const f32x16& au, int cs, unsigned ks) {
                float y[16];
#pragma unroll
                for (int g = 0; g < 4; ++g) {
                    const f32x4 cg = *(LAS f32x4*)(lds + L_CS3 + (cs * 64 + 8 * g + 4 * h2) * 4), cu = *(LAS f32x4*)(lds + L_CS3 + (cs * 64 + 32 + 8 * g + 4 * h2) * 4);
#pragma unroll
                    for (int e = 0; e < 4; ++e) y[4 * g + e] = (__builtin_fmaf(-mean, cg[e], ag[4 * g + e]) * r2) * __builtin_fmaf(-mean, cu[e], au[4 * g + e]);
                }
                const unsigned ab = WS_ACT + (T * 86 + ks) * 1024;
                st16(rsW, (u32x4){pack2(y[0], y[1]), pack2(y[2], y[3]), pack2(y[4], y[5]), pack2(y[6], y[7])}, lane * 16, ab);
                st16(rsW, (u32x4){pack2(y[8], y[9]), pack2(y[10], y[11]), pack2(y[12], y[13]), pack2(y[14], y[15])}, lane * 16, ab + 1024);
            };
            {
                f32x16 g0 = {}, g1 = {}, u0 = {}, u1 = {};
                gemm4<0>(lds, L_Y + t128, L_Y + (t128 ^ 64), hf, g0, g1, u0, u1, [&](int i) {
                    if (i < 8) hf[8 + i] = ld16(rsW, lane * 16, WS_H + (T * 32 + ((8 + i + r3) & 31)) * 1024);
                    else { hf[2 * i] = ld16(rsW, lane * 16, WS_H + (T * 32 + ((2 * i + r3) & 31)) * 1024); hf[2 * i + 1] = ld16(rsW, lane * 16, WS_H + (T * 32 + ((2 * i + 1 + r3) & 31)) * 1024); }
                    if (i < 4) {
#pragma unroll
                        for (int k = 0; k < 4; ++k) sv[4 * i + k] = asf(ld4(rsW, lane * 4, WS_HST + (T * 16 + 4 * i + k) * 256));
                    }
                    if (i == 9) {
                        float tot = 0.f;
#pragma unroll
                        for (int k = 0; k < 16; ++k) tot += sv[k];
                        const u32x2 r = both_halves(tot);
                        mean = asf(r[0]) * (1.0f / 512.0f);
                        const float var = fmaxf(asf(r[1]) * (1.0f / 512.0f) - mean * mean, 0.f);
                        r2 = __builtin_amdgcn_rcpf(var + 1e-5f);
                        asm volatile("" : "+v"(mean), "+v"(r2));
                    }
                    SB(); });
                BAR();
                STAMP(16);
                gemm4<16>(lds, L_X + t128, L_X + (t128 ^ 64), hf, g0, g1, u0, u1, nohook);
                STAMP(17);
                act_out(g0, u0, 0, 4 * j);
                act_out(g1, u1, 1, 4 * j + 2);
                SBAR();
            }
            if (npass == 3) {
                f32x16 ag = {}, au = {};
                gemm2<32, 2048, 512, 0>(lds, L_Y + trp, L_Y + 1024 + trp, hf, ag, au, [&](int g) {
                    if (g == 3) { WAITV(0); arrive_publish(lds, 3, 4, ws, WS_F4 + (p * 16 + j) * 256, tag, lane); PIN(); } });
                BAR();
                STAMP(18);
                act_out(ag, au, 2, 2 * u3);
            } else {
                WAITV(0); arrive_publish(lds, 3, 4, ws, WS_F4 + (p * 16 + j) * 256, tag, lane);
            }
        }
        STAMP(19);
        {
            f32x16 a0, a1 = {};
            {
                const u32x4 hres0 = *(LAS u32x4*)(lds + L_HRES + w * 2048 + lane * 16), hres1 = *(LAS u32x4*)(lds + L_HRES + w * 2048 + 1024 + lane * 16);
#pragma unroll
                for (int e = 0; e < 4; ++e) { a0[2 * e] = blo(hres0[e]); a0[2 * e + 1] = bhi(hres0[e]); a0[8 + 2 * e] = blo(hres1[e]); a0[8 + 2 * e + 1] = bhi(hres1[e]); }
                asm volatile("" : "+v"(a0));
            }
            const unsigned tk = kh * 2048 + (t128 ^ (64 * kh)), ts = kh * 2048 + (t128 ^ (64 * (kh ^ 1)));
            const unsigned h0 = npass == 3 ? L_X : L_Y, h1 = npass == 3 ? L_Y : L_X;
            if (w == 0) poll_flags(ws, WS_F4 + pP * 16 * 256, 16, tag, lane);
            BAR();
            STAMP(20);
            u32x4 af[43];
            auto ld_act = [&](int i) {
                const int ks = i < 32 ? 32 * (i >> 4) + kh * 16 + ((i + r2) & 15) : 64 + kh * 11 + (i - 32 + r4 >= 11 ? i - 32 + r4 - 11 : i - 32 + r4);
                af[i] = ld16(rsW, lane * 16, WS_ACT + (T2 * 86 + ks) * 1024); };
            if (npass == 3) { WAITV(0); lsig(lb, 11); }
#pragma unroll
            for (int i = 0; i < 8; ++i) ld_act(i);
            SBAR();
            gemm2<16, 4096, 1024, 0>(lds, h0 + tk, h0 + ts, af, a0, a1, [&](int g) { ld_act(8 + 2 * g); ld_act(9 + 2 * g); SB(); });
            STAMP(21);
            BAR();
            STAMP(22);
            gemm2<16, 4096, 1024, 16>(lds, h1 + tk, h1 + ts, af, a0, a1, [&](int g) {
                if (g == 4) lwait(lds, 14, 1);
                ld_act(24 + 2 * g); ld_act(25 + 2 * g); SB(); });
            BAR();
            STAMP(23);
            gemm2<11, 4096, 1024, 32>(lds, h0 + tk, h0 + ts, af, a0, a1, [&](int g) { if (g < 2) { ld_act(40 + 2 * g < 43 ? 40 + 2 * g : 42); if (g == 0) ld_act(41); SB(); } });
#pragma unroll
            for (int g = 0; g < 4; ++g) *(LAS f32x4*)(lds + L_XCH + w * 4096 + g * 1024 + lane * 16) = (f32x4){a1[4 * g], a1[4 * g + 1], a1[4 * g + 2], a1[4 * g + 3]};
            BAR();
            STAMP(24);
#pragma unroll
            for (int g = 0; g < 4; ++g) {
                const f32x4 v = *(LAS f32x4*)(lds + L_XCH + (w ^ 2) * 4096 + g * 1024 + lane * 16);
#pragma unroll
                for (int e = 0; e < 4; ++e) a0[4 * g + e] += v[e];
            }
            u32x4 o0, o1;
#pragma unroll
            for (int e = 0; e < 4; ++e) { o0[e] = pack2(a0[2 * e], a0[2 * e + 1]); o1[e] = pack2(a0[8 + 2 * e], a0[8 + 2 * e + 1]); }
            { auto r = __builtin_amdgcn_permlane32_swap(o0[0], o0[2], false, false); o0[0] = r[0]; o0[2] = r[1];
              r = __builtin_amdgcn_permlane32_swap(o0[1], o0[3], false, false); o0[1] = r[0]; o0[3] = r[1];
              r = __builtin_amdgcn_permlane32_swap(o1[0], o1[2], false, false); o1[0] = r[0]; o1[2] = r[1];
              r = __builtin_amdgcn_permlane32_swap(o1[1], o1[3], false, false); o1[1] = r[0]; o1[3] = r[1]; }
            const rsrc_t rsOut = __builtin_amdgcn_make_buffer_rsrc((void*)out, 0, 2048 * 512 * 2, 0x00020000);
            int tz = threadIdx.x; asm volatile("" : "+v"(tz));
            const unsigned vz = (32 * T2 + (tz & 31)) * 1024 + (C0 + 8 * ((tz >> 5) & 1)) * 2;
            __builtin_amdgcn_raw_buffer_store_b128(o0, rsOut, vz, 0, 0);
            __builtin_amdgcn_raw_buffer_store_b128(o1, rsOut, vz + 32, 0, 0);
        }
        STAMP(25); CSTAMP(31);
    }
}

extern "C" void kernel_launch(void* const* d_in, const int* in_sizes, int n_in, void* d_out, int out_size, void* d_ws, size_t ws_size, hipStream_t stream) {
    (void)in_sizes; (void)n_in; (void)out_size; (void)ws_size;
    (void)hipFuncSetAttribute((const void*)hedgehog_block, hipFuncAttributeMaxDynamicSharedMemorySize, LDS_BYTES);
    hedgehog_block<<<256, 512, LDS_BYTES, stream>>>((const bf16_t*)d_in[0], (const bf16_t*)d_in[1], (const bf16_t*)d_in[2], (const bf16_t*)d_in[3],
                                                    (const bf16_t*)d_in[5], (const bf16_t*)d_in[6], (const bf16_t*)d_in[7], (bf16_t*)d_out, (char*)d_ws);
}
```

```cpp
#include <hip/hip_runtime.h>
#include <hip/hip_bf16.h>

#define LAS __attribute__((address_space(3)))
#define GAS __attribute__((address_space(1)))
#define DI __device__ __forceinline__
typedef unsigned short bf16_t;
typedef unsigned u32x4 __attribute__((ext_vector_type(4)));
typedef unsigned u32x2 __attribute__((ext_vector_type(2)));
typedef short s16x4 __attribute__((ext_vector_type(4)));
typedef __bf16 bf16x8 __attribute__((ext_vector_type(8)));
typedef __bf16 bf16x2 __attribute__((ext_vector_type(2)));
typedef float f32x16 __attribute__((ext_vector_type(16)));
typedef float f32x4 __attribute__((ext_vector_type(4)));
typedef float f32x2 __attribute__((ext_vector_type(2)));
typedef unsigned long long u64;
typedef __amdgpu_buffer_rsrc_t rsrc_t;

constexpr unsigned WS_F1 = 0x10000, WS_F2 = 0x20000, WS_F3 = 0x30000, WS_F4 = 0x40000;
constexpr unsigned WS_KVP = 0x80000, KVP_BYTES = 2176;
constexpr unsigned WS_HST = 0x110000;
constexpr unsigned WS_XST = 0x160000;
constexpr unsigned WS_O = 0x200000, WS_H = 0x400000, WS_ACT = 0x600000, WS_BYTES = 0x1000000;
constexpr unsigned L_X = 0, L_Y = 65536, L_XCH = 131072, L_MISC = 147456;
constexpr unsigned L_STAT = L_MISC, L_CS1 = L_MISC + 1024, L_CSQ = L_MISC + 1280, L_WPHI = L_MISC + 1536, L_KVA = L_MISC + 2560, L_CS3 = L_MISC + 4736, L_CNT = L_MISC + 5504, L_TAG = L_MISC + 6016, L_DUMMY = L_MISC + 6144, L_HRES = L_MISC + 8192;
constexpr unsigned L_WQ = 65536, L_PHI = 98304, L_VIMG = 114688  , SS_BYTES = 24576;
constexpr unsigned LDS_BYTES = 163840;
constexpr unsigned SPIN_CAP = 40000;
constexpr float LOG2E = 1.4426950408889634f;

DI float asf(unsigned v) { return __builtin_bit_cast(float, v); }
DI unsigned asu(float v) { return __builtin_bit_cast(unsigned, v); }
DI unsigned pack2(float lo, float hi) { bf16x2 v = {(__bf16)lo, (__bf16)hi}; return __builtin_bit_cast(unsigned, v); }
DI float blo(unsigned v) { return __builtin_bit_cast(float, v << 16); }
DI float bhi(unsigned v) { return __builtin_bit_cast(float, v & 0xffff0000u); }
DI f32x16 mfma(u32x4 a, u32x4 b, f32x16 c) { return __builtin_amdgcn_mfma_f32_32x32x16_bf16(__builtin_bit_cast(bf16x8, a), __builtin_bit_cast(bf16x8, b), c, 0, 0, 0); }
DI f32x4 mfma16(u32x4 a, u32x4 b, f32x4 c) { return __builtin_amdgcn_mfma_f32_16x16x32_bf16(__builtin_bit_cast(bf16x8, a), __builtin_bit_cast(bf16x8, b), c, 0, 0, 0); }
DI u32x4 tr2(LAS char* lds, unsigned off, unsigned second) {
    const s16x4 lo = __builtin_amdgcn_ds_read_tr16_b64_v4i16((LAS s16x4*)(lds + off));
    const s16x4 hi = __builtin_amdgcn_ds_read_tr16_b64_v4i16((LAS s16x4*)(lds + off + second));
    return __builtin_bit_cast(u32x4, __builtin_shufflevector(lo, hi, 0, 1, 2, 3, 4, 5, 6, 7));
}
DI void dma16(const rsrc_t rs, unsigned ldsaddr, unsigned voff, unsigned soff) {
    asm volatile("s_mov_b32 m0, %0\n\ts_nop 0\n\tbuffer_load_dwordx4 %1, %2, %3 offen lds" ::"s"(ldsaddr), "v"(voff), "s"(rs), "s"(soff) : "memory");
}
constexpr unsigned BIAS = 4096;
DI rsrc_t biased_rsrc(const void* p, unsigned bytes) { return __builtin_amdgcn_make_buffer_rsrc((void*)((const char*)p - BIAS), 0, bytes + BIAS, 0x00020000); }
DI void dma4(unsigned ldsaddr, const rsrc_t ra, const rsrc_t rb, const rsrc_t rc, const rsrc_t rd, unsigned va, unsigned vb, unsigned s0, unsigned s1, unsigned s2, unsigned s3) {
    asm volatile("s_mov_b32 m0, %0\n\ts_nop 0\n\t"
                 "buffer_load_dwordx4 %5, %1, %7 offen lds\n\tbuffer_load_dwordx4 %6, %2, %8 offen offset:1024 lds\n\t"
                 "buffer_load_dwordx4 %5, %3, %9 offen offset:2048 lds\n\tbuffer_load_dwordx4 %6, %4, %10 offen offset:3072 lds"
                 ::"s"(ldsaddr), "s"(ra), "s"(rb), "s"(rc), "s"(rd), "v"(va), "v"(vb), "s"(s0 + BIAS), "s"(s1 + BIAS - 1024), "s"(s2 + BIAS - 2048), "s"(s3 + BIAS - 3072) : "memory");
}
DI void dma2(unsigned ldsaddr, const rsrc_t r, unsigned va, unsigned vb, unsigned s0, unsigned s1) {
    asm volatile("s_mov_b32 m0, %0\n\ts_nop 0\n\tbuffer_load_dwordx4 %2, %1, %4 offen lds\n\tbuffer_load_dwordx4 %3, %1, %5 offen offset:1024 lds"
                 ::"s"(ldsaddr), "s"(r), "v"(va), "v"(vb), "s"(s0 + BIAS), "s"(s1 + BIAS - 1024) : "memory");
}
#define WAITV(n) asm volatile("s_waitcnt vmcnt(" #n ")" ::: "memory")
#define PIN() do { asm volatile("" ::: "memory"); __builtin_amdgcn_sched_barrier(0); } while (0)
template <int N> DI void waitv() { asm volatile("s_waitcnt vmcnt(%0)" ::"n"(N) : "memory"); }
constexpr int K0 = 3;
constexpr int B_EARLY = 1;
constexpr int PD = 4;
#define SBAR() asm volatile("s_barrier" ::: "memory")
#define BAR() asm volatile("s_waitcnt lgkmcnt(0)\n\ts_barrier" ::: "memory")
DI u32x2 both_halves(float v) { const unsigned a = asu(v); const auto r = __builtin_amdgcn_permlane32_swap(a, a, false, false); return (u32x2){r[0], r[1]}; }
DI float halves_add(float v) { const u32x2 r = both_halves(v); return asf(r[0]) + asf(r[1]); }
DI float halves_max(float v) { const u32x2 r = both_halves(v); return fmaxf(asf(r[0]), asf(r[1])); }
DI float halves_min(float v) { const u32x2 r = both_halves(v); return fminf(asf(r[0]), asf(r[1])); }
DI float dpp_ror4(float v) { return asf(__builtin_amdgcn_update_dpp(0, (int)asu(v), 0x124, 0xf, 0xf, true)); }
DI float dpp_ror8(float v) { return asf(__builtin_amdgcn_update_dpp(0, (int)asu(v), 0x128, 0xf, 0xf, true)); }
DI float rows16_add(float v) { const unsigned a = asu(v); const auto r = __builtin_amdgcn_permlane16_swap(a, a, false, false); return asf(r[0]) + asf(r[1]); }
DI float stride4_add(float v) { v += dpp_ror4(v); v += dpp_ror8(v); v = rows16_add(v); return halves_add(v); }
DI float quad_add(float v) {
    v += __builtin_bit_cast(float, __builtin_amdgcn_update_dpp(0, __builtin_bit_cast(int, v), 0xB1, 0xf, 0xf, true));
    v += __builtin_bit_cast(float, __builtin_amdgcn_update_dpp(0, __builtin_bit_cast(int, v), 0x4E, 0xf, 0xf, true));
    return v;
}
DI void st16(const rsrc_t rs, u32x4 v, unsigned voff, unsigned soff) { __builtin_amdgcn_raw_buffer_store_b128(v, rs, voff + soff, 0, 16); }
DI u32x4 ld16(const rsrc_t rs, unsigned voff, unsigned soff) { return __builtin_amdgcn_raw_buffer_load_b128(rs, voff, soff, 16); }
DI unsigned ld4(const rsrc_t rs, unsigned voff, unsigned soff) { return __builtin_amdgcn_raw_buffer_load_b32(rs, voff, soff, 16); }

DI void poll_flags3(const char* ws, unsigned off, u64 tag, int lane) {
    const int l = lane < 11 ? lane : 10;
    const GAS u64* f = (const GAS u64*)(ws + off) + (l < 6 ? l : l + 2) * 32;
#pragma unroll 1
    for (unsigned spin = 0; spin < SPIN_CAP; ++spin) {
        const u64 v = __hip_atomic_load(f, __ATOMIC_RELAXED, __HIP_MEMORY_SCOPE_AGENT);
        if (__all(v == tag)) break;
        __builtin_amdgcn_s_sleep(1);
    }
    asm volatile("" ::: "memory");
}
DI void nohook(int) {}
template <int DELAY = 0>
DI void poll_flags(const char* ws, unsigned off, int n, u64 tag, int lane) {
#pragma unroll
    for (int i = 0; i < DELAY; ++i) __builtin_amdgcn_s_sleep(2);
    const GAS u64* f = (const GAS u64*)(ws + off) + (lane < n ? lane : n - 1) * 32;
#pragma unroll 1
    for (unsigned spin = 0; spin < SPIN_CAP; ++spin) {
        const u64 v = __hip_atomic_load(f, __ATOMIC_RELAXED, __HIP_MEMORY_SCOPE_AGENT);
        if (__all(v == tag)) break;
        __builtin_amdgcn_s_sleep(1);
    }
    asm volatile("" ::: "memory");
}
DI void arrive_publish(LAS char* lds, int cnt, unsigned n, char* ws, unsigned flag_off, u64 tag, int lane) {
    if (lane == 0) {
        const unsigned old = __hip_atomic_fetch_add((LAS unsigned*)(lds + L_CNT) + cnt, 1u, __ATOMIC_RELAXED, __HIP_MEMORY_SCOPE_WORKGROUP);
        if (old == n - 1) __hip_atomic_store((GAS u64*)(ws + flag_off), tag, __ATOMIC_RELAXED, __HIP_MEMORY_SCOPE_AGENT);
    }
}
DI void lsig(unsigned lb, int idx) {
    const unsigned a = lb + L_CNT + idx * 4, one = 1;
    asm volatile("s_mov_b64 exec, 1\n\tds_add_u32 %0, %1\n\ts_mov_b64 exec, -1" ::"v"(a), "v"(one) : "memory");
}
DI void lwait(LAS char* lds, int idx, unsigned n) {
    const volatile LAS unsigned* c = (const volatile LAS unsigned*)(lds + L_CNT) + idx;
#pragma unroll 1
    for (unsigned spin = 0; spin < 64 * SPIN_CAP; ++spin) {
        if ((unsigned)__builtin_amdgcn_readfirstlane(*c) >= n) break;
        __builtin_amdgcn_s_sleep(1);
    }
    asm volatile("" ::: "memory");
}
DI void dual_softmax(const f32x16& p, float sl, u32x4& pos, u32x4& neg) {
    float mx = fmaxf(fmaxf(fmaxf(p[0], p[1]), fmaxf(p[2], p[3])), fmaxf(fmaxf(p[4], p[5]), fmaxf(p[6], p[7])));
    float mn = fminf(fminf(fminf(p[0], p[1]), fminf(p[2], p[3])), fminf(fminf(p[4], p[5]), fminf(p[6], p[7])));
    mx = halves_max(mx); mn = halves_min(mn);
    const float cx = -mx * sl, cn = mn * sl;
    f32x2 ep[4], en[4];
#pragma unroll
    for (int i = 0; i < 4; ++i) {
        const f32x2 v = {p[2 * i], p[2 * i + 1]};
        const f32x2 u = v * sl + cx, d = v * (-sl) + cn;
        ep[i] = (f32x2){__builtin_amdgcn_exp2f(u[0]), __builtin_amdgcn_exp2f(u[1])};
        en[i] = (f32x2){__builtin_amdgcn_exp2f(d[0]), __builtin_amdgcn_exp2f(d[1])};
    }
    const f32x2 sp = (ep[0] + ep[1]) + (ep[2] + ep[3]), sn = (en[0] + en[1]) + (en[2] + en[3]);
    const float rp = __builtin_amdgcn_rcpf(halves_add(sp[0] + sp[1])), rn = __builtin_amdgcn_rcpf(halves_add(sn[0] + sn[1]));
#pragma unroll
    for (int i = 0; i < 4; ++i) { const f32x2 a = ep[i] * rp, b = en[i] * rn; pos[i] = pack2(a[0], a[1]); neg[i] = pack2(b[0], b[1]); }
}

#define SB() __builtin_amdgcn_sched_barrier(0)
template <int N, int STRIDE, int SECOND, int OFF, int NA, class Hook>
DI void gemm2(LAS char* lds, unsigned b0, unsigned b1, const u32x4 (&act)[NA], f32x16& acc0, f32x16& acc1, Hook hook) {
    u32x4 fb[2][4];
    asm volatile("" : "+v"(b0), "+v"(b1));
    constexpr int NG = (N + 1) / 2;
#pragma unroll
    for (int e = 0; e < 2; ++e) { fb[0][2 * e] = tr2(lds, b0 + e * STRIDE, SECOND); fb[0][2 * e + 1] = tr2(lds, b1 + e * STRIDE, SECOND); }
    SB();
#pragma unroll
    for (int g = 0; g < NG; ++g) {
#pragma unroll
        for (int e = 0; e < 2; ++e) {
            const int i = 2 * g + e, n = i + 2;
            if (i < N) acc0 = mfma(fb[g & 1][2 * e], act[OFF + i], acc0);
            if (n < N) fb[(g + 1) & 1][2 * e] = tr2(lds, b0 + n * STRIDE, SECOND);
            SB();
            if (i < N) acc1 = mfma(fb[g & 1][2 * e + 1], act[OFF + i], acc1);
            if (n < N) fb[(g + 1) & 1][2 * e + 1] = tr2(lds, b1 + n * STRIDE, SECOND);
            SB();
        }
        hook(g);
    }
    asm volatile("" : "+v"(acc0), "+v"(acc1));
}
template <int OFF, class Hook>
DI void gemm4(LAS char* lds, unsigned b0, unsigned b1, const u32x4 (&act)[32], f32x16& g0, f32x16& g1, f32x16& u0, f32x16& u1, Hook hook) {
    u32x4 fb[2][4];
    asm volatile("" : "+v"(b0), "+v"(b1));
    fb[0][0] = tr2(lds, b0, 1024); fb[0][1] = tr2(lds, b1, 1024); fb[0][2] = tr2(lds, b0 + 2048, 1024); fb[0][3] = tr2(lds, b1 + 2048, 1024);
    SB();
#pragma unroll
    for (int i = 0; i < 16; ++i) {
        const int o = (i + 1) * 4096;
        g0 = mfma(fb[i & 1][0], act[OFF + i], g0);
        if (i < 15) fb[(i + 1) & 1][0] = tr2(lds, b0 + o, 1024);
        SB();
        g1 = mfma(fb[i & 1][1], act[OFF + i], g1);
        if (i < 15) fb[(i + 1) & 1][1] = tr2(lds, b1 + o, 1024);
        SB();
        u0 = mfma(fb[i & 1][2], act[OFF + i], u0);
        if (i < 15) fb[(i + 1) & 1][2] = tr2(lds, b0 + o + 2048, 1024);
        SB();
        u1 = mfma(fb[i & 1][3], act[OFF + i], u1);
        if (i < 15) fb[(i + 1) & 1][3] = tr2(lds, b1 + o + 2048, 1024);
        SB();
        hook(i);
    }
    asm volatile("" : "+v"(g0), "+v"(g1), "+v"(u0), "+v"(u1));
}
DI void touch_lines(const rsrc_t rs, unsigned ldsaddr, unsigned voff, unsigned soff) {
    asm volatile("s_mov_b32 m0, %0\n\ts_nop 0\n\tbuffer_load_dword %1, %2, %3 offen lds" ::"s"(ldsaddr), "v"(voff), "s"(rs), "s"(soff) : "memory");
}


__global__ void __launch_bounds__(512, 2)
hedgehog_block(const bf16_t* __restrict__ x, const bf16_t* __restrict__ wqkv, const bf16_t* __restrict__ wphi, const bf16_t* __restrict__ wo,
               const bf16_t* __restrict__ wg, const bf16_t* __restrict__ wu, const bf16_t* __restrict__ wd, bf16_t* __restrict__ out, char* __restrict__ ws) {
    extern __shared__ __attribute__((aligned(16))) char smem[];
    LAS char* lds = (LAS char*)smem;
    const unsigned lb = (unsigned)(size_t)lds;
    const int tid = threadIdx.x, lane = tid & 63;
    const int w = __builtin_amdgcn_readfirstlane(tid >> 6);
    const int b = blockIdx.x, xcd = b & 7, slot = b >> 3;
    const int p = 4 * (xcd >> 1) + (slot >> 3), j = 8 * (xcd & 1) + (slot & 7);
    const int pP = 2 * xcd + (slot >> 4), jP = slot & 15, c = slot & 7, half = (slot >> 3) & 1;
    const bool has3 = (j & 7) < (j < 8 ? 6 : 5);
    const int u3 = 32 + (j < 8 ? j : j - 2);
    const int t = lane & 31, h2 = lane >> 5, q = (lane & 15) >> 2, pp = lane & 3, blk = (lane >> 4) & 1;
    const int m = w & 3, a = w >> 2, lw = w & 3;
    const int q4 = slot >> 3;
    const int r1 = 4 * (q4 & 1), r2 = (2 * c + 4 * q4) & 15, r3 = (4 * c + 8 * q4) & 31, r4 = (c + 3 * q4) % 11;
    const rsrc_t rsX = __builtin_amdgcn_make_buffer_rsrc((void*)x, 0, 2048 * 512 * 2, 0x00020000);
    const rsrc_t rsW = __builtin_amdgcn_make_buffer_rsrc((void*)ws, 0, WS_BYTES, 0x00020000);
    u64 tag = 0;
#define UNIFORM64(v) (((u64)(unsigned)__builtin_amdgcn_readfirstlane((unsigned)((v) >> 32)) << 32) | (unsigned)__builtin_amdgcn_readfirstlane((unsigned)(v)))
    const u32x4 ones = {0x3F803F80u, 0x3F803F80u, 0x3F803F80u, 0x3F803F80u};
    const unsigned c128 = ((lane & 7) ^ (4 * ((lane >> 4) & 1))) * 16;
    const unsigned vrow = (lane >> 3) * 1024 + c128;
    const unsigned trn = (8 * h2 + q) * 64 + pp * 8;

    const rsrc_t rsG = biased_rsrc(wg, 512 * 1376 * 2), rsU = biased_rsrc(wu, 512 * 1376 * 2);
    const unsigned vgu = (lane >> 2) * 2752 + (lane & 3) * 16, vgu8 = (lane >> 3) * 2752 + c128;
#define ISSUE_STAGE(st, halfbase, g0, g1) do { _Pragma("unroll") for (int g_ = (g0); g_ < (g1); ++g_) { const int i_ = 4 * g_ + lw; const unsigned so_ = 16 * ((16 * (st) + i_ + r3) & 31) * 2752 + 128 * j; \
            dma4(lb + (halfbase) + i_ * 4096, rsG, rsG, rsU, rsU, vgu8, vgu8, so_, so_ + 8 * 2752, so_, so_ + 8 * 2752); } } while (0)
#define ISSUE_UNIT(u, halfbase) do { _Pragma("unroll") for (int g_ = 0; g_ < 4; ++g_) { const int s_ = 2 * (4 * g_ + lw); \
            const unsigned sa_ = 16 * ((s_ + r3) & 31) * 2752 + 64 * (u), sb_ = 16 * ((s_ + 1 + r3) & 31) * 2752 + 64 * (u); \
            dma4(lb + (halfbase) + s_ * 2048, rsG, rsU, rsG, rsU, vgu, vgu, sa_, sa_, sb_, sb_); } } while (0)
    if (w == 3) {
        u64 pc; asm volatile("s_getpc_b64 %0" : "=s"(pc));
        const rsrc_t rsC = __builtin_amdgcn_make_buffer_rsrc((void*)(pc & ~127ull), 0, 36864, 0x00020000);
        touch_lines(rsC, lb + L_DUMMY, (slot * 9 + (lane < 9 ? lane : 0)) * 128, 0);
    }
    u32x4 xf[32];
    float yk[16];
    float mean1, rstd1;
    if (w < 4) {
        {
            u64 epoch = __hip_atomic_load((const GAS u64*)ws, __ATOMIC_RELAXED, __HIP_MEMORY_SCOPE_AGENT);
            const unsigned R = 128 * p + 8 * j + 2 * w;
            const u32x4 v0 = __builtin_amdgcn_raw_buffer_load_b128(rsX, lane * 16, R * 1024, 0), v1 = __builtin_amdgcn_raw_buffer_load_b128(rsX, lane * 16, R * 1024 + 1024, 0);
            if (tid < 16) *(LAS unsigned*)(lds + L_CNT + tid * 4) = 0u;
            f32x2 a1 = {0.f, 0.f}, a2 = {0.f, 0.f}, b1 = {0.f, 0.f}, b2 = {0.f, 0.f};
#pragma unroll
            for (int e = 0; e < 4; ++e) {
                const f32x2 z0 = {blo(v0[e]), bhi(v0[e])}, z1 = {blo(v1[e]), bhi(v1[e])};
                a1 += z0; a2 += z0 * z0; b1 += z1; b2 += z1 * z1;
            }
            const float su0 = stride4_add(quad_add(a1[0] + a1[1])), sq0 = stride4_add(quad_add(a2[0] + a2[1]));
            const float su1 = stride4_add(quad_add(b1[0] + b1[1])), sq1 = stride4_add(quad_add(b2[0] + b2[1]));
            const float su = (lane & 1) ? su1 : su0, sq = (lane & 1) ? sq1 : sq0;
            const float mean = su * (1.0f / 512.0f), var = fmaxf(sq * (1.0f / 512.0f) - mean * mean, 0.f);
            const float rstd = __builtin_amdgcn_rsqf(var + 1e-5f);
            asm volatile("s_waitcnt vmcnt(0)" : "+v"(epoch)::"memory");
            tag = UNIFORM64(epoch) + 0x9E3779B97F4A7C15ull;
            if (tid == 0) *(LAS u64*)(lds + L_TAG) = tag;
            if (lane < 2) st16(rsW, (u32x4){asu(mean), asu(rstd), (unsigned)tag ^ asu(mean), (unsigned)(tag >> 32) ^ asu(rstd)}, (R + lane) * 16, WS_XST);
        }
        const unsigned xa0 = (32 * m + t) * 128 + ((h2 ^ ((t >> 1) & 7)) * 16);
        const unsigned twA = 16384 + (blk ? 1024 + 32 : 0) + trn, twB = 16384 + (blk ? 1024 : 32) + trn;
        f32x16 accA = {}, accB = {};
        u32x4 fa[2][2], fb[2][2], xs;
        unsigned bA = twA, bB = twB, bx0 = xa0, bx1 = xa0 ^ 32, bx2 = xa0 ^ 64, bx3 = xa0 ^ 96;
        asm volatile("" : "+v"(bA), "+v"(bB), "+v"(bx0), "+v"(bx1), "+v"(bx2), "+v"(bx3));
#pragma unroll
        for (int ss = 0; ss <= 8; ++ss) {
            if (ss < 8) {
                BAR();
                if (ss == 2) {
                    if (w < 3) {
                        const rsrc_t rsA = __builtin_amdgcn_make_buffer_rsrc((void*)wg, 0, 512 * 1376 * 2, 0x00020000), rsB = __builtin_amdgcn_make_buffer_rsrc((void*)wu, 0, 512 * 1376 * 2, 0x00020000);
                        const rsrc_t rsCc = __builtin_amdgcn_make_buffer_rsrc((void*)wd, 0, 512 * 1376 * 2, 0x00020000), rsE = __builtin_amdgcn_make_buffer_rsrc((void*)wo, 0, 512 * 512 * 2, 0x00020000);
                        const unsigned blk64 = b * 3 + w;
                        if (blk64 < 172) touch_lines(rsA, lb + L_DUMMY, (blk64 * 64 + lane) * 128, 0);
                        else if (blk64 < 344) touch_lines(rsB, lb + L_DUMMY, ((blk64 - 172) * 64 + lane) * 128, 0);
                        else if (blk64 < 516) touch_lines(rsCc, lb + L_DUMMY, ((blk64 - 344) * 64 + lane) * 128, 0);
                        else if (blk64 < 580) touch_lines(rsE, lb + L_DUMMY, ((blk64 - 516) * 64 + lane) * 128, 0);
                    }
                    if (w == 3) {
                        unsigned o = WS_KVP + (j * 16 + p) * KVP_BYTES + (lane < 17 ? lane : 0) * 128;
                        o = lane == 17 ? WS_F1 + (j * 16 + p) * 256 : o;
                        o = lane == 18 ? WS_F2 + (p * 16 + j) * 256 : o;
                        o = lane == 19 ? WS_F3 + (pP * 16 + jP) * 256 : o;
                        o = lane == 20 ? WS_F4 + (p * 16 + j) * 256 : o;
                        o = lane == 21 ? WS_F4 + 0x10000 + (p * 16 + j) * 256 : o;
                        o = lane == 23 ? WS_O + ((4 * p) * 32 + 2 * j) * 1024 : o;
                        o = lane == 24 ? WS_H + ((4 * pP + 2 * half) * 32 + 4 * c) * 1024 : o;
                        o = lane == 25 ? WS_ACT + ((4 * p) * 86 + 4 * j) * 1024 : o;
                        o = lane == 26 ? WS_ACT + ((4 * p + 3) * 86 + 4 * j) * 1024 : o;
                        o = lane == 27 ? WS_HST + ((4 * pP + 2 * half) * 16 + 2 * c) * 256 : o;
                        touch_lines(rsW, lb + L_DUMMY, o, 0);
                    }
                }
                if (ss == 6) { xs = ld16(rsW, (128 * p + 32 * m + t) * 16, WS_XST); SB(); }
            }
#pragma unroll
            for (int e = 0; e < 2; ++e) {
                if (ss > 0) accA = mfma(fa[1][e], xf[4 * ss - 2 + e], accA);
                if (ss < 8) { xf[4 * ss + e] = *(LAS u32x4*)(lds + (e ? bx1 : bx0)); fa[0][e] = tr2(lds, bA + e * 2048, 256); }
                SB();
                if (ss > 0) accB = mfma(fb[1][e], xf[4 * ss - 2 + e], accB);
                if (ss < 8) fb[0][e] = tr2(lds, bB + e * 2048, 256);
                SB();
            }
            if (ss < 8) {
#pragma unroll
                for (int e = 0; e < 2; ++e) {
                    accA = mfma(fa[0][e], xf[4 * ss + e], accA);
                    xf[4 * ss + 2 + e] = *(LAS u32x4*)(lds + (e ? bx3 : bx2)); fa[1][e] = tr2(lds, bA + (2 + e) * 2048, 256);
                    SB();
                    accB = mfma(fb[0][e], xf[4 * ss + e], accB);
                    fb[1][e] = tr2(lds, bB + (2 + e) * 2048, 256);
                    SB();
                }
                if (ss < 7) {
                    const unsigned d = ss == 5 ? (unsigned)(-5 * (int)SS_BYTES) : SS_BYTES;
                    bA += d; bB += d; bx0 += d; bx1 += d; bx2 += d; bx3 += d;
                    asm volatile("" : "+v"(bA), "+v"(bB), "+v"(bx0), "+v"(bx1), "+v"(bx2), "+v"(bx3));
                }
            }
        }
#pragma unroll
        for (int g = 0; g < 4; ++g) *(LAS f32x4*)(lds + L_XCH + m * 4096 + g * 1024 + lane * 16) = (f32x4){accB[4 * g], accB[4 * g + 1], accB[4 * g + 2], accB[4 * g + 3]};
#pragma unroll 1
        for (unsigned spin = 0; spin < SPIN_CAP; ++spin) {
            if (__all((xs[2] ^ xs[0]) == (unsigned)tag && (xs[3] ^ xs[1]) == (unsigned)(tag >> 32))) break;
            __builtin_amdgcn_s_sleep(1);
            xs = ld16(rsW, (128 * p + 32 * m + t) * 16, WS_XST);
        }
        mean1 = asf(xs[0]); rstd1 = asf(xs[1]);
        if (h2 == 0) *(LAS f32x2*)(lds + L_STAT + (32 * m + t) * 8) = (f32x2){mean1, rstd1};
        BAR();
#pragma unroll
        for (int g = 0; g < 4; ++g) {
            const f32x4 cs = *(LAS f32x4*)(lds + L_CS1 + (8 * g + 4 * h2) * 4);
#pragma unroll
            for (int e = 0; e < 4; ++e) { const float y = __builtin_fmaf(-mean1, cs[e], accA[4 * g + e]); yk[4 * g + e] = g < 2 ? y : y * rstd1; }
        }
    } else {
        const unsigned vx0 = (lane >> 3) * 1024 + (((lane & 7) ^ (lane >> 4)) * 16), vx1 = (lane >> 3) * 1024 + (((lane & 7) ^ (4 + (lane >> 4))) * 16);
        const unsigned vwk = (lane >> 2) * 3072 + (lane & 3) * 16, vwv = (lane >> 2) * 3072 + ((lane & 3) ^ 2) * 16;
        const rsrc_t rbX = biased_rsrc(x, 2048 * 512 * 2), rbQ = biased_rsrc(wqkv, 512 * 1536 * 2);
#define ISSUE_SS(ss, v) do { const unsigned sb_ = lb + ((ss) % 6) * SS_BYTES, ks_ = ((ss) + r1) & 7; \
            const unsigned sx_ = (128 * p + 32 * (v)) * 1024 + ks_ * 128, sw_ = (16 * (v) + 64 * ks_) * 3072 + (512 + 32 * j) * 2; \
            dma2(sb_ + 16384 + (v) * 2048, rbQ, vwk, vwv, sw_, sw_ + 1024); \
            dma4(sb_ + (v) * 4096, rbX, rbX, rbX, rbX, vx0, vx1, sx_, sx_ + 8 * 1024, sx_ + 16 * 1024, sx_ + 24 * 1024); } while (0)
        if (lw == 0) {
            const rsrc_t rsP = __builtin_amdgcn_make_buffer_rsrc((void*)wphi, 0, 32 * 256 * 2, 0x00020000);
            dma16(rsP, lb + L_WPHI, lane * 16, j * 1024);
        }
#pragma unroll
        for (int ss = 0; ss < K0; ++ss) ISSUE_SS(ss, lw);
        f32x4 csacc = {};
        const unsigned tw = 16384 + ((lw & 1) ? 1024 + ((lw >> 1) ^ 1) * 32 : (lw >> 1) * 32) + (lane >> 5) * 2048 + (8 * ((lane >> 4) & 1) + q) * 64 + pp * 8;
        constexpr int NL = 8 - K0;
#define P1WAIT(s) (6 * ((((s) + K0 - 1 < 7 ? (s) + K0 - 1 : 7) - (s))) + ((s) > 4 ? 2 * ((s) - 4) : 0))
#pragma unroll
        for (int ss = 0; ss < 8; ++ss) {
            if (ss == 0) waitv<P1WAIT(0)>();
            if (ss == 1) waitv<P1WAIT(1)>();
            if (ss == 2) waitv<P1WAIT(2)>();
            if (ss == 3) waitv<P1WAIT(3)>();
            if (ss == 4) waitv<P1WAIT(4)>();
            if (ss == 5) waitv<P1WAIT(5)>();
            if (ss == 6) waitv<P1WAIT(6)>();
            if (ss == 7) waitv<P1WAIT(7)>();
            BAR();
            if (ss == 0) { const u64 tv = *(LAS u64*)(lds + L_TAG); tag = UNIFORM64(tv); }
            unsigned bw = (ss % 6) * SS_BYTES + tw;
            asm volatile("" : "+v"(bw));
            const u32x4 f0 = tr2(lds, bw, 256), f1 = tr2(lds, bw + 4096, 256);
            SB();
            if (ss < NL) ISSUE_SS(K0 + ss, lw);
            if (ss == 6) {
#pragma unroll
                for (int i = 0; i < 4; ++i) *(LAS u32x4*)(lds + L_VIMG + (4 * lw + i) * 1024 + lane * 16) = ones;
            }
            if (ss >= 4) {
                const int pc = 8 * lw + 2 * (ss - 4);
                const unsigned so = 16 * ((pc + 4 * r1) & 31) * 3072 + 64 * j;
                dma2(lb + L_WQ + pc * 1024, rbQ, vwk, vwk, so, so + 16 * 3072);
            }
            csacc = mfma16(f0, ones, csacc);
            csacc = mfma16(f1, ones, csacc);
            SB();
        }
        if ((lane & 15) == 0) *(LAS f32x4*)(lds + L_CS1 + (lw >> 1) * 128 + (lw & 1) * 64 + (lane >> 4) * 16) = csacc;
        BAR();
        const f32x2 st = *(LAS f32x2*)(lds + L_STAT + (32 * m + t) * 8);
        mean1 = st[0]; rstd1 = st[1];
#pragma unroll
        for (int g = 0; g < 4; ++g) {
            const f32x4 cs = *(LAS f32x4*)(lds + L_CS1 + 128 + (8 * g + 4 * h2) * 4);
            const f32x4 v = *(LAS f32x4*)(lds + L_XCH + m * 4096 + g * 1024 + lane * 16);
#pragma unroll
            for (int e = 0; e < 4; ++e) { const float y = __builtin_fmaf(-mean1, cs[e], v[e]); yk[4 * g + e] = g < 2 ? y : y * rstd1; }
        }
    }
    const unsigned imgA = L_PHI + a * 8192 + (32 * m + t) * 64 + ((h2 ^ ((t >> 1) & 7)) * 8);
    const u32x4 aphi = tr2(lds, L_WPHI + a * 512 + (4 * h2 + q) * 32 + pp * 8, 256);
    {
        const u32x4 kf = {pack2(yk[0], yk[1]), pack2(yk[2], yk[3]), pack2(yk[4], yk[5]), pack2(yk[6], yk[7])};
        const f32x16 zero = {};
        const f32x16 pk = mfma(aphi, kf, zero);
        *(LAS u32x2*)(lds + imgA + (L_VIMG - L_PHI)) = (u32x2){pack2(yk[8], yk[9]), pack2(yk[10], yk[11])};
        *(LAS u32x2*)(lds + (imgA ^ 16) + (L_VIMG - L_PHI)) = (u32x2){pack2(yk[12], yk[13]), pack2(yk[14], yk[15])};
        u32x4 pos, neg;
        dual_softmax(pk, rstd1 * LOG2E, pos, neg);
        *(LAS u32x2*)(lds + imgA) = (u32x2){pos[0], pos[1]};
        *(LAS u32x2*)(lds + (imgA ^ 16)) = (u32x2){pos[2], pos[3]};
        *(LAS u32x2*)(lds + (imgA ^ 32)) = (u32x2){neg[0], neg[1]};
        *(LAS u32x2*)(lds + (imgA ^ 48)) = (u32x2){neg[2], neg[3]};
    }
    if (w >= 4) waitv<0>();
    BAR();
    float yq[8];
    if (w < 4) {
        unsigned tq = L_WQ + trn + blk * 32;
        asm volatile("" : "+v"(tq));
        f32x16 qa = {};
        u32x4 qb[2][4];
#pragma unroll
        for (int s = 0; s < 4; ++s) qb[0][s] = tr2(lds, tq + s * 1024, 256);
        SB();
#pragma unroll
        for (int s = 0; s < 32; ++s) {
            qa = mfma(qb[(s >> 2) & 1][s & 3], xf[s], qa);
            if (s < 28) qb[((s >> 2) + 1) & 1][s & 3] = tr2(lds, tq + (s + 4) * 1024, 256);
            SB();
        }
#pragma unroll
        for (int g = 0; g < 2; ++g) *(LAS f32x4*)(lds + L_XCH + m * 4096 + g * 1024 + lane * 16) = (f32x4){qa[8 + 4 * g], qa[9 + 4 * g], qa[10 + 4 * g], qa[11 + 4 * g]};
        BAR();
        {
            const rsrc_t rbX2 = biased_rsrc(x, 2048 * 512 * 2);
            const unsigned vt = (lane >> 3) * 1024 + (((lane & 7) ^ ((lane >> 3) & 7)) * 16), so = (64 * (2 * pP + half) + 16 * w) * 1024 + 128 * c;
            dma2(lb + L_HRES + w * 2048, rbX2, vt, vt, so, so + 8 * 1024);
        }
#pragma unroll
        for (int g = 0; g < 2; ++g) {
            const f32x4 c0 = *(LAS f32x4*)(lds + L_CSQ + (8 * g + 4 * h2) * 4);
#pragma unroll
            for (int e = 0; e < 4; ++e) yq[4 * g + e] = __builtin_fmaf(-mean1, c0[e], qa[4 * g + e]);
        }
    } else {
        if (lw >= 2) {
            __builtin_amdgcn_s_setprio(3);
            const int ah = lw - 2;
            f32x16 kv = {};
            unsigned aa[2], ab[2];
#pragma unroll
            for (int u = 0; u < 2; ++u) {
                const unsigned R = 8 * h2 + 4 * u + q, sw = (R >> 1) & 7;
                aa[u] = L_PHI + ah * 8192 + R * 64 + (((4 * blk + pp) ^ sw) * 8);
                ab[u] = aa[u] + (L_VIMG - L_PHI);
                asm volatile("" : "+v"(aa[u]), "+v"(ab[u]));
            }
            u32x4 A[8], B[8];
#pragma unroll
            for (int s = 0; s < 8; ++s) {
                const s16x4 a0 = __builtin_amdgcn_ds_read_tr16_b64_v4i16((LAS s16x4*)(lds + aa[0] + s * 1024));
                const s16x4 a1 = __builtin_amdgcn_ds_read_tr16_b64_v4i16((LAS s16x4*)(lds + aa[1] + s * 1024));
                const s16x4 b0 = __builtin_amdgcn_ds_read_tr16_b64_v4i16((LAS s16x4*)(lds + ab[0] + s * 1024));
                const s16x4 b1 = __builtin_amdgcn_ds_read_tr16_b64_v4i16((LAS s16x4*)(lds + ab[1] + s * 1024));
                A[s] = __builtin_bit_cast(u32x4, __builtin_shufflevector(a0, a1, 0, 1, 2, 3, 4, 5, 6, 7));
                B[s] = __builtin_bit_cast(u32x4, __builtin_shufflevector(b0, b1, 0, 1, 2, 3, 4, 5, 6, 7));
                if (s == 3) PIN();
            }
            PIN();
#pragma unroll
            for (int s = 0; s < 8; ++s) kv = mfma(A[s], B[s], kv);
            if (t <= 16) {
                const unsigned base = WS_KVP + (j * 16 + p) * KVP_BYTES + ((ah * 4 + h2) * 17 + t) * 16;
                st16(rsW, (u32x4){pack2(kv[0], kv[1]), pack2(kv[2], kv[3]), pack2(kv[4], kv[5]), pack2(kv[6], kv[7])}, base, 0);
                st16(rsW, (u32x4){pack2(kv[8], kv[9]), pack2(kv[10], kv[11]), pack2(kv[12], kv[13]), pack2(kv[14], kv[15])}, base + 34 * 16, 0);
            }
            WAITV(0);
            arrive_publish(lds, 0, 2, ws, WS_F1 + (j * 16 + p) * 256, tag, lane);
            __builtin_amdgcn_s_setprio(0);
        } else {
            unsigned tq = L_WQ + lw * 32 + (8 * (lane >> 4) + q) * 64 + pp * 8;
            asm volatile("" : "+v"(tq));
            f32x4 qcs = {};
#pragma unroll
            for (int g = 0; g < 4; ++g) {
                u32x4 f[4];
#pragma unroll
                for (int s = 0; s < 4; ++s) f[s] = tr2(lds, tq + (4 * g + s) * 2048, 256);
                PIN();
#pragma unroll
                for (int s = 0; s < 4; ++s) qcs = mfma16(f[s], ones, qcs);
            }
            if ((lane & 15) == 0) *(LAS f32x4*)(lds + L_CSQ + lw * 64 + (lane >> 4) * 16) = qcs;
        }
        BAR();
        {
            const rsrc_t rsO = biased_rsrc(wo, 512 * 512 * 2);
#pragma unroll
            for (int g = 0; g < 4; ++g) {
                const int i = 4 * g + lw;
                const unsigned so = 16 * ((i + r2) & 15) * 1024 + 128 * c;
                dma4(lb + L_X + i * 4096, rsO, rsO, rsO, rsO, vrow, vrow, so, so + 8 * 1024, so + 256 * 1024, so + 264 * 1024);
            }
        }
#pragma unroll
        for (int g = 0; g < 2; ++g) {
            const f32x4 c0 = *(LAS f32x4*)(lds + L_CSQ + (16 + 8 * g + 4 * h2) * 4);
            const f32x4 v = *(LAS f32x4*)(lds + L_XCH + m * 4096 + g * 1024 + lane * 16);
#pragma unroll
            for (int e = 0; e < 4; ++e) yq[4 * g + e] = __builtin_fmaf(-mean1, c0[e], v[e]);
        }
    }
    u32x4 qpos, qneg;
    {
        const u32x4 qf = {pack2(yq[0], yq[1]), pack2(yq[2], yq[3]), pack2(yq[4], yq[5]), pack2(yq[6], yq[7])};
        const f32x16 zero = {};
        const f32x16 pq = mfma(aphi, qf, zero);
        dual_softmax(pq, rstd1 * LOG2E, qpos, qneg);
    }
    unsigned kso[16];
#pragma unroll
    for (int i = 0; i < 16; ++i) { kso[i] = WS_KVP + (j * 16 + ((i + 4 * q4) & 15)) * KVP_BYTES; asm volatile("" : "+s"(kso[i])); }
    if (w == 0) poll_flags<0>(ws, WS_F1 + j * 16 * 256, 16, tag, lane);
    BAR();
    {
        const unsigned kb = WS_KVP + j * 16 * KVP_BYTES;
        float lo = 0.f, hi = 0.f;
        unsigned v[16], z[8];
#pragma unroll
        for (int i = 0; i < 16; ++i) v[i] = ld4(rsW, tid * 4, kso[i]);
        if (w == 0) {
#pragma unroll
            for (int i = 0; i < 8; ++i) z[i] = ld4(rsW, (512 + t) * 4 + h2 * 8 * KVP_BYTES, kb + i * KVP_BYTES);
        }
        PIN();
#pragma unroll
        for (int i = 0; i < 16; ++i) { lo += blo(v[i]); hi += bhi(v[i]); }
        if (w == 0) {
            float zl = 0.f, zh = 0.f;
#pragma unroll
            for (int i = 0; i < 8; ++i) { zl += blo(z[i]); zh += bhi(z[i]); }
            zl = halves_add(zl); zh = halves_add(zh);
            if (h2 == 0) *(LAS unsigned*)(lds + L_KVA + (512 + t) * 4) = pack2(zl, zh);
        }
        *(LAS unsigned*)(lds + L_KVA + tid * 4) = pack2(lo, hi);
    }
    BAR();
    const int npass = has3 ? 3 : 2;
    const int m2 = w & 1, kh = (w >> 1) & 1;
    const int T2 = 4 * pP + 2 * half + m2;
    const unsigned C0 = 64 * c + 32 * kh;
    {
        const unsigned col = t < 16 ? t : 16;
        const u32x4 kp = *(LAS u32x4*)(lds + L_KVA + ((a * 4 + h2) * 17 + col) * 16);
        const u32x4 kn = *(LAS u32x4*)(lds + L_KVA + ((a * 4 + 2 + h2) * 17 + col) * 16);
        f32x16 o = {};
        o = mfma(kp, qpos, o);
        o = mfma(kn, qneg, o);
        const float r = __builtin_amdgcn_rcpf(o[8] + 1e-6f);
        st16(rsW, (u32x4){pack2(o[0] * r, o[1] * r), pack2(o[2] * r, o[3] * r), pack2(o[4] * r, o[5] * r), pack2(o[6] * r, o[7] * r)},
             WS_O + ((4 * p + m) * 32 + 2 * j + a) * 1024 + lane * 16, 0);
        WAITV(0);
        BAR();
        if (tid == 0) __hip_atomic_store((GAS u64*)(ws + WS_F2 + (p * 16 + j) * 256), tag, __ATOMIC_RELAXED, __HIP_MEMORY_SCOPE_AGENT);
        SBAR();
    }
    const unsigned trp = (4 * h2 + q) * 64 + blk * 32 + pp * 8;
    const unsigned t128 = (4 * h2 + q) * 128 + (q >> 1) * 64 + blk * 32 + pp * 8;

    if (w >= 4) {
        ISSUE_STAGE(0, L_Y, 0, 4);
        const rsrc_t rsD = biased_rsrc(wd, 1376 * 512 * 2);
#define ISSUE_WD(pi, halfbase) do { _Pragma("unroll") for (int g_ = 0; g_ < ((pi) == 2 ? 3 : 4); ++g_) { const int i_ = 4 * g_ + lw; if ((pi) < 2 || i_ < 11) { \
                const int ii_ = (pi) == 2 ? (i_ + r4 >= 11 ? i_ + r4 - 11 : i_ + r4) : ((i_ + r2) & 15); \
                const unsigned sa_ = 16 * (((pi) == 2 ? 64 : 32 * (pi)) + ii_) * 1024 + 128 * c, sb_ = sa_ + 16 * ((pi) == 2 ? 11 : 16) * 1024; \
                dma4(lb + (halfbase) + i_ * 4096, rsD, rsD, rsD, rsD, vrow, vrow, sa_, sa_ + 8 * 1024, sb_, sb_ + 8 * 1024); } } } while (0)
        f32x4 cs0 = {}, cs1 = {};
        const unsigned gam = lane >> 4;
        const unsigned ca0 = (lw >> 1) * 2048 + (gam >> 1) * 4096 + (8 * (gam & 1) + q) * 128 + (((lw & 1) * 64) ^ (64 * (q >> 1))) + pp * 8;
#define COLSUM_STAGE(halfbase) do { unsigned cb_ = (halfbase) + ca0; asm volatile("" : "+v"(cb_)); \
            _Pragma("unroll") for (int g_ = 0; g_ < 4; ++g_) { const u32x4 f0_ = tr2(lds, cb_ + g_ * 16384, 512), f1_ = tr2(lds, cb_ + g_ * 16384 + 32, 512), \
                                                                         f2_ = tr2(lds, cb_ + g_ * 16384 + 8192, 512), f3_ = tr2(lds, cb_ + g_ * 16384 + 8192 + 32, 512); \
                PIN(); cs0 = mfma16(f0_, ones, cs0); cs1 = mfma16(f1_, ones, cs1); cs0 = mfma16(f2_, ones, cs0); cs1 = mfma16(f3_, ones, cs1); } } while (0)
        BAR();
        BAR();
        SBAR();
        ISSUE_STAGE(1, L_X, 0, B_EARLY);
        BAR();
        SBAR();
        ISSUE_STAGE(1, L_X, B_EARLY, 4);
        waitv<16>();
        BAR();
        COLSUM_STAGE(L_Y);
        WAITV(0);
        lsig(lb, 8); lwait(lds, 8, 4);
        COLSUM_STAGE(L_X);
        if ((lane & 15) == 0) {
            *(LAS f32x4*)(lds + L_CS3 + ((lw & 1) * 64 + (lw >> 1) * 32 + 4 * gam) * 4) = cs0;
            *(LAS f32x4*)(lds + L_CS3 + ((lw & 1) * 64 + (lw >> 1) * 32 + 16 + 4 * gam) * 4) = cs1;
        }
        BAR();
        if (npass == 3) {
            ISSUE_UNIT(u3, L_Y);
            WAITV(0);
            SBAR();
            ISSUE_WD(0, L_X);
            {
                unsigned cb = L_Y + (lw >> 1) * 1024 + (gam >> 1) * 2048 + (8 * (gam & 1) + q) * 64 + (lw & 1) * 32 + pp * 8;
                asm volatile("" : "+v"(cb));
                f32x4 c3 = {};
#pragma unroll
                for (int g = 0; g < 4; ++g) {
                    u32x4 f[4];
#pragma unroll
                    for (int e = 0; e < 4; ++e) f[e] = tr2(lds, cb + (4 * g + e) * 4096, 256);
                    PIN();
#pragma unroll
                    for (int e = 0; e < 4; ++e) c3 = mfma16(f[e], ones, c3);
                }
                if ((lane & 15) == 0) *(LAS f32x4*)(lds + L_CS3 + (128 + (lw >> 1) * 32 + (lw & 1) * 16 + 4 * gam) * 4) = c3;
            }
            BAR();
            WAITV(0);
            BAR();
            if (lw == 0) {
                lwait(lds, 11, 4);
                if (lane == 0) __hip_atomic_store((GAS u64*)(ws + WS_F4 + 0x10000 + (p * 16 + j) * 256), tag, __ATOMIC_RELAXED, __HIP_MEMORY_SCOPE_AGENT);
            }
            SBAR();
            ISSUE_WD(1, L_Y);
            WAITV(0);
            BAR();
            if (lw == 0) { poll_flags3(ws, WS_F4 + 0x10000 + pP * 16 * 256, tag, lane); lsig(lb, 14); }
            ISSUE_WD(2, L_X);
            WAITV(0);
            BAR();
        } else {
            ISSUE_WD(0, L_Y);
            SBAR();
            ISSUE_WD(1, L_X);
            WAITV(0);
            BAR();
            SBAR();
            BAR();
            if (lw == 0) { poll_flags3(ws, WS_F4 + 0x10000 + pP * 16 * 256, tag, lane); lsig(lb, 14); }
            ISSUE_WD(2, L_Y);
            WAITV(0);
            BAR();
        }
        BAR();
    } else {
        f32x16 keep;
#pragma unroll
        for (int g = 0; g < 4; ++g) {
            const u32x2 v = *(LAS u32x2*)(lds + L_HRES + (32 * m2 + t) * 128 + (((4 * kh + g) ^ (t & 7)) * 16) + 8 * h2);
            keep[4 * g] = blo(v[0]); keep[4 * g + 1] = bhi(v[0]); keep[4 * g + 2] = blo(v[1]); keep[4 * g + 3] = bhi(v[1]);
        }
        asm volatile("" : "+v"(keep));
        if (w == 0) poll_flags<PD>(ws, WS_F2 + pP * 16 * 256, 16, tag, lane);
        BAR();
        if (b == 0 && w == 1 && lane == 0) __hip_atomic_store((GAS u64*)ws, tag, __ATOMIC_RELAXED, __HIP_MEMORY_SCOPE_AGENT);
        {
            u32x4 hres0, hres1;
            u32x4 of[16];
#pragma unroll
            for (int i = 0; i < 8; ++i) of[i] = ld16(rsW, lane * 16, WS_O + (T2 * 32 + 16 * kh + ((i + r2) & 15)) * 1024);
            PIN();
            f32x16 snd = {};
            gemm2<16, 4096, 1024, 0>(lds, L_X + kh * 2048 + (t128 ^ (64 * kh)), L_X + kh * 2048 + (t128 ^ (64 * (kh ^ 1))), of, keep, snd, [&](int g) {
                if (g < 4) {
#pragma unroll
                    for (int e = 0; e < 2; ++e) of[8 + 2 * g + e] = ld16(rsW, lane * 16, WS_O + (T2 * 32 + 16 * kh + ((8 + 2 * g + e + r2) & 15)) * 1024);
                    SB();
                } });
#pragma unroll
            for (int g = 0; g < 4; ++g) *(LAS f32x4*)(lds + L_XCH + w * 4096 + g * 1024 + lane * 16) = (f32x4){snd[4 * g], snd[4 * g + 1], snd[4 * g + 2], snd[4 * g + 3]};
            BAR();
#pragma unroll
            for (int g = 0; g < 4; ++g) {
                const f32x4 v = *(LAS f32x4*)(lds + L_XCH + (w ^ 2) * 4096 + g * 1024 + lane * 16);
#pragma unroll
                for (int e = 0; e < 4; ++e) keep[4 * g + e] += v[e];
            }
#pragma unroll
            for (int e = 0; e < 4; ++e) { hres0[e] = pack2(keep[2 * e], keep[2 * e + 1]); hres1[e] = pack2(keep[8 + 2 * e], keep[8 + 2 * e + 1]); }
            const unsigned hb = WS_H + (T2 * 32 + 4 * c + 2 * kh) * 1024;
            st16(rsW, hres0, lane * 16, hb);
            st16(rsW, hres1, lane * 16, hb + 1024);
            *(LAS u32x4*)(lds + L_HRES + w * 2048 + lane * 16) = hres0;
            *(LAS u32x4*)(lds + L_HRES + w * 2048 + 1024 + lane * 16) = hres1;
            f32x2 s1 = {0.f, 0.f}, s2 = {0.f, 0.f};
#pragma unroll
            for (int e = 0; e < 8; ++e) { const f32x2 v = {keep[2 * e], keep[2 * e + 1]}; s1 += v; s2 += v * v; }
            float su = s1[0] + s1[1], sq = s2[0] + s2[1];
            su = halves_add(su); sq = halves_add(sq);
            __builtin_amdgcn_raw_buffer_store_b32(asu(h2 ? sq : su), rsW, lane * 4 + WS_HST + (T2 * 16 + 2 * c + kh) * 256, 0, 16);
            SBAR();
            WAITV(0);
            BAR();
            if (tid == 0) __hip_atomic_store((GAS u64*)(ws + WS_F3 + (pP * 16 + jP) * 256), tag, __ATOMIC_RELAXED, __HIP_MEMORY_SCOPE_AGENT);
            SBAR();
        }
        if (w == 0) poll_flags<PD>(ws, WS_F3 + p * 16 * 256, 16, tag, lane);
        BAR();
        {
            const int T = 4 * p + w;
            u32x4 hf[32];
            float sv[16];
#pragma unroll
            for (int s = 0; s < 8; ++s) hf[s] = ld16(rsW, lane * 16, WS_H + (T * 32 + ((s + r3) & 31)) * 1024);
            PIN();
            float mean, r2;
            auto act_out = [&](const f32x16& ag, const f32x16& au, int cs, unsigned ks) {
                float y[16];
#pragma unroll
                for (int g = 0; g < 4; ++g) {
                    const f32x4 cg = *(LAS f32x4*)(lds + L_CS3 + (cs * 64 + 8 * g + 4 * h2) * 4), cu = *(LAS f32x4*)(lds + L_CS3 + (cs * 64 + 32 + 8 * g + 4 * h2) * 4);
#pragma unroll
                    for (int e = 0; e < 4; ++e) y[4 * g + e] = (__builtin_fmaf(-mean, cg[e], ag[4 * g + e]) * r2) * __builtin_fmaf(-mean, cu[e], au[4 * g + e]);
                }
                const unsigned ab = WS_ACT + (T * 86 + ks) * 1024;
                st16(rsW, (u32x4){pack2(y[0], y[1]), pack2(y[2], y[3]), pack2(y[4], y[5]), pack2(y[6], y[7])}, lane * 16, ab);
                st16(rsW, (u32x4){pack2(y[8], y[9]), pack2(y[10], y[11]), pack2(y[12], y[13]), pack2(y[14], y[15])}, lane * 16, ab + 1024);
            };
            {
                f32x16 g0 = {}, g1 = {}, u0 = {}, u1 = {};
                gemm4<0>(lds, L_Y + t128, L_Y + (t128 ^ 64), hf, g0, g1, u0, u1, [&](int i) {
                    if (i < 8) hf[8 + i] = ld16(rsW, lane * 16, WS_H + (T * 32 + ((8 + i + r3) & 31)) * 1024);
                    else { hf[2 * i] = ld16(rsW, lane * 16, WS_H + (T * 32 + ((2 * i + r3) & 31)) * 1024); hf[2 * i + 1] = ld16(rsW, lane * 16, WS_H + (T * 32 + ((2 * i + 1 + r3) & 31)) * 1024); }
                    if (i < 4) {
#pragma unroll
                        for (int k = 0; k < 4; ++k) sv[4 * i + k] = asf(ld4(rsW, lane * 4, WS_HST + (T * 16 + 4 * i + k) * 256));
                    }
                    if (i == 9) {
                        float tot = 0.f;
#pragma unroll
                        for (int k = 0; k < 16; ++k) tot += sv[k];
                        const u32x2 r = both_halves(tot);
                        mean = asf(r[0]) * (1.0f / 512.0f);
                        const float var = fmaxf(asf(r[1]) * (1.0f / 512.0f) - mean * mean, 0.f);
                        r2 = __builtin_amdgcn_rcpf(var + 1e-5f);
                        asm volatile("" : "+v"(mean), "+v"(r2));
                    }
                    SB(); });
                BAR();
                gemm4<16>(lds, L_X + t128, L_X + (t128 ^ 64), hf, g0, g1, u0, u1, nohook);
                act_out(g0, u0, 0, 4 * j);
                act_out(g1, u1, 1, 4 * j + 2);
                SBAR();
            }
            if (npass == 3) {
                f32x16 ag = {}, au = {};
                gemm2<32, 2048, 512, 0>(lds, L_Y + trp, L_Y + 1024 + trp, hf, ag, au, [&](int g) {
                    if (g == 3) { WAITV(0); arrive_publish(lds, 3, 4, ws, WS_F4 + (p * 16 + j) * 256, tag, lane); PIN(); } });
                BAR();
                act_out(ag, au, 2, 2 * u3);
            } else {
                WAITV(0); arrive_publish(lds, 3, 4, ws, WS_F4 + (p * 16 + j) * 256, tag, lane);
            }
        }
        {
            f32x16 a0, a1 = {};
            {
                const u32x4 hres0 = *(LAS u32x4*)(lds + L_HRES + w * 2048 + lane * 16), hres1 = *(LAS u32x4*)(lds + L_HRES + w * 2048 + 1024 + lane * 16);
#pragma unroll
                for (int e = 0; e < 4; ++e) { a0[2 * e] = blo(hres0[e]); a0[2 * e + 1] = bhi(hres0[e]); a0[8 + 2 * e] = blo(hres1[e]); a0[8 + 2 * e + 1] = bhi(hres1[e]); }
                asm volatile("" : "+v"(a0));
            }
            const unsigned tk = kh * 2048 + (t128 ^ (64 * kh)), ts = kh * 2048 + (t128 ^ (64 * (kh ^ 1)));
            const unsigned h0 = npass == 3 ? L_X : L_Y, h1 = npass == 3 ? L_Y : L_X;
            if (w == 0) poll_flags(ws, WS_F4 + pP * 16 * 256, 16, tag, lane);
            BAR();
            u32x4 af[43];
            auto ld_act = [&](int i) {
                const int ks = i < 32 ? 32 * (i >> 4) + kh * 16 + ((i + r2) & 15) : 64 + kh * 11 + (i - 32 + r4 >= 11 ? i - 32 + r4 - 11 : i - 32 + r4);
                af[i] = ld16(rsW, lane * 16, WS_ACT + (T2 * 86 + ks) * 1024); };
            if (npass == 3) { WAITV(0); lsig(lb, 11); }
#pragma unroll
            for (int i = 0; i < 8; ++i) ld_act(i);
            SBAR();
            gemm2<16, 4096, 1024, 0>(lds, h0 + tk, h0 + ts, af, a0, a1, [&](int g) { ld_act(8 + 2 * g); ld_act(9 + 2 * g); SB(); });
            BAR();
            gemm2<16, 4096, 1024, 16>(lds, h1 + tk, h1 + ts, af, a0, a1, [&](int g) {
                if (g == 4) lwait(lds, 14, 1);
                ld_act(24 + 2 * g); ld_act(25 + 2 * g); SB(); });
            BAR();
            gemm2<11, 4096, 1024, 32>(lds, h0 + tk, h0 + ts, af, a0, a1, [&](int g) { if (g < 2) { ld_act(40 + 2 * g < 43 ? 40 + 2 * g : 42); if (g == 0) ld_act(41); SB(); } });
#pragma unroll
            for (int g = 0; g < 4; ++g) *(LAS f32x4*)(lds + L_XCH + w * 4096 + g * 1024 + lane * 16) = (f32x4){a1[4 * g], a1[4 * g + 1], a1[4 * g + 2], a1[4 * g + 3]};
            BAR();
#pragma unroll
            for (int g = 0; g < 4; ++g) {
                const f32x4 v = *(LAS f32x4*)(lds + L_XCH + (w ^ 2) * 4096 + g * 1024 + lane * 16);
#pragma unroll
                for (int e = 0; e < 4; ++e) a0[4 * g + e] += v[e];
            }
            u32x4 o0, o1;
#pragma unroll
            for (int e = 0; e < 4; ++e) { o0[e] = pack2(a0[2 * e], a0[2 * e + 1]); o1[e] = pack2(a0[8 + 2 * e], a0[8 + 2 * e + 1]); }
            { auto r = __builtin_amdgcn_permlane32_swap(o0[0], o0[2], false, false); o0[0] = r[0]; o0[2] = r[1];
              r = __builtin_amdgcn_permlane32_swap(o0[1], o0[3], false, false); o0[1] = r[0]; o0[3] = r[1];
              r = __builtin_amdgcn_permlane32_swap(o1[0], o1[2], false, false); o1[0] = r[0]; o1[2] = r[1];
              r = __builtin_amdgcn_permlane32_swap(o1[1], o1[3], false, false); o1[1] = r[0]; o1[3] = r[1]; }
            const rsrc_t rsOut = __builtin_amdgcn_make_buffer_rsrc((void*)out, 0, 2048 * 512 * 2, 0x00020000);
            int tz = threadIdx.x; asm volatile("" : "+v"(tz));
            const unsigned vz = (32 * T2 + (tz & 31)) * 1024 + (C0 + 8 * ((tz >> 5) & 1)) * 2;
            __builtin_amdgcn_raw_buffer_store_b128(o0, rsOut, vz, 0, 0);
            __builtin_amdgcn_raw_buffer_store_b128(o1, rsOut, vz + 32, 0, 0);
        }
    }
}

extern "C" void kernel_launch(void* const* d_in, const int* in_sizes, int n_in, void* d_out, int out_size, void* d_ws, size_t ws_size, hipStream_t stream) {
    (void)in_sizes; (void)n_in; (void)out_size; (void)ws_size;
    (void)hipFuncSetAttribute((const void*)hedgehog_block, hipFuncAttributeMaxDynamicSharedMemorySize, LDS_BYTES);
    hedgehog_block<<<256, 512, LDS_BYTES, stream>>>((const bf16_t*)d_in[0], (const bf16_t*)d_in[1], (const bf16_t*)d_in[2], (const bf16_t*)d_in[3],
                                                    (const bf16_t*)d_in[5], (const bf16_t*)d_in[6], (const bf16_t*)d_in[7], (bf16_t*)d_out, (char*)d_ws);
}
```
